```python
import torch

def _softmax(x, axis=-1):
    return torch.softmax(x, dim=axis)
def _gelu(x):
    return torch.nn.functional.gelu(x, approximate="tanh")
def _silu(x):
    return torch.nn.functional.silu(x)
def _sigmoid(x):
    return torch.sigmoid(x)
def _layernorm(x, axis=-1, eps=1e-5):
    m = x.mean(dim=axis, keepdim=True)
    v = x.var(dim=axis, keepdim=True, unbiased=False)
    return (x - m) / torch.sqrt(v + eps)
def _scatter_add(tgt, idx, src, axis=0):
    return tgt.clone().index_add_(axis, idx, src)
def _segment_sum(src, seg, n):
    out = torch.zeros((n,) + src.shape[1:], dtype=src.dtype)
    return out.index_add_(0, seg, src)
def _pad(x, pw):
    flat = tuple(v for pair in reversed(pw) for v in pair)
    return torch.nn.functional.pad(x, flat)

kernel_name = 'recurrent_scan__sliding_block_cached__dense_activation_gelu__decode'

def setup_inputs():

    _w = (lambda a: (lambda t: t.to(torch.bfloat16) if t.is_floating_point() else t)(torch.as_tensor(a)))
    def _A_setup():
        B, Sq, D = 8, 1, 512
        Hq, Hkv, Sk, Dh = 32, 8, 4096, 128
        assert Hq % Hkv == 0, (Hq, Hkv)
        return {
            "x": _w((lambda *s: torch.randn(*s, dtype=torch.bfloat16) * 0.05)(B, Sq, D)),
            "k_cache": _w((lambda *s: torch.randn(*s, dtype=torch.bfloat16) * 3.2)(B, Hkv, Sk, Dh)),
            "v_cache": _w((lambda *s: torch.randn(*s, dtype=torch.bfloat16) * 3.2)(B, Hkv, Sk, Dh)),
            "wq": _w((lambda *s: torch.randn(*s, dtype=torch.bfloat16) * 3.2)(D, Hq * Dh) * (D ** -0.5)),
            "wo": _w((lambda *s: torch.randn(*s, dtype=torch.bfloat16) * 3.2)(Hq * Dh, D) * ((Hq * Dh) ** -0.5)),
        }
    _A_in = _A_setup()

    def _B_setup():
        B, S, D, F = 8, 1, 512, 1792
        return {
            "x": _w((lambda *s: torch.randn(*s, dtype=torch.bfloat16) * 0.05)(B, S, D)),
            "w1": _w((lambda *s: torch.randn(*s, dtype=torch.bfloat16) * 3.2)(D, F) * (D ** -0.5)),
            "b1": _w((lambda *s: torch.randn(*s, dtype=torch.bfloat16) * 3.2)(F) * 0.01),
            "w2": _w((lambda *s: torch.randn(*s, dtype=torch.bfloat16) * 0.4)(F, D) * (F ** -0.5)),
            "b2": _w((lambda *s: torch.randn(*s, dtype=torch.bfloat16) * 0.4)(D) * 0.01),
        }
    _B_in = _B_setup()

    return {
        "A_x": _A_in["x"],
        "A_k_cache": _A_in["k_cache"],
        "A_v_cache": _A_in["v_cache"],
        "A_wq": _A_in["wq"],
        "A_wo": _A_in["wo"],
        "B_x": _B_in["x"],
        "B_w1": _B_in["w1"],
        "B_b1": _B_in["b1"],
        "B_w2": _B_in["w2"],
        "B_b2": _B_in["b2"],
    }

def reference(A_x, A_k_cache, A_v_cache, A_wq, A_wo, B_x, B_w1, B_b1, B_w2, B_b2):
    def _A_ref(x, k_cache, v_cache, wq, wo):
        B, Sq, D = x.shape
        Hq, Dh = 32, 128
        Hkv, Sk = k_cache.shape[1], k_cache.shape[2]
        W = min(4096, max(256, Sk // 4))
        rep = Hq // Hkv
        q = (lambda x, *d: x.permute(*d))((x @ wq).reshape(B, Sq, Hq, Dh), 0, 2, 1, 3)
        k = (lambda x, n, axis: torch.repeat_interleave(x, n, dim=axis))(k_cache[:, :, Sk - W:, :], rep, axis=1)
        v = (lambda x, n, axis: torch.repeat_interleave(x, n, dim=axis))(v_cache[:, :, Sk - W:, :], rep, axis=1)
        scores = torch.einsum("bhqd,bhkd->bhqk", q, k) * (Dh ** -0.5)
        p = _softmax(scores, axis=-1)
        o = torch.einsum("bhqk,bhkd->bhqd", p, v)
        return (lambda x, *d: x.permute(*d))(o, 0, 2, 1, 3).reshape(B, Sq, Hq * Dh) @ wo

    def _B_ref(x, w1, b1, w2, b2):
        h = torch.einsum("bsd,df->bsf", x, w1) + b1
        h = _gelu(h)
        return torch.einsum("bsf,fd->bsd", h, w2) + b2

    x = A_x
    xn = _layernorm(x, axis=-1)
    s = _A_ref(xn, A_k_cache, A_v_cache, A_wq, A_wo)
    g = _silu(_B_ref(xn, B_w1, B_b1, B_w2, B_b2))
    return x + s * g
```

```cpp
#include <hip/hip_runtime.h>
#include <hip/hip_bf16.h>
#include <stdint.h>

typedef unsigned short u16;
__device__ __forceinline__ float bf2f(u16 v) { return __uint_as_float(((unsigned)v) << 16); }
__device__ __forceinline__ float rbf(float f) { return __bfloat162float(__float2bfloat16(f)); }
__device__ __forceinline__ u16 f2bf(float f) { __hip_bfloat16 h = __float2bfloat16(f); return *reinterpret_cast<u16*>(&h); }

constexpr int B = 8, D = 512, HQ = 32, HKV = 8, SK = 4096, DH = 128, W = 1024, F = 1792;

__global__ void k_ln(const u16* x, float* xn) {
    int b = blockIdx.x, t = threadIdx.x;
    float v[8]; float s = 0;
    for (int i = 0; i < 8; ++i) { v[i] = bf2f(x[b * D + t * 8 + i]); s += v[i]; }
    for (int m = 32; m >= 1; m >>= 1) s += __shfl_xor(s, m);
    float mean = s / D;
    float q = 0;
    for (int i = 0; i < 8; ++i) { float d = v[i] - mean; q += d * d; }
    for (int m = 32; m >= 1; m >>= 1) q += __shfl_xor(q, m);
    float var = rbf(q / D);
    float mb = rbf(mean);
    float den = rbf(sqrtf(rbf(var + 1e-5f)));
    for (int i = 0; i < 8; ++i) xn[b * D + t * 8 + i] = rbf(rbf(v[i] - mb) / den);
}
__global__ void k_proj(const float* xn, const u16* wq, const u16* w1, const u16* b1, float* q, float* h) {
    int c = blockIdx.x * blockDim.x + threadIdx.x;
    if (c >= 4096 + F) return;
    float acc[8] = {0};
    const u16* w = c < 4096 ? wq + c : w1 + (c - 4096);
    int ld = c < 4096 ? 4096 : F;
    for (int k = 0; k < D; ++k) { float wv = bf2f(w[(size_t)k * ld]); for (int b = 0; b < 8; ++b) acc[b] += xn[b * D + k] * wv; }
    if (c < 4096) { for (int b = 0; b < 8; ++b) q[b * 4096 + c] = rbf(acc[b]); }
    else {
        int f = c - 4096;
        for (int b = 0; b < 8; ++b) {
            float a = rbf(rbf(acc[b]) + bf2f(b1[f]));
            float u = 0.7978845608028654f * (a + 0.044715f * a * a * a);
            h[b * F + f] = rbf(0.5f * a * (1.f + tanhf(u)));
        }
    }
}
__global__ void k_attn(const float* q, const u16* kc, const u16* vc, float* o) {
    __shared__ float sc[W]; __shared__ float red[256];
    int b = blockIdx.x / HQ, h = blockIdx.x % HQ, hk = h / 4, t = threadIdx.x;
    const u16* K = kc + ((size_t)(b * HKV + hk) * SK + (SK - W)) * DH;
    const u16* V = vc + ((size_t)(b * HKV + hk) * SK + (SK - W)) * DH;
    const float* qq = q + b * 4096 + h * DH;
    float mx = -INFINITY;
    for (int j = t; j < W; j += 256) {
        float a = 0; for (int d = 0; d < DH; ++d) a += qq[d] * bf2f(K[(size_t)j * DH + d]);
        a = rbf(rbf(a) * 0.08838834764831845f); sc[j] = a; mx = fmaxf(mx, a);
    }
    red[t] = mx; __syncthreads();
    for (int s = 128; s >= 1; s >>= 1) { if (t < s) red[t] = fmaxf(red[t], red[t + s]); __syncthreads(); }
    mx = red[0]; __syncthreads();
    float sm = 0;
    for (int j = t; j < W; j += 256) { float e = __expf(sc[j] - mx); sc[j] = e; sm += e; }
    red[t] = sm; __syncthreads();
    for (int s = 128; s >= 1; s >>= 1) { if (t < s) red[t] += red[t + s]; __syncthreads(); }
    sm = red[0]; __syncthreads();
    for (int j = t; j < W; j += 256) sc[j] = rbf(sc[j] / sm);
    __syncthreads();
    if (t < DH) { float a = 0; for (int j = 0; j < W; ++j) a += sc[j] * bf2f(V[(size_t)j * DH + t]); o[b * 4096 + h * DH + t] = rbf(a); }
}
__global__ void k_out(const float* o, const float* h, const u16* wo, const u16* w2, const u16* b2, const u16* x, u16* out) {
    int b = blockIdx.x / 8, c = (blockIdx.x % 8) * 64 + threadIdx.x;
    float s = 0; for (int k = 0; k < 4096; ++k) s += o[b * 4096 + k] * bf2f(wo[(size_t)k * D + c]);
    float y = 0; for (int k = 0; k < F; ++k) y += h[b * F + k] * bf2f(w2[(size_t)k * D + c]);
    s = rbf(s); y = rbf(rbf(y) + bf2f(b2[c]));
    float g = rbf(y / (1.f + __expf(-y)));
    out[b * D + c] = f2bf(bf2f(x[b * D + c]) + rbf(s * g));
}

extern "C" void kernel_launch(void* const* d_in, const int* in_sizes, int n_in,
                              void* d_out, int out_size, void* d_ws, size_t ws_size,
                              hipStream_t stream) {
    const u16* x = (const u16*)d_in[0]; const u16* kc = (const u16*)d_in[1]; const u16* vc = (const u16*)d_in[2];
    const u16* wq = (const u16*)d_in[3]; const u16* wo = (const u16*)d_in[4];
    const u16* w1 = (const u16*)d_in[6]; const u16* b1 = (const u16*)d_in[7]; const u16* w2 = (const u16*)d_in[8]; const u16* b2 = (const u16*)d_in[9];
    float* ws = (float*)d_ws;
    float* xn = ws; float* q = ws + 4096; float* h = q + 8 * 4096; float* o = h + 8 * F;
    k_ln<<<8, 64, 0, stream>>>(x, xn);
    k_proj<<<(4096 + F) / 64, 64, 0, stream>>>(xn, wq, w1, b1, q, h);
    k_attn<<<256, 256, 0, stream>>>(q, kc, vc, o);
    k_out<<<64, 64, 0, stream>>>(o, h, wo, w2, b2, x, (u16*)d_out);
}
```

```cpp
#include <hip/hip_runtime.h>
#include <hip/hip_bf16.h>
#include <stdint.h>

typedef unsigned short u16;
typedef unsigned v4u __attribute__((ext_vector_type(4)));
typedef unsigned v2u __attribute__((ext_vector_type(2)));
typedef float f32x4 __attribute__((ext_vector_type(4)));
typedef float f32x2 __attribute__((ext_vector_type(2)));
typedef __bf16 bf16x8 __attribute__((ext_vector_type(8)));
typedef __bf16 bf16x2 __attribute__((ext_vector_type(2)));
typedef short v4i16 __attribute__((ext_vector_type(4)));
#define GAS __attribute__((address_space(1)))
#define LAS __attribute__((address_space(3)))

#define STAMPS 0

constexpr int NWG = 256, NTHR = 576;
constexpr unsigned WS_EPOCH = 0;
constexpr unsigned WS_Q = 4096;
constexpr unsigned WS_H = WS_Q + 8 * 2048 * 8;
constexpr unsigned WS_ML = WS_H + 8 * 896 * 8;
constexpr unsigned WS_O = WS_ML + 256 * 4 * 16;
constexpr unsigned WS_S = WS_O + 256 * 4 * 128 * 8;
constexpr unsigned WS_Y = WS_S + 256 * 64 * 16 * 8;
constexpr unsigned WS_END = WS_Y + 256 * 16 * 16 * 8;
constexpr unsigned WS_STAMP = 8u << 20;
constexpr int L_V = 0;
constexpr int L_WQ = 0;
constexpr int L_W1 = 16384;
constexpr int L_XN = 24576;
constexpr int XN_STR = 1040;
constexpr int L_RED = 33792;
constexpr int L_WO = 65536;
constexpr int L_W2 = 81920;
constexpr int L_P = 90112;
constexpr int L_Q = 98304;
constexpr int L_H = L_Q + 4 * 272;
constexpr int L_ML = L_H + 8 * 272;
constexpr int L_O = L_ML + 256;
constexpr int L_TAG = L_O + 8 * 144;
constexpr int L_BYTES = L_TAG + 16;
static_assert(L_RED + 8 * 64 * 32 <= 65536, "early buffers inside V image");
constexpr unsigned SPIN_CAP = 1u << 18;

struct Args {
    const u16* x; const u16* kc; const u16* vc; const u16* wq; const u16* wo;
    const u16* w1; const u16* b1; const u16* w2; const u16* b2; u16* out; unsigned char* ws;
};

__device__ __forceinline__ float bf_lo(unsigned u) { return __uint_as_float(u << 16); }
__device__ __forceinline__ float bf_hi(unsigned u) { return __uint_as_float(u & 0xffff0000u); }
__device__ __forceinline__ unsigned pk_bf(float lo, float hi) { bf16x2 v = {(__bf16)lo, (__bf16)hi}; return __builtin_bit_cast(unsigned, v); }
__device__ __forceinline__ float rbf(float f) { return (float)(__bf16)f; }
__device__ __forceinline__ f32x4 mfma16(v4u a, v4u b, f32x4 c) {
    return __builtin_amdgcn_mfma_f32_16x16x32_bf16(__builtin_bit_cast(bf16x8, a), __builtin_bit_cast(bf16x8, b), c, 0, 0, 0);
}
__device__ __forceinline__ v2u trread(LAS unsigned char* p) { return __builtin_bit_cast(v2u, __builtin_amdgcn_ds_read_tr16_b64_v4i16((LAS v4i16*)p)); }
__device__ __forceinline__ v4u trfrag(LAS unsigned char* p0, LAS unsigned char* p1) { v2u a = trread(p0), b = trread(p1); return (v4u){a.x, a.y, b.x, b.y}; }
template <int CTRL> __device__ __forceinline__ float dpp(float x) {
    return __builtin_bit_cast(float, __builtin_amdgcn_mov_dpp(__builtin_bit_cast(int, x), CTRL, 0xf, 0xf, true));
}
__device__ __forceinline__ float x16_sum(float x) { auto s = __builtin_amdgcn_permlane16_swap(__float_as_uint(x), __float_as_uint(x), false, false); return __uint_as_float(s[0]) + __uint_as_float(s[1]); }
__device__ __forceinline__ float x32_sum(float x) { auto s = __builtin_amdgcn_permlane32_swap(__float_as_uint(x), __float_as_uint(x), false, false); return __uint_as_float(s[0]) + __uint_as_float(s[1]); }
__device__ __forceinline__ float x16_max(float x) { auto s = __builtin_amdgcn_permlane16_swap(__float_as_uint(x), __float_as_uint(x), false, false); return fmaxf(__uint_as_float(s[0]), __uint_as_float(s[1])); }
__device__ __forceinline__ float x32_max(float x) { auto s = __builtin_amdgcn_permlane32_swap(__float_as_uint(x), __float_as_uint(x), false, false); return fmaxf(__uint_as_float(s[0]), __uint_as_float(s[1])); }
__device__ __forceinline__ float wave_sum(float x) {
    x += dpp<0xB1>(x); x += dpp<0x4E>(x); x += dpp<0x141>(x); x += dpp<0x140>(x);
    return x32_sum(x16_sum(x));
}
__device__ __forceinline__ v4u ldg(const u16* p) { return *(const GAS v4u*)p; }
__device__ __forceinline__ v4u ldg_nt(const u16* p) { return __builtin_nontemporal_load((const GAS v4u*)p); }
__device__ __forceinline__ v4u ld_sc1(__amdgpu_buffer_rsrc_t rs, unsigned off) { return __builtin_amdgcn_raw_buffer_load_b128(rs, off, 0, 16); }
__device__ __forceinline__ void st_sc1(__amdgpu_buffer_rsrc_t rs, unsigned off, v4u v) { __builtin_amdgcn_raw_buffer_store_b128(v, rs, off, 0, 16); }
#define CFENCE() asm volatile("" ::: "memory")
#define LDS16(off) (*(LAS v4u*)(L + (off)))
#define LDS8(off) (*(LAS v2u*)(L + (off)))
#define LDS4(off) (*(LAS unsigned*)(L + (off)))
#define STAMP(i) do { } while (0)

__global__ void __launch_bounds__(NTHR, 1) fused_decode(Args a) {
    extern __shared__ __attribute__((aligned(16))) unsigned char smem[];
    LAS unsigned char* L = (LAS unsigned char*)smem;
    const int tid = threadIdx.x, lane = tid & 63, wave = __builtin_amdgcn_readfirstlane(tid >> 6);
    const int j = blockIdx.x, xcd = j & 7, r = j >> 3;
    const int n16 = lane & 15, g = lane >> 4;
    const __amdgpu_buffer_rsrc_t rs = __builtin_amdgcn_make_buffer_rsrc(a.ws, 0, WS_END, 0x00020000);
    const int hkv = xcd, b_att = r >> 2, s_att = r & 3;
    const int cg = xcd * 32 + r;
    const int fg = xcd * 28 + (r < 28 ? r : 27);
    const int rgf = xcd * 2 + (r >> 4), cf = r & 15;
    const int rg = xcd * 8 + (r >> 2), co = r & 3;
    const int b_fin = j >> 5, cg_fin = j & 31;
    constexpr float LOG2E = 1.4426950408889634f;
    STAMP(0);

    if (wave < 8) {
        const int w = wave;
        const v4u xv = ldg(a.x + w * 512 + lane * 8);
        v4u wqv[2];
#pragma unroll
        for (int i = 0; i < 2; ++i) wqv[i] = ldg(a.wq + (size_t)(64 * w + 32 * i + (lane >> 1)) * 4096 + 16 * cg + 8 * (lane & 1));
        const v4u w1v = ldg(a.w1 + (size_t)(64 * w + lane) * 1792 + 8 * fg);
        const v4u w2v = ldg(a.w2 + (size_t)(112 * rgf + 16 * (w < 7 ? w : 6) + (lane >> 2)) * 512 + 32 * cf + 8 * (lane & 3));
        const size_t kvbase = ((size_t)(b_att * 8 + hkv) * 4096 + 3072 + 256 * s_att + 32 * w) * 128;
        v4u kf[8];
#pragma unroll
        for (int i = 0; i < 8; ++i) kf[i] = ldg_nt(a.kc + kvbase + (16 * (i >> 2) + n16) * 128 + 32 * (i & 3) + 8 * g);
        v4u vv[8];
#pragma unroll
        for (int i = 0; i < 8; ++i) vv[i] = ldg_nt(a.vc + kvbase + (4 * i + g) * 128 + 8 * n16);
        v4u wov[2];
#pragma unroll
        for (int i = 0; i < 2; ++i) wov[i] = ldg_nt(a.wo + (size_t)(64 * rg + 8 * w + 4 * i + g) * 512 + 128 * co + 8 * n16);
        CFENCE();
        __builtin_amdgcn_sched_barrier(0);
        {
            float v[8];
#pragma unroll
            for (int k = 0; k < 4; ++k) { v[2 * k] = bf_lo(xv[k]); v[2 * k + 1] = bf_hi(xv[k]); }
            float s = ((v[0] + v[1]) + (v[2] + v[3])) + ((v[4] + v[5]) + (v[6] + v[7]));
            const float mean = wave_sum(s) * (1.f / 512.f);
            float q = 0.f;
#pragma unroll
            for (int k = 0; k < 8; ++k) { const float d = v[k] - mean; q = fmaf(d, d, q); }
            const float var = rbf(wave_sum(q) * (1.f / 512.f));
            const float mb = rbf(mean);
            const float den = rbf(sqrtf(rbf(var + 1e-5f)));
            const float rc = 1.f / den;
            float o[8];
#pragma unroll
            for (int k = 0; k < 8; ++k) { const float t = rbf(v[k] - mb); const float q0 = t * rc; const float e = fmaf(-q0, den, t); o[k] = fmaf(e, rc, q0); }
            LDS16(L_XN + w * XN_STR + lane * 16) = (v4u){pk_bf(o[0], o[1]), pk_bf(o[2], o[3]), pk_bf(o[4], o[5]), pk_bf(o[6], o[7])};
        }
        STAMP(1);
        LDS16(L_WQ + w * 2048 + lane * 16) = wqv[0];
        LDS16(L_WQ + w * 2048 + 1024 + lane * 16) = wqv[1];
        LDS16(L_W1 + w * 1024 + lane * 16) = w1v;
        LDS16(L_W2 + w * 1024 + lane * 16) = (w < 7) ? w2v : (v4u){0u, 0u, 0u, 0u};
        STAMP(2);
        __syncthreads();
        {
            const int q4 = n16 >> 2, p4 = lane & 3;
            f32x4 dq = {0.f, 0.f, 0.f, 0.f}, dh = {0.f, 0.f, 0.f, 0.f};
#pragma unroll
            for (int ks = 0; ks < 2; ++ks) {
                const v4u xf = LDS16(L_XN + (n16 & 7) * XN_STR + (64 * w + 32 * ks + 8 * g) * 2);
                const int row = 32 * ks + 8 * g + q4;
                const v4u wf = trfrag(L + L_WQ + w * 2048 + row * 32 + 8 * p4, L + L_WQ + w * 2048 + (row + 4) * 32 + 8 * p4);
                const v4u uf = trfrag(L + L_W1 + w * 1024 + row * 16 + 8 * (p4 & 1), L + L_W1 + w * 1024 + (row + 4) * 16 + 8 * (p4 & 1));
                dq = mfma16(wf, xf, dq);
                dh = mfma16(uf, xf, dh);
            }
            *(LAS f32x4*)(L + L_RED + w * 2048 + lane * 16) = dq;
            *(LAS f32x4*)(L + L_RED + w * 2048 + 1024 + lane * 16) = dh;
        }
        __syncthreads();
        STAMP(3);
        __syncthreads();
        STAMP(4);
        const unsigned tagA = LDS4(L_TAG), tagB = LDS4(L_TAG + 4);
        {
            v4u qf[4];
#pragma unroll
            for (int t = 0; t < 4; ++t) qf[t] = LDS16(L_Q + (n16 & 3) * 272 + (32 * t + 8 * g) * 2);
            f32x4 s0 = {0.f, 0.f, 0.f, 0.f}, s1 = {0.f, 0.f, 0.f, 0.f};
#pragma unroll
            for (int t = 0; t < 4; ++t) { s0 = mfma16(kf[t], qf[t], s0); s1 = mfma16(kf[4 + t], qf[t], s1); }
            float sc[8];
#pragma unroll
            for (int k = 0; k < 4; ++k) { sc[k] = rbf(rbf(s0[k]) * 0.08838834764831845f); sc[4 + k] = rbf(rbf(s1[k]) * 0.08838834764831845f); }
            float m = fmaxf(fmaxf(fmaxf(sc[0], sc[1]), fmaxf(sc[2], sc[3])), fmaxf(fmaxf(sc[4], sc[5]), fmaxf(sc[6], sc[7])));
            m = x32_max(x16_max(m));
            float p[8], l = 0.f;
#pragma unroll
            for (int k = 0; k < 8; ++k) { p[k] = __builtin_amdgcn_exp2f((sc[k] - m) * LOG2E); l += p[k]; }
            l = x32_sum(x16_sum(l));
            LDS16(L_P + w * 1024 + lane * 16) = (v4u){pk_bf(p[0], p[1]), pk_bf(p[2], p[3]), pk_bf(p[4], p[5]), pk_bf(p[6], p[7])};
            if (lane < 4) *(LAS f32x2*)(L + L_ML + lane * 64 + w * 8) = (f32x2){m, l};
        }
        STAMP(5);
#pragma unroll
        for (int i = 0; i < 8; ++i) { const int row = 32 * w + 4 * i + g; LDS16(L_V + row * 256 + 16 * (n16 ^ ((row & 7) << 1))) = vv[i]; }
        STAMP(6);
        __syncthreads();
        STAMP(7);
        {
            float ms[8], ls[8];
#pragma unroll
            for (int k = 0; k < 4; ++k) { const f32x4 t = *(LAS f32x4*)(L + L_ML + (n16 & 3) * 64 + k * 16); ms[2 * k] = t[0]; ls[2 * k] = t[1]; ms[2 * k + 1] = t[2]; ls[2 * k + 1] = t[3]; }
            float M = ms[0];
#pragma unroll
            for (int k = 1; k < 8; ++k) M = fmaxf(M, ms[k]);
            const int q4 = n16 >> 2, p4 = lane & 3;
            f32x4 acc = {0.f, 0.f, 0.f, 0.f}; float Lsum = 0.f;
#pragma unroll
            for (int s = 0; s < 8; ++s) {
                const int row0 = 32 * s + 4 * g + q4, row1 = row0 + 16;
                const int ch = 2 * w + (p4 >> 1);
                const v4u vf = trfrag(L + L_V + row0 * 256 + 16 * (ch ^ ((row0 & 7) << 1)) + 8 * (p4 & 1), L + L_V + row1 * 256 + 16 * (ch ^ ((row1 & 7) << 1)) + 8 * (p4 & 1));
                const v4u pf = LDS16(L_P + s * 1024 + lane * 16);
                const f32x4 d = mfma16(vf, pf, (f32x4){0.f, 0.f, 0.f, 0.f});
                const float al = __builtin_amdgcn_exp2f((ms[s] - M) * LOG2E);
                Lsum = fmaf(al, ls[s], Lsum);
#pragma unroll
                for (int k = 0; k < 4; ++k) acc[k] = fmaf(al, d[k], acc[k]);
            }
            const int head = n16 & 3, cp = n16 >> 2;
            if (cp < 2) {
                const float v0 = cp ? acc[2] : acc[0], v1 = cp ? acc[3] : acc[1];
                st_sc1(rs, WS_O + (unsigned)(((j * 4 + head) * 128 + 16 * w + 4 * g + 2 * cp) * 8), (v4u){__float_as_uint(v0), tagA, __float_as_uint(v1), tagB});
            }
            if (w == 0 && lane < 4) st_sc1(rs, WS_ML + (unsigned)((j * 4 + lane) * 16), (v4u){__float_as_uint(M), tagA, __float_as_uint(Lsum), tagB});
        }
        STAMP(8);
#pragma unroll
        for (int i = 0; i < 2; ++i) { const int row = 8 * w + 4 * i + g; LDS16(L_WO + row * 256 + 16 * (n16 ^ ((row & 7) << 1))) = wov[i]; }
        STAMP(9);
        {
            const int head = (rg >> 1) & 3, half = rg & 1, hi = lane >> 5, l31 = lane & 31;
            const int pid0 = ((w * 4 + hi) << 3) | hkv, pid1 = ((w * 4 + hi + 2) << 3) | hkv;
            const unsigned o0 = WS_O + (unsigned)(((pid0 * 4 + head) * 128 + 64 * half + 2 * l31) * 8), o1 = WS_O + (unsigned)(((pid1 * 4 + head) * 128 + 64 * half + 2 * l31) * 8);
            const unsigned m0 = WS_ML + (unsigned)((pid0 * 4 + head) * 16), m1 = WS_ML + (unsigned)((pid1 * 4 + head) * 16);
            v4u A0, A1, M0, M1;
            for (unsigned spin = 0;; ++spin) {
                CFENCE();
                A0 = ld_sc1(rs, o0); A1 = ld_sc1(rs, o1); M0 = ld_sc1(rs, m0); M1 = ld_sc1(rs, m1);
                const bool ok = A0.y == tagA && A0.w == tagB && A1.y == tagA && A1.w == tagB && M0.y == tagA && M0.w == tagB && M1.y == tagA && M1.w == tagB;
                if (__all(ok) || spin > SPIN_CAP) break;
            }
            STAMP(10);
            const float ma = __uint_as_float(M0.x), mb = __uint_as_float(M1.x);
            const float M = x32_max(fmaxf(ma, mb));
            const float wa = __builtin_amdgcn_exp2f((ma - M) * LOG2E), wb = __builtin_amdgcn_exp2f((mb - M) * LOG2E);
            const float Ls = x32_sum(wa * __uint_as_float(M0.z) + wb * __uint_as_float(M1.z));
            const float e0 = x32_sum(wa * __uint_as_float(A0.x) + wb * __uint_as_float(A1.x));
            const float e1 = x32_sum(wa * __uint_as_float(A0.z) + wb * __uint_as_float(A1.z));
            const float inv = 1.f / Ls;
            if (hi == 0) LDS4(L_O + w * 144 + l31 * 4) = pk_bf(e0 * inv, e1 * inv);
        }
        __syncthreads();
        STAMP(11);
        {
            const int q4 = n16 >> 2, p4 = lane & 3;
            f32x4 d = {0.f, 0.f, 0.f, 0.f};
#pragma unroll
            for (int ks = 0; ks < 2; ++ks) {
                const int row0 = 32 * ks + 8 * g + q4, row1 = row0 + 4, ch = 2 * w + (p4 >> 1);
                const v4u wf = trfrag(L + L_WO + row0 * 256 + 16 * (ch ^ ((row0 & 7) << 1)) + 8 * (p4 & 1), L + L_WO + row1 * 256 + 16 * (ch ^ ((row1 & 7) << 1)) + 8 * (p4 & 1));
                const v4u of = LDS16(L_O + (n16 & 7) * 144 + (32 * ks + 8 * g) * 2);
                d = mfma16(wf, of, d);
            }
            const int b = n16 & 7, cp = n16 >> 3, f = b * 32 + 8 * co + w;
            const float v0 = cp ? d[2] : d[0], v1 = cp ? d[3] : d[1];
            st_sc1(rs, WS_S + (unsigned)(((f * 64 + rg) * 16 + 4 * g + 2 * cp) * 8), (v4u){__float_as_uint(v0), tagA, __float_as_uint(v1), tagB});
        }
        STAMP(12);
    } else {
        const unsigned long long ep = __hip_atomic_load((GAS unsigned long long*)(a.ws + WS_EPOCH), __ATOMIC_RELAXED, __HIP_MEMORY_SCOPE_AGENT);
        const v2u b1v = *(const GAS v2u*)(a.b1 + 8 * fg + 4 * (g & 1));
        const unsigned xfin = *(const GAS unsigned*)(a.x + b_fin * 512 + 16 * cg_fin + 2 * (lane & 7));
        const unsigned b2v = *(const GAS unsigned*)(a.b2 + 16 * cg_fin + 2 * (lane & 7));
        const unsigned tagA = (unsigned)ep + 1u, tagB = (unsigned)(ep >> 32) + 1u;
        if (lane == 0) { LDS4(L_TAG) = tagA; LDS4(L_TAG + 4) = tagB; }
        if (lane < 16) LDS16(L_H + (lane >> 1) * 272 + 224 + (lane & 1) * 16) = (v4u){0u, 0u, 0u, 0u};
        STAMP(1);
        __syncthreads();
        __syncthreads();
        STAMP(3);
        {
            f32x4 dq = {0.f, 0.f, 0.f, 0.f}, dh = {0.f, 0.f, 0.f, 0.f};
#pragma unroll
            for (int w = 0; w < 8; ++w) { dq += *(LAS f32x4*)(L + L_RED + w * 2048 + lane * 16); dh += *(LAS f32x4*)(L + L_RED + w * 2048 + 1024 + lane * 16); }
            if (n16 < 8) st_sc1(rs, WS_Q + (unsigned)((n16 * 2048 + 8 * cg + 2 * g) * 8), (v4u){pk_bf(dq[0], dq[1]), tagA, pk_bf(dq[2], dq[3]), tagB});
            float hv[4];
#pragma unroll
            for (int k = 0; k < 4; ++k) {
                const float bb = (k & 1) ? bf_hi(b1v[k >> 1]) : bf_lo(b1v[k >> 1]);
                const float t = rbf(rbf(dh[k]) + bb);
                const float u = 0.7978845608028654f * (t + 0.044715f * t * t * t);
                hv[k] = 0.5f * t * (1.f + tanhf(u));
            }
            if (n16 < 8 && g < 2 && r < 28) st_sc1(rs, WS_H + (unsigned)((n16 * 896 + 4 * fg + 2 * g) * 8), (v4u){pk_bf(hv[0], hv[1]), tagA, pk_bf(hv[2], hv[3]), tagB});
        }
        STAMP(4);
        {
            unsigned qo[2], ho[4]; int hdst[4];
#pragma unroll
            for (int k = 0; k < 2; ++k) qo[k] = WS_Q + (unsigned)((b_att * 2048 + hkv * 256 + 2 * (lane + 64 * k)) * 8);
#pragma unroll
            for (int k = 0; k < 4; ++k) { int p = lane + 64 * k; if (p >= 224) p -= 64; const int b = p / 28, i = p - 28 * b; ho[k] = WS_H + (unsigned)((b * 896 + 56 * rgf + 2 * i) * 8); hdst[k] = L_H + b * 272 + i * 8; }
            v4u Q[2], H[4];
            for (unsigned spin = 0;; ++spin) {
                CFENCE();
                bool ok = true;
#pragma unroll
                for (int k = 0; k < 2; ++k) Q[k] = ld_sc1(rs, qo[k]);
#pragma unroll
                for (int k = 0; k < 4; ++k) H[k] = ld_sc1(rs, ho[k]);
#pragma unroll
                for (int k = 0; k < 2; ++k) ok = ok && Q[k].y == tagA && Q[k].w == tagB;
#pragma unroll
                for (int k = 0; k < 4; ++k) ok = ok && H[k].y == tagA && H[k].w == tagB;
                if (__all(ok) || spin > SPIN_CAP) break;
            }
#pragma unroll
            for (int k = 0; k < 2; ++k) { const int p = lane + 64 * k; LDS8(L_Q + (p >> 5) * 272 + (p & 31) * 8) = (v2u){Q[k].x, Q[k].z}; }
#pragma unroll
            for (int k = 0; k < 4; ++k) LDS8(hdst[k]) = (v2u){H[k].x, H[k].z};
        }
        STAMP(5);
        __syncthreads();
        {
            const int q4 = n16 >> 2, p4 = lane & 3;
            f32x4 d0 = {0.f, 0.f, 0.f, 0.f}, d1 = {0.f, 0.f, 0.f, 0.f};
#pragma unroll
            for (int ks = 0; ks < 4; ++ks) {
                const int row = 32 * ks + 8 * g + q4;
                const v4u hf = LDS16(L_H + (n16 & 7) * 272 + (32 * ks + 8 * g) * 2);
                const v4u f0 = trfrag(L + L_W2 + row * 64 + 8 * p4, L + L_W2 + (row + 4) * 64 + 8 * p4);
                const v4u f1 = trfrag(L + L_W2 + row * 64 + 32 + 8 * p4, L + L_W2 + (row + 4) * 64 + 32 + 8 * p4);
                d0 = mfma16(f0, hf, d0); d1 = mfma16(f1, hf, d1);
            }
            const int b = n16 & 7, cp = n16 >> 3, f = b * 32 + 2 * cf;
            st_sc1(rs, WS_Y + (unsigned)(((f * 16 + rgf) * 16 + 4 * g + 2 * cp) * 8), (v4u){__float_as_uint(cp ? d0[2] : d0[0]), tagA, __float_as_uint(cp ? d0[3] : d0[1]), tagB});
            st_sc1(rs, WS_Y + (unsigned)((((f + 1) * 16 + rgf) * 16 + 4 * g + 2 * cp) * 8), (v4u){__float_as_uint(cp ? d1[2] : d1[0]), tagA, __float_as_uint(cp ? d1[3] : d1[1]), tagB});
        }
        STAMP(6);
        __syncthreads();
        __syncthreads();
        STAMP(11);
        {
            v4u S[8], Y[2];
            for (unsigned spin = 0;; ++spin) {
                CFENCE();
                bool ok = true;
#pragma unroll
                for (int k = 0; k < 8; ++k) S[k] = ld_sc1(rs, WS_S + (unsigned)(j * 8192 + (lane + 64 * k) * 16));
#pragma unroll
                for (int k = 0; k < 2; ++k) Y[k] = ld_sc1(rs, WS_Y + (unsigned)(j * 2048 + (lane + 64 * k) * 16));
#pragma unroll
                for (int k = 0; k < 8; ++k) ok = ok && S[k].y == tagA && S[k].w == tagB;
#pragma unroll
                for (int k = 0; k < 2; ++k) ok = ok && Y[k].y == tagA && Y[k].w == tagB;
                if (__all(ok) || spin > SPIN_CAP) break;
            }
            STAMP(12);
            float s0 = 0.f, s1 = 0.f;
#pragma unroll
            for (int k = 0; k < 8; ++k) { s0 += __uint_as_float(S[k].x); s1 += __uint_as_float(S[k].z); }
            float y0 = __uint_as_float(Y[0].x) + __uint_as_float(Y[1].x), y1 = __uint_as_float(Y[0].z) + __uint_as_float(Y[1].z);
            s0 += dpp<0x128>(s0); s1 += dpp<0x128>(s1); y0 += dpp<0x128>(y0); y1 += dpp<0x128>(y1);
            s0 = x32_sum(x16_sum(s0)); s1 = x32_sum(x16_sum(s1)); y0 = x32_sum(x16_sum(y0)); y1 = x32_sum(x16_sum(y1));
            const float ya = rbf(rbf(y0) + bf_lo(b2v)), yb = rbf(rbf(y1) + bf_hi(b2v));
            const float ga = rbf(ya / (1.f + __expf(-ya))), gb = rbf(yb / (1.f + __expf(-yb)));
            const float oa = bf_lo(xfin) + rbf(rbf(s0) * ga), ob = bf_hi(xfin) + rbf(rbf(s1) * gb);
            if (lane < 8) *(GAS unsigned*)(a.out + b_fin * 512 + 16 * cg_fin + 2 * lane) = pk_bf(oa, ob);
            if (j == 0 && lane == 0) __hip_atomic_store((GAS unsigned long long*)(a.ws + WS_EPOCH), ((unsigned long long)tagB << 32) | tagA, __ATOMIC_RELAXED, __HIP_MEMORY_SCOPE_AGENT);
        }
        STAMP(13);
    }
}

extern "C" void kernel_launch(void* const* d_in, const int* in_sizes, int n_in,
                              void* d_out, int out_size, void* d_ws, size_t ws_size,
                              hipStream_t stream) {
    Args a;
    a.x = (const u16*)d_in[0]; a.kc = (const u16*)d_in[1]; a.vc = (const u16*)d_in[2]; a.wq = (const u16*)d_in[3]; a.wo = (const u16*)d_in[4];
    a.w1 = (const u16*)d_in[6]; a.b1 = (const u16*)d_in[7]; a.w2 = (const u16*)d_in[8]; a.b2 = (const u16*)d_in[9];
    a.out = (u16*)d_out; a.ws = (unsigned char*)d_ws;
    hipFuncSetAttribute((const void*)fused_decode, hipFuncAttributeMaxDynamicSharedMemorySize, L_BYTES);
    hipLaunchKernelGGL(fused_decode, dim3(NWG), dim3(NTHR), L_BYTES, stream, a);
}
```

```cpp
#include <hip/hip_runtime.h>
#include <hip/hip_bf16.h>
#include <stdint.h>

typedef unsigned short u16;
typedef unsigned v4u __attribute__((ext_vector_type(4)));
typedef unsigned v2u __attribute__((ext_vector_type(2)));
typedef float f32x4 __attribute__((ext_vector_type(4)));
typedef float f32x2 __attribute__((ext_vector_type(2)));
typedef __bf16 bf16x8 __attribute__((ext_vector_type(8)));
typedef __bf16 bf16x2 __attribute__((ext_vector_type(2)));
typedef short v4i16 __attribute__((ext_vector_type(4)));
#define GAS __attribute__((address_space(1)))
#define LAS __attribute__((address_space(3)))

#define STAMPS 0
#define WIN 4
#define KV_NT 1

constexpr int NWG = 256, NWAVE = 10, NTHR = NWAVE * 64;
constexpr unsigned WS_EPOCH = 0;
constexpr unsigned WS_Q = 4096;
constexpr unsigned WS_H = WS_Q + 8 * 2048 * 8;
constexpr unsigned WS_SC = WS_H + 8 * 896 * 8;
constexpr unsigned WS_S = WS_SC + 64 * 32 * 512;
constexpr unsigned WS_Y = WS_S + 256 * 32 * 16 * 8;
constexpr unsigned WS_END = WS_Y + 256 * 16 * 16 * 8;
constexpr unsigned WS_STAMP = 8u << 20;
constexpr int L_RING = 0;
constexpr int L_WQ = 13 * 8192;
constexpr int L_W1 = L_WQ + 16384;
constexpr int L_XN = 131072;
constexpr int XN_STR = 1040;
constexpr int L_W2 = L_XN + 8 * XN_STR;
constexpr int L_Q = L_W2 + 8192;
constexpr int L_H = L_Q + 4 * 272;
constexpr int L_OP = L_H + 8 * 272;
constexpr int L_ML = L_OP + 4096;
constexpr int L_FLG = L_ML + 256;
constexpr int F_PROG = L_FLG, F_XN = L_FLG + 4, F_RED = L_FLG + 8, F_Q = L_FLG + 12, F_O = L_FLG + 16, F_EARLY = L_FLG + 20;
constexpr int F_TAG = L_FLG + 24;
constexpr int F_FREE = L_FLG + 32;
constexpr int L_BYTES = F_FREE + 64;
static_assert(L_BYTES <= 163840, "LDS");
constexpr unsigned SPIN_CAP = 1u << 18;

struct Args {
    const u16* x; const u16* kc; const u16* vc; const u16* wq; const u16* wo;
    const u16* w1; const u16* b1; const u16* w2; const u16* b2; u16* out; unsigned char* ws;
};

__device__ __forceinline__ float bf_lo(unsigned u) { return __uint_as_float(u << 16); }
__device__ __forceinline__ float bf_hi(unsigned u) { return __uint_as_float(u & 0xffff0000u); }
__device__ __forceinline__ unsigned pk_bf(float lo, float hi) { bf16x2 v = {(__bf16)lo, (__bf16)hi}; return __builtin_bit_cast(unsigned, v); }
__device__ __forceinline__ float rbf(float f) { return (float)(__bf16)f; }
__device__ __forceinline__ f32x4 mfma16(v4u a, v4u b, f32x4 c) {
    return __builtin_amdgcn_mfma_f32_16x16x32_bf16(__builtin_bit_cast(bf16x8, a), __builtin_bit_cast(bf16x8, b), c, 0, 0, 0);
}
__device__ __forceinline__ v2u trread(LAS unsigned char* p) { return __builtin_bit_cast(v2u, __builtin_amdgcn_ds_read_tr16_b64_v4i16((LAS v4i16*)p)); }
__device__ __forceinline__ v4u trfrag(LAS unsigned char* p0, LAS unsigned char* p1) { v2u a = trread(p0), b = trread(p1); return (v4u){a.x, a.y, b.x, b.y}; }
template <int CTRL> __device__ __forceinline__ unsigned dppu(unsigned x) { return (unsigned)__builtin_amdgcn_mov_dpp((int)x, CTRL, 0xf, 0xf, true); }
template <int CTRL> __device__ __forceinline__ float dpp(float x) { return __uint_as_float(dppu<CTRL>(__float_as_uint(x))); }
template <int CTRL> __device__ __forceinline__ v4u dpp4(v4u x) { return (v4u){dppu<CTRL>(x.x), dppu<CTRL>(x.y), dppu<CTRL>(x.z), dppu<CTRL>(x.w)}; }
__device__ __forceinline__ float x16_sum(float x) { auto s = __builtin_amdgcn_permlane16_swap(__float_as_uint(x), __float_as_uint(x), false, false); return __uint_as_float(s[0]) + __uint_as_float(s[1]); }
__device__ __forceinline__ float x32_sum(float x) { auto s = __builtin_amdgcn_permlane32_swap(__float_as_uint(x), __float_as_uint(x), false, false); return __uint_as_float(s[0]) + __uint_as_float(s[1]); }
__device__ __forceinline__ float x16_max(float x) { auto s = __builtin_amdgcn_permlane16_swap(__float_as_uint(x), __float_as_uint(x), false, false); return fmaxf(__uint_as_float(s[0]), __uint_as_float(s[1])); }
__device__ __forceinline__ float x32_max(float x) { auto s = __builtin_amdgcn_permlane32_swap(__float_as_uint(x), __float_as_uint(x), false, false); return fmaxf(__uint_as_float(s[0]), __uint_as_float(s[1])); }
__device__ __forceinline__ float wave_sum(float x) {
    x += dpp<0xB1>(x); x += dpp<0x4E>(x); x += dpp<0x141>(x); x += dpp<0x140>(x);
    return x32_sum(x16_sum(x));
}
__device__ __forceinline__ v4u ldg(const u16* p) { return *(const GAS v4u*)p; }
__device__ __forceinline__ v4u ld_sc1(__amdgpu_buffer_rsrc_t rs, unsigned off) { return __builtin_amdgcn_raw_buffer_load_b128(rs, off, 0, 16); }
__device__ __forceinline__ void st_sc1(__amdgpu_buffer_rsrc_t rs, unsigned off, v4u v) { __builtin_amdgcn_raw_buffer_store_b128(v, rs, off, 0, 16); }
#define CFENCE() asm volatile("" ::: "memory")
#define LDS16(off) (*(LAS v4u*)(L + (off)))
#define LDS8(off) (*(LAS v2u*)(L + (off)))
#define LDS4(off) (*(LAS unsigned*)(L + (off)))
#define VLDS4(off) (*(volatile LAS unsigned*)(L + (off)))
#define STAMP(i) do { } while (0)

template <int NT> __device__ __forceinline__ void glds(const void* base, unsigned voff, unsigned dst) {
    unsigned keep;
    if (NT) asm volatile("s_mov_b32 %0, m0\n\ts_mov_b32 m0, %3\n\ts_nop 0\n\tglobal_load_lds_dwordx4 %1, %2 nt\n\ts_mov_b32 m0, %0" : "=&s"(keep) : "v"(voff), "s"(base), "s"(dst) : "memory");
    else    asm volatile("s_mov_b32 %0, m0\n\ts_mov_b32 m0, %3\n\ts_nop 0\n\tglobal_load_lds_dwordx4 %1, %2\n\ts_mov_b32 m0, %0" : "=&s"(keep) : "v"(voff), "s"(base), "s"(dst) : "memory");
}
__device__ __forceinline__ void lds_wait_ge(LAS unsigned char* L, int off, unsigned need) {
#pragma clang loop unroll(disable)
    for (unsigned spin = 0; spin < SPIN_CAP; ++spin) {
        if ((unsigned)__builtin_amdgcn_readfirstlane(VLDS4(off)) >= need) break;
        __builtin_amdgcn_s_sleep(1);
    }
    CFENCE();
}
__device__ __forceinline__ void lds_arrive(LAS unsigned char* L, int off, int lane) {
    CFENCE();
    if (lane == 0) __hip_atomic_fetch_add((LAS unsigned*)(L + off), 1u, __ATOMIC_RELAXED, __HIP_MEMORY_SCOPE_WORKGROUP);
    CFENCE();
}

__global__ void __launch_bounds__(NTHR, 1) fused_decode(Args a) {
    extern __shared__ __attribute__((aligned(16))) unsigned char smem[];
    LAS unsigned char* L = (LAS unsigned char*)smem;
    const int tid = threadIdx.x, lane = tid & 63, wave = __builtin_amdgcn_readfirstlane(tid >> 6);
    const int j = blockIdx.x, xcd = j & 7, r = j >> 3;
    const int n16 = lane & 15, g = lane >> 4, q4 = n16 >> 2, p4 = lane & 3;
    const __amdgpu_buffer_rsrc_t rs = __builtin_amdgcn_make_buffer_rsrc(a.ws, 0, WS_END, 0x00020000);
    const int hkv = xcd, b_att = r >> 2, s_att = r & 3;
    const int cg = xcd * 32 + r;
    const int fg = xcd * 28 + (r < 28 ? r : 27);
    const int rgf = xcd * 2 + (r >> 4), cf = r & 15;
    const int b_fin = j >> 5, cg_fin = j & 31;
    constexpr float LOG2E = 1.4426950408889634f;
    STAMP(0);

    if (wave < 8) {
        const int w = wave;
        const v4u xv = ldg(a.x + w * 512 + lane * 8);
        v4u wqv[2];
#pragma unroll
        for (int i = 0; i < 2; ++i) wqv[i] = ldg(a.wq + (size_t)(64 * w + 32 * i + (lane >> 1)) * 4096 + 16 * cg + 8 * (lane & 1));
        const v4u w1v = ldg(a.w1 + (size_t)(64 * w + lane) * 1792 + 8 * fg);
        const v4u w2v = ldg(a.w2 + (size_t)(112 * rgf + 16 * (w < 7 ? w : 6) + (lane >> 2)) * 512 + 32 * cf + 8 * (lane & 3));
        CFENCE();
        __builtin_amdgcn_sched_barrier(0);
        __builtin_amdgcn_s_barrier();
        __builtin_amdgcn_sched_barrier(0);
        CFENCE();
        {
            float v[8];
#pragma unroll
            for (int k = 0; k < 4; ++k) { v[2 * k] = bf_lo(xv[k]); v[2 * k + 1] = bf_hi(xv[k]); }
            float s = ((v[0] + v[1]) + (v[2] + v[3])) + ((v[4] + v[5]) + (v[6] + v[7]));
            const float mean = wave_sum(s) * (1.f / 512.f);
            float q = 0.f;
#pragma unroll
            for (int k = 0; k < 8; ++k) { const float d = v[k] - mean; q = fmaf(d, d, q); }
            const float var = rbf(wave_sum(q) * (1.f / 512.f));
            const float mb = rbf(mean);
            const float den = rbf(sqrtf(rbf(var + 1e-5f)));
            const float rc = 1.f / den;
            float o[8];
#pragma unroll
            for (int k = 0; k < 8; ++k) { const float t = rbf(v[k] - mb); const float q0 = t * rc; const float e = fmaf(-q0, den, t); o[k] = fmaf(e, rc, q0); }
            LDS16(L_XN + w * XN_STR + lane * 16) = (v4u){pk_bf(o[0], o[1]), pk_bf(o[2], o[3]), pk_bf(o[4], o[5]), pk_bf(o[6], o[7])};
        }
        lds_arrive(L, F_XN, lane);
        STAMP(1);
        LDS16(L_WQ + w * 2048 + lane * 16) = wqv[0];
        LDS16(L_WQ + w * 2048 + 1024 + lane * 16) = wqv[1];
        LDS16(L_W1 + w * 1024 + lane * 16) = w1v;
        LDS16(L_W2 + w * 1024 + lane * 16) = (w < 7) ? w2v : (v4u){0u, 0u, 0u, 0u};
        STAMP(2);
        lds_wait_ge(L, F_XN, 8);
        {
            f32x4 dq = {0.f, 0.f, 0.f, 0.f}, dh = {0.f, 0.f, 0.f, 0.f};
#pragma unroll
            for (int ks = 0; ks < 2; ++ks) {
                const v4u xf = LDS16(L_XN + (n16 & 7) * XN_STR + (64 * w + 32 * ks + 8 * g) * 2);
                const int row = 32 * ks + 8 * g + q4;
                const v4u wf = trfrag(L + L_WQ + w * 2048 + row * 32 + 8 * p4, L + L_WQ + w * 2048 + (row + 4) * 32 + 8 * p4);
                const v4u uf = trfrag(L + L_W1 + w * 1024 + row * 16 + 8 * (p4 & 1), L + L_W1 + w * 1024 + (row + 4) * 16 + 8 * (p4 & 1));
                dq = mfma16(wf, xf, dq);
                dh = mfma16(uf, xf, dh);
            }
            CFENCE();
            *(LAS f32x4*)(L + L_WQ + w * 2048 + lane * 16) = dq;
            *(LAS f32x4*)(L + L_WQ + w * 2048 + 1024 + lane * 16) = dh;
        }
        lds_arrive(L, F_RED, lane);
        STAMP(3);
        lds_wait_ge(L, F_PROG, w + 1);
        v4u kf[8];
#pragma unroll
        for (int i = 0; i < 8; ++i) kf[i] = LDS16(L_RING + w * 8192 + (16 * (i >> 2) + n16) * 256 + (((4 * (i & 3) + g) ^ n16) << 4));
        asm volatile("s_waitcnt lgkmcnt(0)" ::: "memory");
        asm volatile("" : "+v"(kf[0]), "+v"(kf[1]), "+v"(kf[2]), "+v"(kf[3]), "+v"(kf[4]), "+v"(kf[5]), "+v"(kf[6]), "+v"(kf[7]) : : "memory");
        if (lane == 0) VLDS4(F_FREE + 4 * w) = 1u;
        STAMP(4);
        lds_wait_ge(L, F_Q, 1);
        STAMP(5);
        const unsigned tagA = LDS4(F_TAG), tagB = LDS4(F_TAG + 4);
        {
            v4u qf[4];
#pragma unroll
            for (int t = 0; t < 4; ++t) qf[t] = LDS16(L_Q + (n16 & 3) * 272 + (32 * t + 8 * g) * 2);
            f32x4 s0 = {0.f, 0.f, 0.f, 0.f}, s1 = {0.f, 0.f, 0.f, 0.f};
#pragma unroll
            for (int t = 0; t < 4; ++t) { s0 = mfma16(kf[t], qf[t], s0); s1 = mfma16(kf[4 + t], qf[t], s1); }
            float sc[4];
#pragma unroll
            for (int k = 0; k < 4; ++k) sc[k] = rbf((q4 & 1) ? s1[k] : s0[k]) * 0.08838834764831845f;
            if (q4 < 2) st_sc1(rs, WS_SC + (unsigned)((((b_att * 8 + hkv) * 32 + s_att * 8 + w) * 16 + (n16 & 3) * 4 + g) * 32 + q4 * 16), (v4u){pk_bf(sc[0], sc[1]), tagA, pk_bf(sc[2], sc[3]), tagB});
        }
        STAMP(6);
        lds_wait_ge(L, F_PROG, 8 + 2 * w + 1);
        v4u vf[8];
#pragma unroll
        for (int i = 0; i < 8; ++i) {
            const int row = 32 * (i >> 1) + 4 * g + q4, pc = ((2 * (i & 1) + (p4 >> 1)) ^ ((g & 1) << 1)) * 16 + 8 * (p4 & 1);
            vf[i] = trfrag(L + L_RING + (8 + w) * 8192 + row * 64 + pc, L + L_RING + (8 + w) * 8192 + (row + 16) * 64 + pc);
        }
        asm volatile("s_waitcnt lgkmcnt(0)" ::: "memory");
        asm volatile("" : "+v"(vf[0]), "+v"(vf[1]), "+v"(vf[2]), "+v"(vf[3]), "+v"(vf[4]), "+v"(vf[5]), "+v"(vf[6]), "+v"(vf[7]) : : "memory");
        if (lane == 0) VLDS4(F_FREE + 4 * (8 + w)) = 1u;
        STAMP(7);
        {
            const unsigned so = WS_SC + (unsigned)((((b_att * 8 + hkv) * 32 + 4 * w + q4) * 16 + (n16 & 3) * 4 + g) * 32);
            v4u A0, A1;
#pragma clang loop unroll(disable)
            for (unsigned spin = 0;; ++spin) {
                CFENCE();
                A0 = ld_sc1(rs, so); A1 = ld_sc1(rs, so + 16);
                const bool ok = A0.y == tagA && A0.w == tagB && A1.y == tagA && A1.w == tagB;
                if (__all(ok) || spin > SPIN_CAP) break;
            }
            STAMP(8);
            float sc[8] = {bf_lo(A0.x), bf_hi(A0.x), bf_lo(A0.z), bf_hi(A0.z), bf_lo(A1.x), bf_hi(A1.x), bf_lo(A1.z), bf_hi(A1.z)};
            float m = fmaxf(fmaxf(fmaxf(sc[0], sc[1]), fmaxf(sc[2], sc[3])), fmaxf(fmaxf(sc[4], sc[5]), fmaxf(sc[6], sc[7])));
            m = fmaxf(m, dpp<0x124>(m)); m = fmaxf(m, dpp<0x128>(m));
            m = x32_max(x16_max(m));
            float p[8], l = 0.f;
#pragma unroll
            for (int k = 0; k < 8; ++k) { p[k] = __builtin_amdgcn_exp2f((sc[k] - m) * LOG2E); l += p[k]; }
            const v4u P = {pk_bf(p[0], p[1]), pk_bf(p[2], p[3]), pk_bf(p[4], p[5]), pk_bf(p[6], p[7])};
            l += dpp<0x124>(l); l += dpp<0x128>(l);
            l = x32_sum(x16_sum(l));
            f32x4 d0 = {0.f, 0.f, 0.f, 0.f}, d1 = {0.f, 0.f, 0.f, 0.f};
            d0 = mfma16(vf[0], P, d0); d1 = mfma16(vf[1], P, d1);
            { const v4u B = dpp4<0x12C>(P); d0 = mfma16(vf[2], B, d0); d1 = mfma16(vf[3], B, d1); }
            { const v4u B = dpp4<0x128>(P); d0 = mfma16(vf[4], B, d0); d1 = mfma16(vf[5], B, d1); }
            { const v4u B = dpp4<0x124>(P); d0 = mfma16(vf[6], B, d0); d1 = mfma16(vf[7], B, d1); }
            if (n16 < 4) {
                *(LAS f32x4*)(L + L_OP + w * 512 + n16 * 128 + g * 16) = d0;
                *(LAS f32x4*)(L + L_OP + w * 512 + n16 * 128 + 64 + g * 16) = d1;
                if (g == 0) *(LAS f32x2*)(L + L_ML + n16 * 64 + w * 8) = (f32x2){m, l};
            }
        }
        lds_arrive(L, F_O, lane);
        STAMP(9);
        lds_wait_ge(L, F_O, 8);
        STAMP(10);
        v4u R;
        {
            float ms[8], ls[8];
#pragma unroll
            for (int k = 0; k < 4; ++k) { const f32x4 t = *(LAS f32x4*)(L + L_ML + q4 * 64 + k * 16); ms[2 * k] = t[0]; ls[2 * k] = t[1]; ms[2 * k + 1] = t[2]; ls[2 * k + 1] = t[3]; }
            const f32x4 own = *(LAS f32x4*)(L + L_ML + q4 * 64 + p4 * 16);
            const f32x4 a0 = *(LAS f32x4*)(L + L_OP + (2 * p4) * 512 + q4 * 128 + g * 16), a1 = *(LAS f32x4*)(L + L_OP + (2 * p4) * 512 + q4 * 128 + 64 + g * 16);
            const f32x4 c0 = *(LAS f32x4*)(L + L_OP + (2 * p4 + 1) * 512 + q4 * 128 + g * 16), c1 = *(LAS f32x4*)(L + L_OP + (2 * p4 + 1) * 512 + q4 * 128 + 64 + g * 16);
            float M = ms[0];
#pragma unroll
            for (int k = 1; k < 8; ++k) M = fmaxf(M, ms[k]);
            float Ls = 0.f;
#pragma unroll
            for (int k = 0; k < 8; ++k) Ls = fmaf(__builtin_amdgcn_exp2f((ms[k] - M) * LOG2E), ls[k], Ls);
            const float inv = 1.f / Ls;
            const float wa = __builtin_amdgcn_exp2f((own[0] - M) * LOG2E) * inv, wb = __builtin_amdgcn_exp2f((own[2] - M) * LOG2E) * inv;
            float o[8];
#pragma unroll
            for (int k = 0; k < 4; ++k) { o[k] = fmaf(wa, a0[k], wb * c0[k]); o[4 + k] = fmaf(wa, a1[k], wb * c1[k]); }
#pragma unroll
            for (int k = 0; k < 8; ++k) { o[k] += dpp<0xB1>(o[k]); o[k] += dpp<0x4E>(o[k]); }
            R = (v4u){pk_bf(o[0], o[1]), pk_bf(o[2], o[3]), pk_bf(o[4], o[5]), pk_bf(o[6], o[7])};
        }
        {
            f32x4 d[4];
#pragma unroll
            for (int nt = 0; nt < 4; ++nt) d[nt] = (f32x4){0.f, 0.f, 0.f, 0.f};
#pragma unroll
            for (int c = 0; c < 4; ++c) {
                lds_wait_ge(L, F_PROG, c == 0 ? 16 : 20 + 4 * c);
                const v4u B = c == 0 ? dpp4<0x150>(R) : c == 1 ? dpp4<0x154>(R) : c == 2 ? dpp4<0x158>(R) : dpp4<0x15C>(R);
                const int row = 32 * c + 4 * g + q4;
#pragma unroll
                for (int nt = 0; nt < 4; ++nt) {
                    const int pc = ((8 * w + 2 * nt + (p4 >> 1)) ^ ((row & 7) << 1)) * 16 + 8 * (p4 & 1);
                    const v4u wf = trfrag(L + L_RING + row * 1024 + pc, L + L_RING + (row + 16) * 1024 + pc);
                    d[nt] = mfma16(wf, B, d[nt]);
                }
            }
            STAMP(11);
            const f32x4 e = q4 == 0 ? d[0] : q4 == 1 ? d[1] : q4 == 2 ? d[2] : d[3];
            const float v0 = (p4 & 1) ? e[2] : e[0], v1 = (p4 & 1) ? e[3] : e[1];
            if (p4 < 2) st_sc1(rs, WS_S + (unsigned)((((b_att * 32 + 4 * w + q4) * 32 + hkv * 4 + s_att) * 16 + 4 * g + 2 * p4) * 8), (v4u){__float_as_uint(v0), tagA, __float_as_uint(v1), tagB});
        }
        STAMP(12);
    } else if (wave == 8) {
        const unsigned long long ep = __hip_atomic_load((GAS unsigned long long*)(a.ws + WS_EPOCH), __ATOMIC_RELAXED, __HIP_MEMORY_SCOPE_AGENT);
        const v2u b1v = *(const GAS v2u*)(a.b1 + 8 * fg + 4 * (g & 1));
        const unsigned xfin = *(const GAS unsigned*)(a.x + b_fin * 512 + 16 * cg_fin + 2 * (lane & 7));
        const unsigned b2v = *(const GAS unsigned*)(a.b2 + 16 * cg_fin + 2 * (lane & 7));
        if (lane < 24) LDS4(L_FLG + lane * 4) = 0u;
        if (lane < 16) LDS16(L_H + (lane >> 1) * 272 + 224 + (lane & 1) * 16) = (v4u){0u, 0u, 0u, 0u};
        asm volatile("s_waitcnt lgkmcnt(0)" ::: "memory");
        __builtin_amdgcn_sched_barrier(0);
        __builtin_amdgcn_s_barrier();
        CFENCE();
        const unsigned tagA = (unsigned)ep + 1u, tagB = (unsigned)(ep >> 32) + 1u;
        if (lane == 0) { LDS4(F_TAG) = tagA; LDS4(F_TAG + 4) = tagB; }
        STAMP(1);
        lds_wait_ge(L, F_RED, 8);
        STAMP(3);
        {
            f32x4 dq = {0.f, 0.f, 0.f, 0.f}, dh = {0.f, 0.f, 0.f, 0.f};
#pragma unroll
            for (int w = 0; w < 8; ++w) { dq += *(LAS f32x4*)(L + L_WQ + w * 2048 + lane * 16); dh += *(LAS f32x4*)(L + L_WQ + w * 2048 + 1024 + lane * 16); }
            if (n16 < 8) st_sc1(rs, WS_Q + (unsigned)((n16 * 2048 + 8 * cg + 2 * g) * 8), (v4u){pk_bf(dq[0], dq[1]), tagA, pk_bf(dq[2], dq[3]), tagB});
            CFENCE();
            if (lane == 0) VLDS4(F_EARLY) = 1u;
            float hv[4];
#pragma unroll
            for (int k = 0; k < 4; ++k) {
                const float bb = (k & 1) ? bf_hi(b1v[k >> 1]) : bf_lo(b1v[k >> 1]);
                const float t = rbf(rbf(dh[k]) + bb);
                const float u = 0.7978845608028654f * (t + 0.044715f * t * t * t);
                hv[k] = t / (1.f + __builtin_amdgcn_exp2f(-2.f * LOG2E * u));
            }
            if (n16 < 8 && g < 2 && r < 28) st_sc1(rs, WS_H + (unsigned)((n16 * 896 + 4 * fg + 2 * g) * 8), (v4u){pk_bf(hv[0], hv[1]), tagA, pk_bf(hv[2], hv[3]), tagB});
        }
        STAMP(4);
        {
            unsigned qo[2], ho[4]; int hdst[4];
#pragma unroll
            for (int k = 0; k < 2; ++k) qo[k] = WS_Q + (unsigned)((b_att * 2048 + hkv * 256 + 2 * (lane + 64 * k)) * 8);
#pragma unroll
            for (int k = 0; k < 4; ++k) { int p = lane + 64 * k; if (p >= 224) p -= 64; const int b = p / 28, i = p - 28 * b; ho[k] = WS_H + (unsigned)((b * 896 + 56 * rgf + 2 * i) * 8); hdst[k] = L_H + b * 272 + i * 8; }
            v4u Q[2], H[4];
#pragma clang loop unroll(disable)
            for (unsigned spin = 0;; ++spin) {
                CFENCE();
                bool ok = true;
#pragma unroll
                for (int k = 0; k < 2; ++k) Q[k] = ld_sc1(rs, qo[k]);
#pragma unroll
                for (int k = 0; k < 2; ++k) ok = ok && Q[k].y == tagA && Q[k].w == tagB;
                if (__all(ok) || spin > SPIN_CAP) break;
            }
#pragma unroll
            for (int k = 0; k < 2; ++k) { const int p = lane + 64 * k; LDS8(L_Q + (p >> 5) * 272 + (p & 31) * 8) = (v2u){Q[k].x, Q[k].z}; }
            CFENCE();
            if (lane == 0) VLDS4(F_Q) = 1u;
            STAMP(5);
#pragma clang loop unroll(disable)
            for (unsigned spin = 0;; ++spin) {
                CFENCE();
                bool ok = true;
#pragma unroll
                for (int k = 0; k < 4; ++k) H[k] = ld_sc1(rs, ho[k]);
#pragma unroll
                for (int k = 0; k < 4; ++k) ok = ok && H[k].y == tagA && H[k].w == tagB;
                if (__all(ok) || spin > SPIN_CAP) break;
            }
#pragma unroll
            for (int k = 0; k < 4; ++k) LDS8(hdst[k]) = (v2u){H[k].x, H[k].z};
        }
        STAMP(6);
        {
            f32x4 d0 = {0.f, 0.f, 0.f, 0.f}, d1 = {0.f, 0.f, 0.f, 0.f};
#pragma unroll
            for (int ks = 0; ks < 4; ++ks) {
                const int row = 32 * ks + 8 * g + q4;
                const v4u hf = LDS16(L_H + (n16 & 7) * 272 + (32 * ks + 8 * g) * 2);
                const v4u f0 = trfrag(L + L_W2 + row * 64 + 8 * p4, L + L_W2 + (row + 4) * 64 + 8 * p4);
                const v4u f1 = trfrag(L + L_W2 + row * 64 + 32 + 8 * p4, L + L_W2 + (row + 4) * 64 + 32 + 8 * p4);
                d0 = mfma16(f0, hf, d0); d1 = mfma16(f1, hf, d1);
            }
            const int b = n16 & 7, cp = n16 >> 3, f = b * 32 + 2 * cf;
            st_sc1(rs, WS_Y + (unsigned)(((f * 16 + rgf) * 16 + 4 * g + 2 * cp) * 8), (v4u){__float_as_uint(cp ? d0[2] : d0[0]), tagA, __float_as_uint(cp ? d0[3] : d0[1]), tagB});
            st_sc1(rs, WS_Y + (unsigned)((((f + 1) * 16 + rgf) * 16 + 4 * g + 2 * cp) * 8), (v4u){__float_as_uint(cp ? d1[2] : d1[0]), tagA, __float_as_uint(cp ? d1[3] : d1[1]), tagB});
        }
        STAMP(7);
        lds_wait_ge(L, F_O, 8);
        STAMP(11);
        {
            v4u S[4], Y[2];
#pragma clang loop unroll(disable)
            for (unsigned spin = 0;; ++spin) {
                CFENCE();
                bool ok = true;
#pragma unroll
                for (int k = 0; k < 4; ++k) S[k] = ld_sc1(rs, WS_S + (unsigned)(j * 4096 + (lane + 64 * k) * 16));
#pragma unroll
                for (int k = 0; k < 2; ++k) Y[k] = ld_sc1(rs, WS_Y + (unsigned)(j * 2048 + (lane + 64 * k) * 16));
#pragma unroll
                for (int k = 0; k < 4; ++k) ok = ok && S[k].y == tagA && S[k].w == tagB;
#pragma unroll
                for (int k = 0; k < 2; ++k) ok = ok && Y[k].y == tagA && Y[k].w == tagB;
                if (__all(ok) || spin > SPIN_CAP) break;
            }
            STAMP(12);
            float s0 = 0.f, s1 = 0.f;
#pragma unroll
            for (int k = 0; k < 4; ++k) { s0 += __uint_as_float(S[k].x); s1 += __uint_as_float(S[k].z); }
            float y0 = __uint_as_float(Y[0].x) + __uint_as_float(Y[1].x), y1 = __uint_as_float(Y[0].z) + __uint_as_float(Y[1].z);
            s0 += dpp<0x128>(s0); s1 += dpp<0x128>(s1); y0 += dpp<0x128>(y0); y1 += dpp<0x128>(y1);
            s0 = x32_sum(x16_sum(s0)); s1 = x32_sum(x16_sum(s1)); y0 = x32_sum(x16_sum(y0)); y1 = x32_sum(x16_sum(y1));
            const float ya = rbf(rbf(y0) + bf_lo(b2v)), yb = rbf(rbf(y1) + bf_hi(b2v));
            const float ga = rbf(ya / (1.f + __expf(-ya))), gb = rbf(yb / (1.f + __expf(-yb)));
            const float oa = bf_lo(xfin) + rbf(rbf(s0) * ga), ob = bf_hi(xfin) + rbf(rbf(s1) * gb);
            if (lane < 8) *(GAS unsigned*)(a.out + b_fin * 512 + 16 * cg_fin + 2 * lane) = pk_bf(oa, ob);
            if (j == 0 && lane == 0) __hip_atomic_store((GAS unsigned long long*)(a.ws + WS_EPOCH), ((unsigned long long)tagB << 32) | tagA, __ATOMIC_RELAXED, __HIP_MEMORY_SCOPE_AGENT);
        }
        STAMP(13);
    } else {
        const unsigned lbase = (unsigned)(size_t)L;
        const size_t kvrow = (size_t)(b_att * 8 + hkv) * 4096 + 3072;
        const unsigned char* kb = (const unsigned char*)a.kc + (kvrow + 256 * s_att) * 256;
        const unsigned char* vb = (const unsigned char*)a.vc + kvrow * 256 + 64 * s_att;
        const unsigned char* wb = (const unsigned char*)a.wo + (size_t)(hkv * 512 + 32 * s_att) * 1024;
        const unsigned kv0 = (unsigned)(g * 256 + ((n16 ^ g) << 4));
        const unsigned vv0 = (unsigned)((lane >> 2) * 256 + ((p4 ^ ((g & 1) << 1)) << 4));
        const unsigned wv0 = (unsigned)(lane << 4);
        int issued = 0, pub = 0;
#define UNIT_DONE() do { ++issued; asm volatile("s_waitcnt vmcnt(%0)" : : "n"(8 * (WIN - 1)) : "memory"); if (issued - (WIN - 1) > pub) { pub = issued - (WIN - 1); if (lane == 0) VLDS4(F_PROG) = (unsigned)pub; } } while (0)
#define WAIT_FREE(off) do { if (__builtin_amdgcn_readfirstlane(VLDS4(off)) == 0) { asm volatile("s_waitcnt vmcnt(0)" ::: "memory"); pub = issued; if (lane == 0) VLDS4(F_PROG) = (unsigned)pub; lds_wait_ge(L, off, 1); } } while (0)
#define K_UNIT(u) do { _Pragma("unroll") for (int i = 0; i < 8; ++i) glds<KV_NT>(kb + (u) * 8192 + i * 1024, kv0 ^ ((i & 3) << 6), lbase + L_RING + (u) * 8192 + i * 1024); } while (0)
#define V_UNIT(u) do { _Pragma("unroll") for (int i = 0; i < 8; ++i) glds<KV_NT>(vb + (u) * 32768 + i * 4096, vv0, lbase + L_RING + (8 + (u)) * 8192 + i * 1024); } while (0)
#define W_UNIT(u) do { _Pragma("unroll") for (int i = 0; i < 8; ++i) glds<0>(wb + ((u) >> 2) * 131072 + ((u) & 3) * 8192 + i * 1024, wv0 ^ (i << 5), lbase + L_RING + (u) * 8192 + i * 1024); } while (0)
#pragma unroll 1
        for (int u = 0; u < WIN - 1; ++u) { K_UNIT(u); ++issued; }
        __builtin_amdgcn_s_barrier();
        STAMP(1);
#pragma unroll 1
        for (int u = WIN - 1; u < 8; ++u) { K_UNIT(u); UNIT_DONE(); }
        STAMP(2);
#pragma unroll 1
        for (int u = 0; u < 8; ++u) {
            if (u == 5) WAIT_FREE(F_EARLY);
            V_UNIT(u); UNIT_DONE();
            WAIT_FREE(F_FREE + 4 * u);
            W_UNIT(u); UNIT_DONE();
        }
        STAMP(3);
#pragma unroll 1
        for (int u = 8; u < 16; ++u) {
            WAIT_FREE(F_FREE + 4 * u);
            W_UNIT(u); UNIT_DONE();
        }
        STAMP(4);
#pragma unroll
        for (int k = WIN - 2; k >= 0; --k) {
            asm volatile("s_waitcnt vmcnt(%0)" : : "n"(8 * k) : "memory");
            if (32 - k > pub && lane == 0) VLDS4(F_PROG) = 32 - k;
        }
        STAMP(5);
    }
}

extern "C" void kernel_launch(void* const* d_in, const int* in_sizes, int n_in,
                              void* d_out, int out_size, void* d_ws, size_t ws_size,
                              hipStream_t stream) {
    Args a;
    a.x = (const u16*)d_in[0]; a.kc = (const u16*)d_in[1]; a.vc = (const u16*)d_in[2]; a.wq = (const u16*)d_in[3]; a.wo = (const u16*)d_in[4];
    a.w1 = (const u16*)d_in[6]; a.b1 = (const u16*)d_in[7]; a.w2 = (const u16*)d_in[8]; a.b2 = (const u16*)d_in[9];
    a.out = (u16*)d_out; a.ws = (unsigned char*)d_ws;
    (void)hipFuncSetAttribute((const void*)fused_decode, hipFuncAttributeMaxDynamicSharedMemorySize, L_BYTES);
    hipLaunchKernelGGL(fused_decode, dim3(NWG), dim3(NTHR), L_BYTES, stream, a);
}
```

```cpp
#include <hip/hip_runtime.h>
#include <hip/hip_bf16.h>
#include <stdint.h>

typedef unsigned short u16;
typedef unsigned v4u __attribute__((ext_vector_type(4)));
typedef unsigned v2u __attribute__((ext_vector_type(2)));
typedef float f32x4 __attribute__((ext_vector_type(4)));
typedef float f32x2 __attribute__((ext_vector_type(2)));
typedef __bf16 bf16x8 __attribute__((ext_vector_type(8)));
typedef __bf16 bf16x2 __attribute__((ext_vector_type(2)));
typedef short v4i16 __attribute__((ext_vector_type(4)));
#define GAS __attribute__((address_space(1)))
#define LAS __attribute__((address_space(3)))

#define STAMPS 0
#define WIN 4
#define V_NT 1
#define CODE_KB 10

constexpr int NWG = 256, NWAVE = 10, NTHR = NWAVE * 64;
constexpr unsigned WS_EPOCH = 0;
constexpr unsigned WS_Q = 4096;
constexpr unsigned WS_H = WS_Q + 8 * 2048 * 8;
constexpr unsigned WS_SC = WS_H + 8 * 896 * 8;
constexpr unsigned WS_S = WS_SC + 64 * 32 * 512;
constexpr unsigned WS_Y = WS_S + 256 * 32 * 16 * 8;
constexpr unsigned WS_END = WS_Y + 256 * 16 * 16 * 8;
constexpr unsigned WS_STAMP = 8u << 20;
constexpr int L_RING = 0;
constexpr int L_WQ = 13 * 8192;
constexpr int L_W1 = L_WQ + 16384;
constexpr int L_W2 = 131072;
constexpr int L_Q = L_W2 + 8192;
constexpr int L_H = L_Q + 4 * 272;
constexpr int L_OP = L_H + 8 * 272;
constexpr int L_ML = L_OP + 4096;
constexpr int L_ST = L_ML + 256;
constexpr int L_FLG = L_ST + 64;
constexpr int F_PROG = L_FLG, F_RED = L_FLG + 4, F_Q = L_FLG + 8, F_O = L_FLG + 12;
constexpr int F_TAG = L_FLG + 16;
constexpr int F_FREE = L_FLG + 32;
constexpr int L_BYTES = F_FREE + 64;
static_assert(L_BYTES <= 163840, "LDS");
constexpr unsigned SPIN_CAP = 1u << 18;

struct Args {
    const u16* x; const u16* kc; const u16* vc; const u16* wq; const u16* wo;
    const u16* w1; const u16* b1; const u16* w2; const u16* b2; u16* out; unsigned char* ws;
};

__device__ __forceinline__ float bf_lo(unsigned u) { return __uint_as_float(u << 16); }
__device__ __forceinline__ float bf_hi(unsigned u) { return __uint_as_float(u & 0xffff0000u); }
__device__ __forceinline__ unsigned pk_bf(float lo, float hi) { bf16x2 v = {(__bf16)lo, (__bf16)hi}; return __builtin_bit_cast(unsigned, v); }
__device__ __forceinline__ float rbf(float f) { return (float)(__bf16)f; }
__device__ __forceinline__ float rcp(float f) { return __builtin_amdgcn_rcpf(f); }
__device__ __forceinline__ float ex2(float f) { return __builtin_amdgcn_exp2f(f); }
__device__ __forceinline__ f32x4 mfma16(v4u a, v4u b, f32x4 c) {
    return __builtin_amdgcn_mfma_f32_16x16x32_bf16(__builtin_bit_cast(bf16x8, a), __builtin_bit_cast(bf16x8, b), c, 0, 0, 0);
}
__device__ __forceinline__ v2u trread(LAS unsigned char* p) { return __builtin_bit_cast(v2u, __builtin_amdgcn_ds_read_tr16_b64_v4i16((LAS v4i16*)p)); }
__device__ __forceinline__ v4u trfrag(LAS unsigned char* p0, LAS unsigned char* p1) { v2u a = trread(p0), b = trread(p1); return (v4u){a.x, a.y, b.x, b.y}; }
template <int CTRL> __device__ __forceinline__ unsigned dppu(unsigned x) { return (unsigned)__builtin_amdgcn_mov_dpp((int)x, CTRL, 0xf, 0xf, true); }
template <int CTRL> __device__ __forceinline__ float dpp(float x) { return __uint_as_float(dppu<CTRL>(__float_as_uint(x))); }
template <int CTRL> __device__ __forceinline__ v4u dpp4(v4u x) { return (v4u){dppu<CTRL>(x.x), dppu<CTRL>(x.y), dppu<CTRL>(x.z), dppu<CTRL>(x.w)}; }
__device__ __forceinline__ float x16_sum(float x) { auto s = __builtin_amdgcn_permlane16_swap(__float_as_uint(x), __float_as_uint(x), false, false); return __uint_as_float(s[0]) + __uint_as_float(s[1]); }
__device__ __forceinline__ float x32_sum(float x) { auto s = __builtin_amdgcn_permlane32_swap(__float_as_uint(x), __float_as_uint(x), false, false); return __uint_as_float(s[0]) + __uint_as_float(s[1]); }
__device__ __forceinline__ float x16_max(float x) { auto s = __builtin_amdgcn_permlane16_swap(__float_as_uint(x), __float_as_uint(x), false, false); return fmaxf(__uint_as_float(s[0]), __uint_as_float(s[1])); }
__device__ __forceinline__ float x32_max(float x) { auto s = __builtin_amdgcn_permlane32_swap(__float_as_uint(x), __float_as_uint(x), false, false); return fmaxf(__uint_as_float(s[0]), __uint_as_float(s[1])); }
__device__ __forceinline__ float wave_sum(float x) {
    x += dpp<0xB1>(x); x += dpp<0x4E>(x); x += dpp<0x141>(x); x += dpp<0x140>(x);
    return x32_sum(x16_sum(x));
}
__device__ __forceinline__ v4u ldg(const u16* p) { return *(const GAS v4u*)p; }
__device__ __forceinline__ v4u ld_sc1(__amdgpu_buffer_rsrc_t rs, unsigned off) { return __builtin_amdgcn_raw_buffer_load_b128(rs, off, 0, 16); }
__device__ __forceinline__ void st_sc1(__amdgpu_buffer_rsrc_t rs, unsigned off, v4u v) { __builtin_amdgcn_raw_buffer_store_b128(v, rs, off, 0, 16); }
#define CFENCE() asm volatile("" ::: "memory")
#define LDS16(off) (*(LAS v4u*)(L + (off)))
#define LDS8(off) (*(LAS v2u*)(L + (off)))
#define LDS4(off) (*(LAS unsigned*)(L + (off)))
#define VLDS4(off) (*(volatile LAS unsigned*)(L + (off)))
#define STAMP(i) do { } while (0)

template <int NT> __device__ __forceinline__ void glds(const void* base, unsigned voff, unsigned dst) {
    if (NT) asm volatile("s_mov_b32 m0, %2\n\ts_nop 0\n\tglobal_load_lds_dwordx4 %0, %1 nt" : : "v"(voff), "s"(base), "s"(dst) : "memory", "m0");
    else    asm volatile("s_mov_b32 m0, %2\n\ts_nop 0\n\tglobal_load_lds_dwordx4 %0, %1" : : "v"(voff), "s"(base), "s"(dst) : "memory", "m0");
}
__device__ __forceinline__ void lds_wait_ge(LAS unsigned char* L, int off, unsigned need) {
#pragma clang loop unroll(disable)
    for (unsigned spin = 0; spin < SPIN_CAP; ++spin) {
        if ((unsigned)__builtin_amdgcn_readfirstlane(VLDS4(off)) >= need) break;
        __builtin_amdgcn_s_sleep(1);
    }
    CFENCE();
}
__device__ __forceinline__ void lds_arrive(LAS unsigned char* L, int off, int lane) {
    CFENCE();
    if (lane == 0) __hip_atomic_fetch_add((LAS unsigned*)(L + off), 1u, __ATOMIC_RELAXED, __HIP_MEMORY_SCOPE_WORKGROUP);
    CFENCE();
}
#define TAGOK(v) ((v).y == tagA && (v).w == tagB)

__global__ void __launch_bounds__(NTHR, 1) fused_decode(Args a) {
    extern __shared__ __attribute__((aligned(16))) unsigned char smem[];
    LAS unsigned char* L = (LAS unsigned char*)smem;
    unsigned long long pc;
    asm volatile("s_getpc_b64 %0" : "=s"(pc));
    const int tid = threadIdx.x, lane = tid & 63, wave = __builtin_amdgcn_readfirstlane(tid >> 6);
    const int j = blockIdx.x, xcd = j & 7, r = j >> 3;
    const int n16 = lane & 15, g = lane >> 4, q4 = n16 >> 2, p4 = lane & 3;
    const __amdgpu_buffer_rsrc_t rs = __builtin_amdgcn_make_buffer_rsrc(a.ws, 0, WS_END, 0x00020000);
    const int hkv = xcd, b_att = r >> 2, s_att = r & 3;
    const int cg = xcd * 32 + r;
    const int fg = xcd * 28 + (r < 28 ? r : 27);
    const int rgf = xcd * 2 + (r >> 4), cf = r & 15;
    const int b_fin = j >> 5, cg_fin = j & 31;
    constexpr float LOG2E = 1.4426950408889634f;
    STAMP(0);

    if (wave == 9) {
        if (lane < 24) LDS4(L_FLG + lane * 4) = (lane >= 16 && lane < 21) ? 1u : 0u;
        {
            const unsigned char* cb = (const unsigned char*)(pc & ~255ull);
            glds<0>(cb + (r % CODE_KB) * 1024, (unsigned)(lane * 16), (unsigned)(size_t)L + L_OP);
            glds<0>(cb + ((r + CODE_KB / 2) % CODE_KB) * 1024, (unsigned)(lane * 16), (unsigned)(size_t)L + L_OP + 1024);
        }
        asm volatile("s_waitcnt lgkmcnt(0)" ::: "memory");
        __builtin_amdgcn_s_barrier();
        STAMP(1);
        const unsigned lbase = (unsigned)(size_t)L + L_RING;
        const size_t kvrow = (size_t)(b_att * 8 + hkv) * 4096 + 3072;
        const unsigned char* kb = (const unsigned char*)a.kc + (kvrow + 256 * s_att) * 256;
        const unsigned char* vb = (const unsigned char*)a.vc + kvrow * 256 + 64 * s_att;
        const unsigned char* wb = (const unsigned char*)a.wo + (size_t)(hkv * 512 + 32 * s_att) * 1024;
        const unsigned kv0 = (unsigned)(g * 256 + ((n16 ^ g) << 4));
        const unsigned vv0 = (unsigned)((lane >> 2) * 256 + ((p4 ^ ((g & 1) << 1)) << 4));
        const unsigned wv0 = (unsigned)(lane << 4);
        int pub = 0;
#pragma clang loop unroll(disable)
        for (int p = 0; p < 32; ++p) {
            const bool isK = p < 8, isV = !isK && p < 24 && !(p & 1);
            const int u = isK ? p : p < 24 ? (p - 8) >> 1 : p - 16;
            const int slot = isV ? 8 + u : u;
            const unsigned need = isK ? 0u : p < 24 ? 1u : 2u;
            const unsigned char* gb = isK ? kb + u * 8192 : isV ? vb + u * 32768 : wb + (u >> 2) * 131072 + (u & 3) * 8192;
            const unsigned gstr = isV ? 4096u : 1024u, xs = isK ? 64u : isV ? 0u : 32u, xm = isK ? 0xC0u : 0xE0u;
            const unsigned vo = isK ? kv0 : isV ? vv0 : wv0;
            if ((unsigned)__builtin_amdgcn_readfirstlane(VLDS4(F_FREE + 4 * slot)) < need) {
                asm volatile("s_waitcnt vmcnt(0)" ::: "memory");
                pub = p; if (lane == 0) VLDS4(F_PROG) = (unsigned)p;
                lds_wait_ge(L, F_FREE + 4 * slot, need);
            }
            if (isK || (V_NT && isV)) {
#pragma unroll 4
                for (unsigned i = 0; i < 8; ++i) glds<1>(gb + i * gstr, vo ^ ((i * xs) & xm), lbase + slot * 8192 + i * 1024);
            } else {
#pragma unroll 4
                for (unsigned i = 0; i < 8; ++i) glds<0>(gb + i * gstr, vo ^ ((i * xs) & xm), lbase + slot * 8192 + i * 1024);
            }
            asm volatile("s_waitcnt vmcnt(%0)" : : "n"(8 * (WIN - 1)) : "memory");
            if (p + 2 - WIN > pub) { pub = p + 2 - WIN; if (lane == 0) VLDS4(F_PROG) = (unsigned)pub; }
        }
        STAMP(4);
#pragma unroll
        for (int k = WIN - 2; k >= 0; --k) {
            asm volatile("s_waitcnt vmcnt(%0)" : : "n"(8 * k) : "memory");
            if (32 - k > pub && lane == 0) VLDS4(F_PROG) = 32 - k;
        }
        STAMP(5);
    } else if (wave < 8) {
        const int w = wave;
        v4u wqv[2], xf[2];
#pragma unroll
        for (int i = 0; i < 2; ++i) wqv[i] = ldg(a.wq + (size_t)(64 * w + 32 * i + (lane >> 1)) * 4096 + 16 * cg + 8 * (lane & 1));
        const v4u w1v = ldg(a.w1 + (size_t)(64 * w + lane) * 1792 + 8 * fg);
        const v4u xv = ldg(a.x + w * 512 + lane * 8);
#pragma unroll
        for (int i = 0; i < 2; ++i) xf[i] = ldg(a.x + (n16 & 7) * 512 + 64 * w + 32 * i + 8 * g);
        const v4u w2v = ldg(a.w2 + (size_t)(112 * rgf + 16 * (w < 7 ? w : 6) + (lane >> 2)) * 512 + 32 * cf + 8 * (lane & 3));
        CFENCE();
        __builtin_amdgcn_sched_barrier(0);
        __builtin_amdgcn_s_barrier();
        __builtin_amdgcn_sched_barrier(0);
        CFENCE();
        STAMP(1);
        {
            LDS16(L_WQ + w * 2048 + lane * 16) = wqv[0];
            LDS16(L_WQ + w * 2048 + 1024 + lane * 16) = wqv[1];
            LDS16(L_W1 + w * 1024 + lane * 16) = w1v;
            STAMP(2);
            f32x4 dq = {0.f, 0.f, 0.f, 0.f}, dh = {0.f, 0.f, 0.f, 0.f};
#pragma unroll
            for (int ks = 0; ks < 2; ++ks) {
                const v4u xb = n16 == 8 ? (v4u){0x3F803F80u, 0x3F803F80u, 0x3F803F80u, 0x3F803F80u} : xf[ks];
                const int row = 32 * ks + 8 * g + q4;
                const v4u wf = trfrag(L + L_WQ + w * 2048 + row * 32 + 8 * p4, L + L_WQ + w * 2048 + (row + 4) * 32 + 8 * p4);
                const v4u uf = trfrag(L + L_W1 + w * 1024 + row * 16 + 8 * (p4 & 1), L + L_W1 + w * 1024 + (row + 4) * 16 + 8 * (p4 & 1));
                dq = mfma16(wf, xb, dq);
                dh = mfma16(uf, xb, dh);
            }
            const float x0 = bf_lo((unsigned)__builtin_amdgcn_readfirstlane(xv[0]));
            float s = 0.f, q = 0.f;
#pragma unroll
            for (int k = 0; k < 4; ++k) { const float d0 = bf_lo(xv[k]) - x0, d1 = bf_hi(xv[k]) - x0; s += d0 + d1; q = fmaf(d0, d0, fmaf(d1, d1, q)); }
            s = wave_sum(s) * (1.f / 512.f); q = wave_sum(q) * (1.f / 512.f);
            const float var = rbf(fmaxf(fmaf(-s, s, q), 0.f));
            const float den = rbf(sqrtf(rbf(var + 1e-5f)));
            CFENCE();
            *(LAS f32x4*)(L + L_WQ + w * 2048 + lane * 16) = dq;
            *(LAS f32x4*)(L + L_WQ + w * 2048 + 1024 + lane * 16) = dh;
            if (lane == 0) *(LAS f32x2*)(L + L_ST + w * 8) = (f32x2){rbf(x0 + s), rcp(den)};
            LDS16(L_W2 + w * 1024 + lane * 16) = (w < 7) ? w2v : (v4u){0u, 0u, 0u, 0u};
        }
        lds_arrive(L, F_RED, lane);
        STAMP(3);
        lds_wait_ge(L, F_PROG, w + 1);
        v4u kf[8];
#pragma unroll
        for (int i = 0; i < 8; ++i) kf[i] = LDS16(L_RING + w * 8192 + (16 * (i >> 2) + n16) * 256 + (((4 * (i & 3) + g) ^ n16) << 4));
        asm volatile("s_waitcnt lgkmcnt(0)" ::: "memory");
        asm volatile("" : "+v"(kf[0]), "+v"(kf[1]), "+v"(kf[2]), "+v"(kf[3]), "+v"(kf[4]), "+v"(kf[5]), "+v"(kf[6]), "+v"(kf[7]) : : "memory");
        if (lane == 0) VLDS4(F_FREE + 4 * w) = 1u;
        STAMP(4);
        const unsigned sco = WS_SC + (unsigned)((((b_att * 8 + hkv) * 32 + s_att * 8 + w) * 16 + (n16 & 3) * 4 + g) * 32 + q4 * 16);
        lds_wait_ge(L, F_Q, 1);
        STAMP(5);
        const unsigned tagA = LDS4(F_TAG), tagB = LDS4(F_TAG + 4);
        {
            v4u qf[4];
#pragma unroll
            for (int t = 0; t < 4; ++t) qf[t] = LDS16(L_Q + (n16 & 3) * 272 + (32 * t + 8 * g) * 2);
            f32x4 s0 = {0.f, 0.f, 0.f, 0.f}, s1 = {0.f, 0.f, 0.f, 0.f};
#pragma unroll
            for (int t = 0; t < 4; ++t) { s0 = mfma16(kf[t], qf[t], s0); s1 = mfma16(kf[4 + t], qf[t], s1); }
            float sc[4];
#pragma unroll
            for (int k = 0; k < 4; ++k) sc[k] = rbf((q4 & 1) ? s1[k] : s0[k]) * 0.08838834764831845f;
            if (q4 < 2) st_sc1(rs, sco, (v4u){pk_bf(sc[0], sc[1]), tagA, pk_bf(sc[2], sc[3]), tagB});
        }
        STAMP(6);
        lds_wait_ge(L, F_PROG, 8 + 2 * w + 1);
        v4u vf[8];
#pragma unroll
        for (int i = 0; i < 8; ++i) {
            const int row = 32 * (i >> 1) + 4 * g + q4, pc = ((2 * (i & 1) + (p4 >> 1)) ^ ((g & 1) << 1)) * 16 + 8 * (p4 & 1);
            vf[i] = trfrag(L + L_RING + (8 + w) * 8192 + row * 64 + pc, L + L_RING + (8 + w) * 8192 + (row + 16) * 64 + pc);
        }
        asm volatile("s_waitcnt lgkmcnt(0)" ::: "memory");
        asm volatile("" : "+v"(vf[0]), "+v"(vf[1]), "+v"(vf[2]), "+v"(vf[3]), "+v"(vf[4]), "+v"(vf[5]), "+v"(vf[6]), "+v"(vf[7]) : : "memory");
        if (lane == 0) VLDS4(F_FREE + 4 * (8 + w)) = 2u;
        STAMP(7);
        {
            const unsigned so = WS_SC + (unsigned)((((b_att * 8 + hkv) * 32 + 4 * w + q4) * 16 + (n16 & 3) * 4 + g) * 32);
            v4u A0, A1;
#pragma clang loop unroll(disable)
            for (unsigned spin = 0;; ++spin) {
                CFENCE();
                A0 = ld_sc1(rs, so); A1 = ld_sc1(rs, so + 16);
                if (__all(TAGOK(A0) && TAGOK(A1)) || spin > SPIN_CAP) break;
            }
            STAMP(8);
            const float sc[8] = {bf_lo(A0.x), bf_hi(A0.x), bf_lo(A0.z), bf_hi(A0.z), bf_lo(A1.x), bf_hi(A1.x), bf_lo(A1.z), bf_hi(A1.z)};
            float m = fmaxf(fmaxf(fmaxf(sc[0], sc[1]), fmaxf(sc[2], sc[3])), fmaxf(fmaxf(sc[4], sc[5]), fmaxf(sc[6], sc[7])));
            m = fmaxf(m, dpp<0x124>(m)); m = fmaxf(m, dpp<0x128>(m));
            m = x32_max(x16_max(m));
            const float ml = m * LOG2E;
            float p[8];
#pragma unroll
            for (int k = 0; k < 8; ++k) p[k] = ex2(fmaf(sc[k], LOG2E, -ml));
            const v4u P = {pk_bf(p[0], p[1]), pk_bf(p[2], p[3]), pk_bf(p[4], p[5]), pk_bf(p[6], p[7])};
            f32x4 d0 = {0.f, 0.f, 0.f, 0.f}, d1 = {0.f, 0.f, 0.f, 0.f};
            d0 = mfma16(vf[0], P, d0); d1 = mfma16(vf[1], P, d1);
            { const v4u B = dpp4<0x12C>(P); d0 = mfma16(vf[2], B, d0); d1 = mfma16(vf[3], B, d1); }
            { const v4u B = dpp4<0x128>(P); d0 = mfma16(vf[4], B, d0); d1 = mfma16(vf[5], B, d1); }
            { const v4u B = dpp4<0x124>(P); d0 = mfma16(vf[6], B, d0); d1 = mfma16(vf[7], B, d1); }
            float l = ((p[0] + p[1]) + (p[2] + p[3])) + ((p[4] + p[5]) + (p[6] + p[7]));
            l += dpp<0x124>(l); l += dpp<0x128>(l);
            l = x32_sum(x16_sum(l));
            if (n16 < 4) {
                *(LAS f32x4*)(L + L_OP + w * 512 + n16 * 128 + g * 16) = d0;
                *(LAS f32x4*)(L + L_OP + w * 512 + n16 * 128 + 64 + g * 16) = d1;
                if (g == 0) *(LAS f32x2*)(L + L_ML + n16 * 64 + w * 8) = (f32x2){ml, l};
            }
        }
        lds_arrive(L, F_O, lane);
        STAMP(9);
        lds_wait_ge(L, F_O, 8);
        STAMP(10);
        v4u R;
        {
            const f32x4 own = *(LAS f32x4*)(L + L_ML + q4 * 64 + p4 * 16);
            const f32x4 a0 = *(LAS f32x4*)(L + L_OP + (2 * p4) * 512 + q4 * 128 + g * 16), a1 = *(LAS f32x4*)(L + L_OP + (2 * p4) * 512 + q4 * 128 + 64 + g * 16);
            const f32x4 c0 = *(LAS f32x4*)(L + L_OP + (2 * p4 + 1) * 512 + q4 * 128 + g * 16), c1 = *(LAS f32x4*)(L + L_OP + (2 * p4 + 1) * 512 + q4 * 128 + 64 + g * 16);
            float M = fmaxf(own[0], own[2]);
            M = fmaxf(M, dpp<0xB1>(M)); M = fmaxf(M, dpp<0x4E>(M));
            const float wa = ex2(own[0] - M), wb = ex2(own[2] - M);
            float o[9];
#pragma unroll
            for (int k = 0; k < 4; ++k) { o[k] = fmaf(wa, a0[k], wb * c0[k]); o[4 + k] = fmaf(wa, a1[k], wb * c1[k]); }
            o[8] = fmaf(wa, own[1], wb * own[3]);
#pragma unroll
            for (int k = 0; k < 9; ++k) { o[k] += dpp<0xB1>(o[k]); o[k] += dpp<0x4E>(o[k]); }
            const float inv = rcp(o[8]);
            R = (v4u){pk_bf(o[0] * inv, o[1] * inv), pk_bf(o[2] * inv, o[3] * inv), pk_bf(o[4] * inv, o[5] * inv), pk_bf(o[6] * inv, o[7] * inv)};
        }
        {
            f32x4 d[4];
#pragma unroll
            for (int nt = 0; nt < 4; ++nt) d[nt] = (f32x4){0.f, 0.f, 0.f, 0.f};
#pragma unroll
            for (int c = 0; c < 4; ++c) {
                lds_wait_ge(L, F_PROG, c == 0 ? 16 : 20 + 4 * c);
                const v4u B = c == 0 ? dpp4<0x150>(R) : c == 1 ? dpp4<0x154>(R) : c == 2 ? dpp4<0x158>(R) : dpp4<0x15C>(R);
                const int row = 32 * c + 4 * g + q4;
#pragma unroll
                for (int nt = 0; nt < 4; ++nt) {
                    const int pc = ((8 * w + 2 * nt + (p4 >> 1)) ^ ((row & 7) << 1)) * 16 + 8 * (p4 & 1);
                    const v4u wf = trfrag(L + L_RING + row * 1024 + pc, L + L_RING + (row + 16) * 1024 + pc);
                    d[nt] = mfma16(wf, B, d[nt]);
                }
            }
            STAMP(11);
            const f32x4 e = q4 == 0 ? d[0] : q4 == 1 ? d[1] : q4 == 2 ? d[2] : d[3];
            const float v0 = (p4 & 1) ? e[2] : e[0], v1 = (p4 & 1) ? e[3] : e[1];
            if (p4 < 2) st_sc1(rs, WS_S + (unsigned)((((b_att * 32 + 4 * w + q4) * 32 + hkv * 4 + s_att) * 16 + 4 * g + 2 * p4) * 8), (v4u){__float_as_uint(v0), tagA, __float_as_uint(v1), tagB});
        }
        STAMP(12);
    } else {
        const unsigned long long ep = __hip_atomic_load((GAS unsigned long long*)(a.ws + WS_EPOCH), __ATOMIC_RELAXED, __HIP_MEMORY_SCOPE_AGENT);
        const v2u b1v = *(const GAS v2u*)(a.b1 + 8 * fg + 4 * (g & 1));
        const unsigned xfin = *(const GAS unsigned*)(a.x + b_fin * 512 + 16 * cg_fin + 2 * (lane & 7));
        const unsigned b2v = *(const GAS unsigned*)(a.b2 + 16 * cg_fin + 2 * (lane & 7));
        if (lane < 16) LDS16(L_H + (lane >> 1) * 272 + 224 + (lane & 1) * 16) = (v4u){0u, 0u, 0u, 0u};
        __builtin_amdgcn_sched_barrier(0);
        __builtin_amdgcn_s_barrier();
        __builtin_amdgcn_sched_barrier(0);
        CFENCE();
        unsigned qo[2], ho[4]; int hdst[4];
#pragma unroll
        for (int k = 0; k < 2; ++k) qo[k] = WS_Q + (unsigned)((b_att * 2048 + hkv * 256 + 2 * (lane + 64 * k)) * 8);
#pragma unroll
        for (int k = 0; k < 4; ++k) { int p = lane + 64 * k; if (p >= 224) p -= 64; const int b = p / 28, i = p - 28 * b; ho[k] = WS_H + (unsigned)((b * 896 + 56 * rgf + 2 * i) * 8); hdst[k] = L_H + b * 272 + i * 8; }
        const unsigned tagA = (unsigned)ep + 1u, tagB = (unsigned)(ep >> 32) + 1u;
        if (lane == 0) { LDS4(F_TAG) = tagA; LDS4(F_TAG + 4) = tagB; }
        STAMP(1);
        lds_wait_ge(L, F_RED, 8);
        STAMP(3);
        {
            const f32x2 st = *(LAS f32x2*)(L + L_ST + (n16 & 7) * 8);
            f32x4 dq = {0.f, 0.f, 0.f, 0.f}, dh = {0.f, 0.f, 0.f, 0.f};
#pragma unroll
            for (int w = 0; w < 8; ++w) dq += *(LAS f32x4*)(L + L_WQ + w * 2048 + lane * 16);
#pragma unroll
            for (int w = 0; w < 8; ++w) dh += *(LAS f32x4*)(L + L_WQ + w * 2048 + 1024 + lane * 16);
#pragma unroll
            for (int k = 0; k < 4; ++k) dq[k] = fmaf(-st[0], dpp<0x158>(dq[k]), dq[k]) * st[1];
            if (n16 < 8) st_sc1(rs, WS_Q + (unsigned)((n16 * 2048 + 8 * cg + 2 * g) * 8), (v4u){pk_bf(dq[0], dq[1]), tagA, pk_bf(dq[2], dq[3]), tagB});
            CFENCE();
            if (lane < 3) VLDS4(F_FREE + 4 * (13 + lane)) = 1u;
            float hv[4];
#pragma unroll
            for (int k = 0; k < 4; ++k) {
                const float bb = (k & 1) ? bf_hi(b1v[k >> 1]) : bf_lo(b1v[k >> 1]);
                const float t = rbf(rbf(fmaf(-st[0], dpp<0x158>(dh[k]), dh[k]) * st[1]) + bb);
                const float u = t * fmaf(t * t, -2.f * LOG2E * 0.7978845608028654f * 0.044715f, -2.f * LOG2E * 0.7978845608028654f);
                hv[k] = t * rcp(1.f + ex2(u));
            }
            if (n16 < 8 && g < 2 && r < 28) st_sc1(rs, WS_H + (unsigned)((n16 * 896 + 4 * fg + 2 * g) * 8), (v4u){pk_bf(hv[0], hv[1]), tagA, pk_bf(hv[2], hv[3]), tagB});
        }
        STAMP(4);
        {
            v4u Q[2], H[4];
#pragma clang loop unroll(disable)
            for (unsigned spin = 0;; ++spin) {
                CFENCE();
#pragma unroll
                for (int k = 0; k < 2; ++k) Q[k] = ld_sc1(rs, qo[k]);
                if (__all(TAGOK(Q[0]) && TAGOK(Q[1])) || spin > SPIN_CAP) break;
            }
#pragma unroll
            for (int k = 0; k < 2; ++k) { const int p = lane + 64 * k; LDS8(L_Q + (p >> 5) * 272 + (p & 31) * 8) = (v2u){Q[k].x, Q[k].z}; }
            CFENCE();
            if (lane == 0) VLDS4(F_Q) = 1u;
            STAMP(5);
#pragma clang loop unroll(disable)
            for (unsigned spin = 0;; ++spin) {
                CFENCE();
#pragma unroll
                for (int k = 0; k < 4; ++k) H[k] = ld_sc1(rs, ho[k]);
                if (__all(TAGOK(H[0]) && TAGOK(H[1]) && TAGOK(H[2]) && TAGOK(H[3])) || spin > SPIN_CAP) break;
            }
#pragma unroll
            for (int k = 0; k < 4; ++k) LDS8(hdst[k]) = (v2u){H[k].x, H[k].z};
        }
        STAMP(6);
        {
            f32x4 d0 = {0.f, 0.f, 0.f, 0.f}, d1 = {0.f, 0.f, 0.f, 0.f};
#pragma unroll
            for (int ks = 0; ks < 4; ++ks) {
                const int row = 32 * ks + 8 * g + q4;
                const v4u hf = LDS16(L_H + (n16 & 7) * 272 + (32 * ks + 8 * g) * 2);
                const v4u f0 = trfrag(L + L_W2 + row * 64 + 8 * p4, L + L_W2 + (row + 4) * 64 + 8 * p4);
                const v4u f1 = trfrag(L + L_W2 + row * 64 + 32 + 8 * p4, L + L_W2 + (row + 4) * 64 + 32 + 8 * p4);
                d0 = mfma16(f0, hf, d0); d1 = mfma16(f1, hf, d1);
            }
            const int b = n16 & 7, cp = n16 >> 3, f = b * 32 + 2 * cf;
            st_sc1(rs, WS_Y + (unsigned)(((f * 16 + rgf) * 16 + 4 * g + 2 * cp) * 8), (v4u){__float_as_uint(cp ? d0[2] : d0[0]), tagA, __float_as_uint(cp ? d0[3] : d0[1]), tagB});
            st_sc1(rs, WS_Y + (unsigned)((((f + 1) * 16 + rgf) * 16 + 4 * g + 2 * cp) * 8), (v4u){__float_as_uint(cp ? d1[2] : d1[0]), tagA, __float_as_uint(cp ? d1[3] : d1[1]), tagB});
        }
        STAMP(7);
        float ga, gb;
        {
            v4u Y[2];
#pragma clang loop unroll(disable)
            for (unsigned spin = 0;; ++spin) {
                CFENCE();
#pragma unroll
                for (int k = 0; k < 2; ++k) Y[k] = ld_sc1(rs, WS_Y + (unsigned)(j * 2048 + (lane + 64 * k) * 16));
                if (__all(TAGOK(Y[0]) && TAGOK(Y[1])) || spin > SPIN_CAP) break;
            }
            float y0 = __uint_as_float(Y[0].x) + __uint_as_float(Y[1].x), y1 = __uint_as_float(Y[0].z) + __uint_as_float(Y[1].z);
            y0 += dpp<0x128>(y0); y1 += dpp<0x128>(y1);
            y0 = x32_sum(x16_sum(y0)); y1 = x32_sum(x16_sum(y1));
            const float ya = rbf(rbf(y0) + bf_lo(b2v)), yb = rbf(rbf(y1) + bf_hi(b2v));
            ga = rbf(ya * rcp(1.f + ex2(-LOG2E * ya))); gb = rbf(yb * rcp(1.f + ex2(-LOG2E * yb)));
        }
        STAMP(8);
        lds_wait_ge(L, F_O, 8);
        STAMP(11);
        {
            v4u S[4];
#pragma clang loop unroll(disable)
            for (unsigned spin = 0;; ++spin) {
                CFENCE();
#pragma unroll
                for (int k = 0; k < 4; ++k) S[k] = ld_sc1(rs, WS_S + (unsigned)(j * 4096 + (lane + 64 * k) * 16));
                if (__all(TAGOK(S[0]) && TAGOK(S[1]) && TAGOK(S[2]) && TAGOK(S[3])) || spin > SPIN_CAP) break;
            }
            STAMP(12);
            float s0 = (__uint_as_float(S[0].x) + __uint_as_float(S[1].x)) + (__uint_as_float(S[2].x) + __uint_as_float(S[3].x));
            float s1 = (__uint_as_float(S[0].z) + __uint_as_float(S[1].z)) + (__uint_as_float(S[2].z) + __uint_as_float(S[3].z));
            s0 += dpp<0x128>(s0); s1 += dpp<0x128>(s1);
            s0 = x32_sum(x16_sum(s0)); s1 = x32_sum(x16_sum(s1));
            const float oa = bf_lo(xfin) + rbf(rbf(s0) * ga), ob = bf_hi(xfin) + rbf(rbf(s1) * gb);
            if (lane < 8) *(GAS unsigned*)(a.out + b_fin * 512 + 16 * cg_fin + 2 * lane) = pk_bf(oa, ob);
            if (j == 0 && lane == 0) __hip_atomic_store((GAS unsigned long long*)(a.ws + WS_EPOCH), ((unsigned long long)tagB << 32) | tagA, __ATOMIC_RELAXED, __HIP_MEMORY_SCOPE_AGENT);
        }
        STAMP(13);
    }
}

extern "C" void kernel_launch(void* const* d_in, const int* in_sizes, int n_in,
                              void* d_out, int out_size, void* d_ws, size_t ws_size,
                              hipStream_t stream) {
    Args a;
    a.x = (const u16*)d_in[0]; a.kc = (const u16*)d_in[1]; a.vc = (const u16*)d_in[2]; a.wq = (const u16*)d_in[3]; a.wo = (const u16*)d_in[4];
    a.w1 = (const u16*)d_in[6]; a.b1 = (const u16*)d_in[7]; a.w2 = (const u16*)d_in[8]; a.b2 = (const u16*)d_in[9];
    a.out = (u16*)d_out; a.ws = (unsigned char*)d_ws;
    (void)hipFuncSetAttribute((const void*)fused_decode, hipFuncAttributeMaxDynamicSharedMemorySize, L_BYTES);
    hipLaunchKernelGGL(fused_decode, dim3(NWG), dim3(NTHR), L_BYTES, stream, a);
}
```

```cpp
#include <hip/hip_runtime.h>
#include <hip/hip_bf16.h>
#include <stdint.h>

typedef unsigned short u16;
typedef unsigned v4u __attribute__((ext_vector_type(4)));
typedef unsigned v2u __attribute__((ext_vector_type(2)));
typedef float f32x4 __attribute__((ext_vector_type(4)));
typedef float f32x2 __attribute__((ext_vector_type(2)));
typedef __bf16 bf16x8 __attribute__((ext_vector_type(8)));
typedef __bf16 bf16x2 __attribute__((ext_vector_type(2)));
typedef short v4i16 __attribute__((ext_vector_type(4)));
#define GAS __attribute__((address_space(1)))
#define LAS __attribute__((address_space(3)))

#define STAMPS 0
#define WIN 6
#define CODE_KB 8

constexpr int NWG = 256, NWAVE = 11, NTHR = NWAVE * 64;
constexpr unsigned WS_EPOCH = 0;
constexpr unsigned WS_Q = 4096;
constexpr unsigned WS_H = WS_Q + 8 * 2048 * 8;
constexpr unsigned WS_SC = WS_H + 8 * 896 * 8;
constexpr unsigned WS_S = WS_SC + 64 * 32 * 512;
constexpr unsigned WS_Y = WS_S + 256 * 32 * 16 * 8;
constexpr unsigned WS_END = WS_Y + 256 * 16 * 16 * 8;
constexpr unsigned WS_STAMP = 8u << 20;
constexpr int L_RING = 0;
constexpr int L_WQ = 14 * 8192;
constexpr int L_XN = 131072;
constexpr int L_W1 = L_XN + 8192;
constexpr int L_W2 = L_W1 + 8192;
constexpr int L_Q = L_W2 + 8192;
constexpr int L_H = L_Q + 4 * 272;
constexpr int L_OP = L_H + 8 * 272;
constexpr int L_ML = L_OP + 4096;
constexpr int L_FLG = L_ML + 256;
constexpr int F_PROG = L_FLG, F_XN = L_FLG + 4, F_Q = L_FLG + 8, F_O = L_FLG + 12;
constexpr int F_TAG = L_FLG + 16;
constexpr int F_FREE = L_FLG + 32;
constexpr int L_BYTES = F_FREE + 64;
static_assert(L_BYTES <= 163840, "LDS");
constexpr unsigned SPIN_CAP = 1u << 18;
constexpr int P_X = 1, P_WQ0 = 2, P_WQ1 = 3, P_W1 = 4, P_K = 5, P_WA = 6, P_W2 = 21, P_WB = 22, P_V = 23, P_ALL = 37;

struct Args {
    const u16* x; const u16* kc; const u16* vc; const u16* wq; const u16* wo;
    const u16* w1; const u16* b1; const u16* w2; const u16* b2; u16* out; unsigned char* ws;
};

__device__ __forceinline__ float bf_lo(unsigned u) { return __uint_as_float(u << 16); }
__device__ __forceinline__ float bf_hi(unsigned u) { return __uint_as_float(u & 0xffff0000u); }
__device__ __forceinline__ unsigned pk_bf(float lo, float hi) { bf16x2 v = {(__bf16)lo, (__bf16)hi}; return __builtin_bit_cast(unsigned, v); }
__device__ __forceinline__ float rbf(float f) { return (float)(__bf16)f; }
__device__ __forceinline__ float rcp(float f) { return __builtin_amdgcn_rcpf(f); }
__device__ __forceinline__ float ex2(float f) { return __builtin_amdgcn_exp2f(f); }
__device__ __forceinline__ f32x4 mfma16(v4u a, v4u b, f32x4 c) {
    return __builtin_amdgcn_mfma_f32_16x16x32_bf16(__builtin_bit_cast(bf16x8, a), __builtin_bit_cast(bf16x8, b), c, 0, 0, 0);
}
__device__ __forceinline__ v2u trread(LAS unsigned char* p) { return __builtin_bit_cast(v2u, __builtin_amdgcn_ds_read_tr16_b64_v4i16((LAS v4i16*)p)); }
__device__ __forceinline__ v4u trfrag(LAS unsigned char* p0, LAS unsigned char* p1) { v2u a = trread(p0), b = trread(p1); return (v4u){a.x, a.y, b.x, b.y}; }
template <int CTRL> __device__ __forceinline__ unsigned dppu(unsigned x) { return (unsigned)__builtin_amdgcn_mov_dpp((int)x, CTRL, 0xf, 0xf, true); }
template <int CTRL> __device__ __forceinline__ float dpp(float x) { return __uint_as_float(dppu<CTRL>(__float_as_uint(x))); }
template <int CTRL> __device__ __forceinline__ v4u dpp4(v4u x) { return (v4u){dppu<CTRL>(x.x), dppu<CTRL>(x.y), dppu<CTRL>(x.z), dppu<CTRL>(x.w)}; }
__device__ __forceinline__ float x16_sum(float x) { auto s = __builtin_amdgcn_permlane16_swap(__float_as_uint(x), __float_as_uint(x), false, false); return __uint_as_float(s[0]) + __uint_as_float(s[1]); }
__device__ __forceinline__ float x32_sum(float x) { auto s = __builtin_amdgcn_permlane32_swap(__float_as_uint(x), __float_as_uint(x), false, false); return __uint_as_float(s[0]) + __uint_as_float(s[1]); }
__device__ __forceinline__ float x16_max(float x) { auto s = __builtin_amdgcn_permlane16_swap(__float_as_uint(x), __float_as_uint(x), false, false); return fmaxf(__uint_as_float(s[0]), __uint_as_float(s[1])); }
__device__ __forceinline__ float x32_max(float x) { auto s = __builtin_amdgcn_permlane32_swap(__float_as_uint(x), __float_as_uint(x), false, false); return fmaxf(__uint_as_float(s[0]), __uint_as_float(s[1])); }
__device__ __forceinline__ float wave_sum(float x) {
    x += dpp<0xB1>(x); x += dpp<0x4E>(x); x += dpp<0x141>(x); x += dpp<0x140>(x);
    return x32_sum(x16_sum(x));
}
__device__ __forceinline__ v4u ldg(const u16* p) { return *(const GAS v4u*)p; }
__device__ __forceinline__ v4u ld_sc1(__amdgpu_buffer_rsrc_t rs, unsigned off) { return __builtin_amdgcn_raw_buffer_load_b128(rs, off, 0, 16); }
__device__ __forceinline__ void st_sc1(__amdgpu_buffer_rsrc_t rs, unsigned off, v4u v) { __builtin_amdgcn_raw_buffer_store_b128(v, rs, off, 0, 16); }
#define CFENCE() asm volatile("" ::: "memory")
#define LDS16(off) (*(LAS v4u*)(L + (off)))
#define LDS8(off) (*(LAS v2u*)(L + (off)))
#define LDS4(off) (*(LAS unsigned*)(L + (off)))
#define VLDS4(off) (*(volatile LAS unsigned*)(L + (off)))
#define STAMP(i) do { } while (0)

template <int NT> __device__ __forceinline__ void glds4(const void* base, unsigned dst, unsigned v0, unsigned v1, unsigned v2, unsigned v3) {
    if (NT) asm volatile("s_mov_b32 m0, %1\n\ts_nop 0\n\tglobal_load_lds_dwordx4 %2, %0 nt\n\tglobal_load_lds_dwordx4 %3, %0 offset:1024 nt\n\t"
                         "global_load_lds_dwordx4 %4, %0 offset:2048 nt\n\tglobal_load_lds_dwordx4 %5, %0 offset:3072 nt"
                         : : "s"(base), "s"(dst), "v"(v0), "v"(v1), "v"(v2), "v"(v3) : "memory", "m0");
    else    asm volatile("s_mov_b32 m0, %1\n\ts_nop 0\n\tglobal_load_lds_dwordx4 %2, %0\n\tglobal_load_lds_dwordx4 %3, %0 offset:1024\n\t"
                         "global_load_lds_dwordx4 %4, %0 offset:2048\n\tglobal_load_lds_dwordx4 %5, %0 offset:3072"
                         : : "s"(base), "s"(dst), "v"(v0), "v"(v1), "v"(v2), "v"(v3) : "memory", "m0");
}
__device__ __forceinline__ void glds1(const void* base, unsigned dst, unsigned v0) {
    asm volatile("s_mov_b32 m0, %1\n\ts_nop 0\n\tglobal_load_lds_dwordx4 %2, %0" : : "s"(base), "s"(dst), "v"(v0) : "memory", "m0");
}
__device__ __forceinline__ void lds_wait_ge(LAS unsigned char* L, int off, unsigned need) {
#pragma clang loop unroll(disable)
    for (unsigned spin = 0; spin < SPIN_CAP; ++spin) {
        if ((unsigned)__builtin_amdgcn_readfirstlane(VLDS4(off)) >= need) break;
        __builtin_amdgcn_s_sleep(1);
    }
    CFENCE();
}
__device__ __forceinline__ void lds_arrive(LAS unsigned char* L, int off, int lane) {
    CFENCE();
    if (lane == 0) __hip_atomic_fetch_add((LAS unsigned*)(L + off), 1u, __ATOMIC_RELAXED, __HIP_MEMORY_SCOPE_WORKGROUP);
    CFENCE();
}
#define TAGOK(v) ((v).y == tagA && (v).w == tagB)
#define KEEP4(a, i) asm volatile("" : "+v"(a[i]), "+v"(a[i + 1]), "+v"(a[i + 2]), "+v"(a[i + 3]) : : "memory")

__global__ void __launch_bounds__(NTHR, 1) fused_decode(Args a) {
    extern __shared__ __attribute__((aligned(16))) unsigned char smem[];
    LAS unsigned char* L = (LAS unsigned char*)smem;
    unsigned long long pc;
    asm volatile("s_getpc_b64 %0" : "=s"(pc));
    const int tid = threadIdx.x, lane = tid & 63, wave = __builtin_amdgcn_readfirstlane(tid >> 6);
    const int j = blockIdx.x, xcd = j & 7, r = j >> 3;
    const int n16 = lane & 15, g = lane >> 4, q4 = n16 >> 2, p4 = lane & 3;
    const __amdgpu_buffer_rsrc_t rs = __builtin_amdgcn_make_buffer_rsrc(a.ws, 0, WS_END, 0x00020000);
    const int hkv = xcd, b_att = r >> 2, s_att = r & 3;
    const int cg = xcd * 32 + r;
    const int fg = xcd * 28 + (r < 28 ? r : 27);
    const int rgf = xcd * 2 + (r >> 4), cf = r & 15;
    const int b_fin = j >> 5, cg_fin = j & 31;
    constexpr float LOG2E = 1.4426950408889634f;
    STAMP(0);

    if (__builtin_expect(wave == 0, 1)) {
        __builtin_amdgcn_s_setprio(3);
        const unsigned lb = (unsigned)(size_t)L;
        const unsigned l16 = (unsigned)lane * 16u;
        {
            const unsigned char* cb = (const unsigned char*)(pc & ~255ull);
            glds1(cb + (r % CODE_KB) * 1024, lb + L_OP, l16);
            glds1(cb + ((r + CODE_KB / 2) % CODE_KB) * 1024, lb + L_OP + 1024, l16);
        }
        if (lane < 24) LDS4(L_FLG + lane * 4) = 0u;
        {
            const unsigned char* xb = (const unsigned char*)a.x;
            glds4<0>(xb, lb + L_XN, l16, l16, l16, l16);
            glds4<0>(xb + 4096, lb + L_XN + 4096, l16, l16, l16, l16);
            const unsigned q0 = (unsigned)((lane >> 1) * 8192 + (lane & 1) * 16);
            const unsigned char* qb = (const unsigned char*)a.wq + 32 * cg;
#pragma unroll
            for (int k = 0; k < 4; ++k) glds4<0>(qb + k * 1048576, lb + L_WQ + k * 4096, q0, q0 + 261120u, q0 + 2u * 261120u, q0 + 3u * 261120u);
            const unsigned u0 = (unsigned)(lane * 3584);
            const unsigned char* ub = (const unsigned char*)a.w1 + 16 * fg;
#pragma unroll
            for (int k = 0; k < 2; ++k) glds4<0>(ub + k * 917504, lb + L_W1 + k * 4096, u0, u0 + 228352u, u0 + 2u * 228352u, u0 + 3u * 228352u);
        }
        const size_t kvrow = (size_t)(b_att * 8 + hkv) * 4096 + 3072;
        const unsigned char* kb = (const unsigned char*)a.kc + (kvrow + 256 * s_att) * 256;
        const unsigned char* vb = (const unsigned char*)a.vc + kvrow * 256 + 64 * s_att;
        const unsigned char* wb = (const unsigned char*)a.wo + (size_t)(hkv * 512 + 32 * s_att) * 1024;
        unsigned ko[8], wo[8];
#pragma unroll
        for (int i = 0; i < 8; ++i) { ko[i] = ((unsigned)(g * 256 + ((n16 ^ g) << 4)) ^ ((i & 3) << 6)) + (i >> 2) * 4096; wo[i] = (l16 ^ (i << 5)) + (i >> 2) * 4096; }
        KEEP4(ko, 0); KEEP4(ko, 4); KEEP4(wo, 0); KEEP4(wo, 4);
        const unsigned need = lane == 8 || lane == 9 ? 8u : 1u;
        const int foff = F_FREE + 4 * n16;
        int c = 4;
#define UNIT_DONE() do { ++c; asm volatile("s_waitcnt vmcnt(%0)" : : "n"(8 * (WIN - 1)) : "memory"); if (c - (WIN - 1) > drained) VLDS4(F_PROG) = (unsigned)(c - (WIN - 1)); } while (0)
#define UNIT_DONE_A() do { if (c + 1 >= WIN) { if (c + 1 == WIN) { __builtin_amdgcn_s_barrier(); STAMP(1); } UNIT_DONE(); } else ++c; } while (0)
#define NEED_FREE(fl, req) do { if (((unsigned)__ballot((fl) >= need) & (req)) != (req)) { \
            asm volatile("s_waitcnt vmcnt(0)" ::: "memory"); VLDS4(F_PROG) = (unsigned)c; \
            _Pragma("clang loop unroll(disable)") for (unsigned sp = 0; sp < SPIN_CAP; ++sp) { if (((unsigned)__ballot(VLDS4(foff) >= need) & (req)) == (req)) break; __builtin_amdgcn_s_sleep(1); } \
            drained = c; } } while (0)
        int drained = 0;
        asm volatile("s_waitcnt lgkmcnt(0)" ::: "memory");
#pragma clang loop unroll(disable)
        for (int n = 0; n < 8; ++n) {
            if (n == 7) { const unsigned fl = VLDS4(foff); NEED_FREE(fl, 1u << 10); }
            glds4<1>(kb, lb + n * 16384, ko[0], ko[1], ko[2], ko[3]);
            glds4<1>(kb, lb + n * 16384 + 4096, ko[4], ko[5], ko[6], ko[7]);
            kb += 8192;
            UNIT_DONE_A();
            glds4<0>(wb, lb + n * 16384 + 8192, wo[0], wo[1], wo[2], wo[3]);
            glds4<0>(wb, lb + n * 16384 + 12288, wo[4], wo[5], wo[6], wo[7]);
            wb += (n & 3) == 3 ? 131072 - 3 * 8192 : 8192;
            UNIT_DONE_A();
        }
        STAMP(2);
        unsigned fl = VLDS4(foff);
        {
            const unsigned t0 = (unsigned)((lane >> 2) * 1024 + p4 * 16);
            const unsigned char* tb = (const unsigned char*)a.w2 + (size_t)(112 * rgf) * 1024 + 64 * cf;
            glds4<0>(tb, lb + L_W2, t0, t0 + 15360u, t0 + 2u * 15360u, t0 + 3u * 15360u);
            glds4<0>(tb + 65536, lb + L_W2 + 4096, t0, t0 + 15360u, t0 + 2u * 15360u, t0 + 2u * 15360u - 1024u);
            UNIT_DONE();
        }
#pragma unroll
        for (int i = 0; i < 8; ++i) ko[i] = (unsigned)((lane >> 2) * 256 + ((p4 ^ ((g & 1) << 1)) << 4)) + i * 4096 - (i & 3) * 1024;
        KEEP4(ko, 0); KEEP4(ko, 4);
#pragma clang loop unroll(disable)
        for (int n = 0; n < 8; ++n) {
            NEED_FREE(fl, (1u << n) | (256u << (n >> 2)));
            glds4<0>(wb, lb + n * 16384 + 8192, wo[0], wo[1], wo[2], wo[3]);
            glds4<0>(wb, lb + n * 16384 + 12288, wo[4], wo[5], wo[6], wo[7]);
            wb += (n & 3) == 3 ? 131072 - 3 * 8192 : 8192;
            UNIT_DONE();
            glds4<1>(vb, lb + n * 16384, ko[0], ko[1], ko[2], ko[3]);
            glds4<1>(vb, lb + n * 16384 + 4096, ko[4], ko[5], ko[6], ko[7]);
            vb += 32768;
            UNIT_DONE();
            fl = VLDS4(foff);
        }
        STAMP(4);
#pragma unroll
        for (int k = WIN - 2; k >= 0; --k) {
            asm volatile("s_waitcnt vmcnt(%0)" : : "n"(8 * k) : "memory");
            if (P_ALL - k > drained) VLDS4(F_PROG) = P_ALL - k;
        }
        STAMP(5);
    } else if (wave >= 3) {
        const int w = wave - 3;
        __builtin_amdgcn_s_barrier();
        CFENCE();
        STAMP(1);
        lds_wait_ge(L, F_PROG, P_X);
        {
            const v4u xv = LDS16(L_XN + w * 1024 + lane * 16);
            float v[8];
#pragma unroll
            for (int k = 0; k < 4; ++k) { v[2 * k] = bf_lo(xv[k]); v[2 * k + 1] = bf_hi(xv[k]); }
            const float s = ((v[0] + v[1]) + (v[2] + v[3])) + ((v[4] + v[5]) + (v[6] + v[7]));
            const float mean = wave_sum(s) * (1.f / 512.f);
            float q = 0.f;
#pragma unroll
            for (int k = 0; k < 8; ++k) { const float d = v[k] - mean; q = fmaf(d, d, q); }
            const float var = rbf(wave_sum(q) * (1.f / 512.f));
            const float mb = rbf(mean);
            const float den = rbf(sqrtf(rbf(var + 1e-5f)));
            const float rc = rcp(den);
            float o[8];
#pragma unroll
            for (int k = 0; k < 8; ++k) { const float t = rbf(v[k] - mb); const float q0 = t * rc; const float e = fmaf(-q0, den, t); o[k] = fmaf(e, rc, q0); }
            const int xo = L_XN + w * 1024 + (((lane & ~3) + 2 * (lane & 1)) ^ (4 * (w & 3))) * 16 + (lane & 2) * 4;
            LDS8(xo) = (v2u){pk_bf(o[0], o[1]), pk_bf(o[2], o[3])};
            LDS8(xo + 16) = (v2u){pk_bf(o[4], o[5]), pk_bf(o[6], o[7])};
        }
        lds_arrive(L, F_XN, lane);
        STAMP(2);
        lds_wait_ge(L, F_PROG, P_K + 2 * w);
        v4u kf[8], wf[16];
#pragma unroll
        for (int i = 0; i < 8; ++i) kf[i] = LDS16(L_RING + w * 16384 + (16 * (i >> 2) + n16) * 256 + (((4 * (i & 3) + g) ^ n16) << 4));
        asm volatile("s_waitcnt lgkmcnt(0)" ::: "memory");
        KEEP4(kf, 0); KEEP4(kf, 4);
        if (lane == 0) VLDS4(F_FREE + 4 * w) = 1u;
        STAMP(3);
        const int wrow = L_RING + 8192 + (g >> 1) * 16384 + (4 * (g & 1) + q4) * 1024 + 8 * (p4 & 1);
        const int wsw = (4 * (g & 1) + q4) << 1;
#define WO_GROUP(c) do { _Pragma("unroll") for (int nt = 0; nt < 4; ++nt) { const int o_ = wrow + ((c) & 1) * 65536 + (((8 * w + 2 * nt + (p4 >> 1)) ^ wsw) << 4); \
            wf[4 * (c) + nt] = trfrag(L + o_, L + o_ + 32768); } asm volatile("s_waitcnt lgkmcnt(0)" ::: "memory"); KEEP4(wf, 4 * (c)); } while (0)
        lds_wait_ge(L, F_PROG, P_WA + 6);
        WO_GROUP(0);
        lds_arrive(L, F_FREE + 32, lane);
        STAMP(4);
        const unsigned sco = WS_SC + (unsigned)((((b_att * 8 + hkv) * 32 + s_att * 8 + w) * 16 + (n16 & 3) * 4 + g) * 32 + q4 * 16);
        lds_wait_ge(L, F_Q, 1);
        STAMP(5);
        const unsigned tagA = LDS4(F_TAG), tagB = LDS4(F_TAG + 4);
        {
            v4u qf[4];
#pragma unroll
            for (int t = 0; t < 4; ++t) qf[t] = LDS16(L_Q + (n16 & 3) * 272 + (32 * t + 8 * g) * 2);
            f32x4 s0 = {0.f, 0.f, 0.f, 0.f}, s1 = {0.f, 0.f, 0.f, 0.f};
#pragma unroll
            for (int t = 0; t < 4; ++t) { s0 = mfma16(kf[t], qf[t], s0); s1 = mfma16(kf[4 + t], qf[t], s1); }
            float sc[4];
#pragma unroll
            for (int k = 0; k < 4; ++k) sc[k] = rbf((q4 & 1) ? s1[k] : s0[k]) * 0.08838834764831845f;
            if (q4 < 2) st_sc1(rs, sco, (v4u){pk_bf(sc[0], sc[1]), tagA, pk_bf(sc[2], sc[3]), tagB});
        }
        STAMP(6);
        lds_wait_ge(L, F_PROG, P_WA + 14);
        WO_GROUP(1);
        lds_arrive(L, F_FREE + 36, lane);
        STAMP(7);
        v4u P; float ml, l;
        {
            const unsigned so = WS_SC + (unsigned)((((b_att * 8 + hkv) * 32 + 4 * w + q4) * 16 + (n16 & 3) * 4 + g) * 32);
            v4u A0, A1;
#pragma clang loop unroll(disable)
            for (unsigned spin = 0;; ++spin) {
                CFENCE();
                A0 = ld_sc1(rs, so); A1 = ld_sc1(rs, so + 16);
                if (__all(TAGOK(A0) && TAGOK(A1)) || spin > SPIN_CAP) break;
            }
            const float sc[8] = {bf_lo(A0.x), bf_hi(A0.x), bf_lo(A0.z), bf_hi(A0.z), bf_lo(A1.x), bf_hi(A1.x), bf_lo(A1.z), bf_hi(A1.z)};
            float m = fmaxf(fmaxf(fmaxf(sc[0], sc[1]), fmaxf(sc[2], sc[3])), fmaxf(fmaxf(sc[4], sc[5]), fmaxf(sc[6], sc[7])));
            m = fmaxf(m, dpp<0x124>(m)); m = fmaxf(m, dpp<0x128>(m));
            m = x32_max(x16_max(m));
            ml = m * LOG2E;
            float p[8];
#pragma unroll
            for (int k = 0; k < 8; ++k) p[k] = ex2(fmaf(sc[k], LOG2E, -ml));
            P = (v4u){pk_bf(p[0], p[1]), pk_bf(p[2], p[3]), pk_bf(p[4], p[5]), pk_bf(p[6], p[7])};
            l = ((p[0] + p[1]) + (p[2] + p[3])) + ((p[4] + p[5]) + (p[6] + p[7]));
            l += dpp<0x124>(l); l += dpp<0x128>(l);
            l = x32_sum(x16_sum(l));
            if (n16 < 4 && g == 0) *(LAS f32x2*)(L + L_ML + n16 * 64 + w * 8) = (f32x2){ml, l};
        }
        const v4u P1 = dpp4<0x12C>(P), P2 = dpp4<0x128>(P), P3 = dpp4<0x124>(P);
        STAMP(8);
        if (w >= 4) { lds_wait_ge(L, F_PROG, P_WB + 6); WO_GROUP(2); }
        if (w == 7) { lds_wait_ge(L, F_PROG, P_WB + 14); WO_GROUP(3); }
        lds_wait_ge(L, F_PROG, P_V + 2 * w);
        {
            v4u vf[8];
#pragma unroll
            for (int i = 0; i < 8; ++i) {
                const int row = 32 * (i >> 1) + 4 * g + q4, pcs = ((2 * (i & 1) + (p4 >> 1)) ^ ((g & 1) << 1)) * 16 + 8 * (p4 & 1);
                vf[i] = trfrag(L + L_RING + w * 16384 + row * 64 + pcs, L + L_RING + w * 16384 + (row + 16) * 64 + pcs);
            }
            f32x4 d0 = {0.f, 0.f, 0.f, 0.f}, d1 = {0.f, 0.f, 0.f, 0.f};
            d0 = mfma16(vf[0], P, d0); d1 = mfma16(vf[1], P, d1);
            d0 = mfma16(vf[2], P1, d0); d1 = mfma16(vf[3], P1, d1);
            d0 = mfma16(vf[4], P2, d0); d1 = mfma16(vf[5], P2, d1);
            d0 = mfma16(vf[6], P3, d0); d1 = mfma16(vf[7], P3, d1);
            if (n16 < 4) {
                *(LAS f32x4*)(L + L_OP + w * 512 + n16 * 128 + g * 16) = d0;
                *(LAS f32x4*)(L + L_OP + w * 512 + n16 * 128 + 64 + g * 16) = d1;
            }
        }
        lds_arrive(L, F_O, lane);
        STAMP(9);
        if (w < 4) { lds_wait_ge(L, F_PROG, P_WB + 6); WO_GROUP(2); }
        if (w < 7) { lds_wait_ge(L, F_PROG, P_WB + 14); WO_GROUP(3); }
        const unsigned sto = WS_S + (unsigned)((((b_att * 32 + 4 * w + q4) * 32 + hkv * 4 + s_att) * 16 + 4 * g + 2 * p4) * 8);
        lds_wait_ge(L, F_O, 8);
        STAMP(10);
        v4u R;
        {
            const f32x4 own = *(LAS f32x4*)(L + L_ML + q4 * 64 + p4 * 16);
            const f32x4 a0 = *(LAS f32x4*)(L + L_OP + (2 * p4) * 512 + q4 * 128 + g * 16), a1 = *(LAS f32x4*)(L + L_OP + (2 * p4) * 512 + q4 * 128 + 64 + g * 16);
            const f32x4 c0 = *(LAS f32x4*)(L + L_OP + (2 * p4 + 1) * 512 + q4 * 128 + g * 16), c1 = *(LAS f32x4*)(L + L_OP + (2 * p4 + 1) * 512 + q4 * 128 + 64 + g * 16);
            float M = fmaxf(own[0], own[2]);
            M = fmaxf(M, dpp<0xB1>(M)); M = fmaxf(M, dpp<0x4E>(M));
            const float wa = ex2(own[0] - M), wb = ex2(own[2] - M);
            float o[9];
#pragma unroll
            for (int k = 0; k < 4; ++k) { o[k] = fmaf(wa, a0[k], wb * c0[k]); o[4 + k] = fmaf(wa, a1[k], wb * c1[k]); }
            o[8] = fmaf(wa, own[1], wb * own[3]);
#pragma unroll
            for (int k = 0; k < 9; ++k) { o[k] += dpp<0xB1>(o[k]); o[k] += dpp<0x4E>(o[k]); }
            const float inv = rcp(o[8]);
            R = (v4u){pk_bf(o[0] * inv, o[1] * inv), pk_bf(o[2] * inv, o[3] * inv), pk_bf(o[4] * inv, o[5] * inv), pk_bf(o[6] * inv, o[7] * inv)};
        }
        {
            f32x4 d[4];
#pragma unroll
            for (int nt = 0; nt < 4; ++nt) d[nt] = (f32x4){0.f, 0.f, 0.f, 0.f};
#pragma unroll
            for (int c = 0; c < 4; ++c) {
                const v4u B = c == 0 ? dpp4<0x150>(R) : c == 1 ? dpp4<0x154>(R) : c == 2 ? dpp4<0x158>(R) : dpp4<0x15C>(R);
#pragma unroll
                for (int nt = 0; nt < 4; ++nt) d[nt] = mfma16(wf[4 * c + nt], B, d[nt]);
            }
            STAMP(11);
            const f32x4 e = q4 == 0 ? d[0] : q4 == 1 ? d[1] : q4 == 2 ? d[2] : d[3];
            const float v0 = (p4 & 1) ? e[2] : e[0], v1 = (p4 & 1) ? e[3] : e[1];
            if (p4 < 2) st_sc1(rs, sto, (v4u){__float_as_uint(v0), tagA, __float_as_uint(v1), tagB});
        }
        STAMP(12);
    } else {
        __builtin_amdgcn_s_setprio(2);
        const bool isq = wave == 1;
        unsigned long long ep = 0;
        if (isq) ep = __hip_atomic_load((GAS unsigned long long*)(a.ws + WS_EPOCH), __ATOMIC_RELAXED, __HIP_MEMORY_SCOPE_AGENT);
        const v2u b1v = *(const GAS v2u*)(a.b1 + 8 * fg + 4 * (g & 1));
        const unsigned xfin = *(const GAS unsigned*)(a.x + b_fin * 512 + 16 * cg_fin + 2 * (lane & 7));
        const unsigned b2v = *(const GAS unsigned*)(a.b2 + 16 * cg_fin + 2 * (lane & 7));
        __builtin_amdgcn_sched_barrier(0);
        __builtin_amdgcn_s_barrier();
        __builtin_amdgcn_sched_barrier(0);
        CFENCE();
        if (isq) {
            if (lane == 0) LDS8(F_TAG) = (v2u){(unsigned)ep + 1u, (unsigned)(ep >> 32) + 1u};
            lds_arrive(L, F_XN, lane);
        } else if (lane < 16) LDS16(L_H + (lane >> 1) * 272 + 224 + (lane & 1) * 16) = (v4u){0u, 0u, 0u, 0u};
        STAMP(1);
        lds_wait_ge(L, F_XN, 9);
        const unsigned tagA = LDS4(F_TAG), tagB = LDS4(F_TAG + 4);
        v4u xf[16];
#pragma unroll
        for (int ks = 0; ks < 16; ++ks) xf[ks] = LDS16(L_XN + (n16 & 7) * 1024 + (((4 * ks + g) ^ (4 * (n16 & 3))) << 4));
        STAMP(2);
        const int rstr = isq ? 32 : 16;
        const int ao = (isq ? L_WQ + 8 * p4 : L_W1 + 8 * (p4 & 1)) + (4 * g + q4) * rstr;
        f32x4 e0 = {0.f, 0.f, 0.f, 0.f}, e1 = {0.f, 0.f, 0.f, 0.f};
#pragma unroll
        for (int hh = 0; hh < 2; ++hh) {
            lds_wait_ge(L, F_PROG, isq ? P_WQ0 + hh : P_W1);
#pragma unroll
            for (int ks = 8 * hh; ks < 8 * hh + 8; ks += 2) {
                e0 = mfma16(trfrag(L + ao + 32 * ks * rstr, L + ao + (32 * ks + 16) * rstr), xf[ks], e0);
                e1 = mfma16(trfrag(L + ao + (32 * ks + 32) * rstr, L + ao + (32 * ks + 48) * rstr), xf[ks + 1], e1);
            }
        }
        e0 += e1;
        STAMP(3);
        if (isq) {
            if (n16 < 8) st_sc1(rs, WS_Q + (unsigned)((n16 * 2048 + 8 * cg + 2 * g) * 8), (v4u){pk_bf(e0[0], e0[1]), tagA, pk_bf(e0[2], e0[3]), tagB});
            CFENCE();
            if (lane == 0) VLDS4(F_FREE + 40) = 1u;
            STAMP(4);
            const unsigned qo = WS_Q + (unsigned)((b_att * 2048 + hkv * 256 + 2 * lane) * 8);
            v4u Q[2];
#pragma clang loop unroll(disable)
            for (unsigned spin = 0;; ++spin) {
                CFENCE();
#pragma unroll
                for (int k = 0; k < 2; ++k) Q[k] = ld_sc1(rs, qo + 1024 * k);
                if (__all(TAGOK(Q[0]) && TAGOK(Q[1])) || spin > SPIN_CAP) break;
            }
#pragma unroll
            for (int k = 0; k < 2; ++k) { const int p = lane + 64 * k; LDS8(L_Q + (p >> 5) * 272 + (p & 31) * 8) = (v2u){Q[k].x, Q[k].z}; }
            CFENCE();
            if (lane == 0) VLDS4(F_Q) = 1u;
            STAMP(5);
        } else {
            {
                float hv[4];
#pragma unroll
                for (int k = 0; k < 4; ++k) {
                    const float bb = (k & 1) ? bf_hi(b1v[k >> 1]) : bf_lo(b1v[k >> 1]);
                    const float t = rbf(rbf(e0[k]) + bb);
                    const float u = t * fmaf(t * t, -2.f * LOG2E * 0.7978845608028654f * 0.044715f, -2.f * LOG2E * 0.7978845608028654f);
                    hv[k] = t * rcp(1.f + ex2(u));
                }
                if (n16 < 8 && g < 2 && r < 28) st_sc1(rs, WS_H + (unsigned)((n16 * 896 + 4 * fg + 2 * g) * 8), (v4u){pk_bf(hv[0], hv[1]), tagA, pk_bf(hv[2], hv[3]), tagB});
            }
            STAMP(4);
            {
                unsigned ho[4]; int hdst[4]; v4u H[4];
#pragma unroll
                for (int k = 0; k < 4; ++k) { int p = lane + 64 * k; if (p >= 224) p -= 64; const int b = p / 28, i = p - 28 * b; ho[k] = WS_H + (unsigned)((b * 896 + 56 * rgf + 2 * i) * 8); hdst[k] = L_H + b * 272 + i * 8; }
#pragma clang loop unroll(disable)
                for (unsigned spin = 0;; ++spin) {
                    CFENCE();
#pragma unroll
                    for (int k = 0; k < 4; ++k) H[k] = ld_sc1(rs, ho[k]);
                    if (__all(TAGOK(H[0]) && TAGOK(H[1]) && TAGOK(H[2]) && TAGOK(H[3])) || spin > SPIN_CAP) break;
                }
#pragma unroll
                for (int k = 0; k < 4; ++k) LDS8(hdst[k]) = (v2u){H[k].x, H[k].z};
            }
            STAMP(6);
            lds_wait_ge(L, F_PROG, P_W2);
            {
                f32x4 d0 = {0.f, 0.f, 0.f, 0.f}, d1 = {0.f, 0.f, 0.f, 0.f};
#pragma unroll
                for (int ks = 0; ks < 4; ++ks) {
                    const int row = 32 * ks + 8 * g + q4;
                    const v4u hf = LDS16(L_H + (n16 & 7) * 272 + (32 * ks + 8 * g) * 2);
                    const v4u f0 = trfrag(L + L_W2 + row * 64 + 8 * p4, L + L_W2 + (row + 4) * 64 + 8 * p4);
                    const v4u f1 = trfrag(L + L_W2 + row * 64 + 32 + 8 * p4, L + L_W2 + (row + 4) * 64 + 32 + 8 * p4);
                    d0 = mfma16(f0, hf, d0); d1 = mfma16(f1, hf, d1);
                }
                const int b = n16 & 7, cp = n16 >> 3, f = b * 32 + 2 * cf;
                st_sc1(rs, WS_Y + (unsigned)(((f * 16 + rgf) * 16 + 4 * g + 2 * cp) * 8), (v4u){__float_as_uint(cp ? d0[2] : d0[0]), tagA, __float_as_uint(cp ? d0[3] : d0[1]), tagB});
                st_sc1(rs, WS_Y + (unsigned)((((f + 1) * 16 + rgf) * 16 + 4 * g + 2 * cp) * 8), (v4u){__float_as_uint(cp ? d1[2] : d1[0]), tagA, __float_as_uint(cp ? d1[3] : d1[1]), tagB});
            }
            STAMP(7);
            float ga, gb;
            {
                v4u Y[2];
#pragma clang loop unroll(disable)
                for (unsigned spin = 0;; ++spin) {
                    CFENCE();
#pragma unroll
                    for (int k = 0; k < 2; ++k) Y[k] = ld_sc1(rs, WS_Y + (unsigned)(j * 2048 + (lane + 64 * k) * 16));
                    if (__all(TAGOK(Y[0]) && TAGOK(Y[1])) || spin > SPIN_CAP) break;
                }
                float y0 = __uint_as_float(Y[0].x) + __uint_as_float(Y[1].x), y1 = __uint_as_float(Y[0].z) + __uint_as_float(Y[1].z);
                y0 += dpp<0x128>(y0); y1 += dpp<0x128>(y1);
                y0 = x32_sum(x16_sum(y0)); y1 = x32_sum(x16_sum(y1));
                const float ya = rbf(rbf(y0) + bf_lo(b2v)), yb = rbf(rbf(y1) + bf_hi(b2v));
                ga = rbf(ya * rcp(1.f + ex2(-LOG2E * ya))); gb = rbf(yb * rcp(1.f + ex2(-LOG2E * yb)));
            }
            STAMP(8);
            lds_wait_ge(L, F_O, 8);
            STAMP(11);
            {
                v4u S[4];
#pragma clang loop unroll(disable)
                for (unsigned spin = 0;; ++spin) {
                    CFENCE();
#pragma unroll
                    for (int k = 0; k < 4; ++k) S[k] = ld_sc1(rs, WS_S + (unsigned)(j * 4096 + (lane + 64 * k) * 16));
                    if (__all(TAGOK(S[0]) && TAGOK(S[1]) && TAGOK(S[2]) && TAGOK(S[3])) || spin > SPIN_CAP) break;
                }
                STAMP(12);
                float s0 = (__uint_as_float(S[0].x) + __uint_as_float(S[1].x)) + (__uint_as_float(S[2].x) + __uint_as_float(S[3].x));
                float s1 = (__uint_as_float(S[0].z) + __uint_as_float(S[1].z)) + (__uint_as_float(S[2].z) + __uint_as_float(S[3].z));
                s0 += dpp<0x128>(s0); s1 += dpp<0x128>(s1);
                s0 = x32_sum(x16_sum(s0)); s1 = x32_sum(x16_sum(s1));
                const float oa = bf_lo(xfin) + rbf(rbf(s0) * ga), ob = bf_hi(xfin) + rbf(rbf(s1) * gb);
                if (lane < 8) *(GAS unsigned*)(a.out + b_fin * 512 + 16 * cg_fin + 2 * lane) = pk_bf(oa, ob);
                if (j == 0 && lane == 0) __hip_atomic_store((GAS unsigned long long*)(a.ws + WS_EPOCH), ((unsigned long long)tagB << 32) | tagA, __ATOMIC_RELAXED, __HIP_MEMORY_SCOPE_AGENT);
            }
            STAMP(13);
        }
    }
}

extern "C" void kernel_launch(void* const* d_in, const int* in_sizes, int n_in,
                              void* d_out, int out_size, void* d_ws, size_t ws_size,
                              hipStream_t stream) {
    Args a;
    a.x = (const u16*)d_in[0]; a.kc = (const u16*)d_in[1]; a.vc = (const u16*)d_in[2]; a.wq = (const u16*)d_in[3]; a.wo = (const u16*)d_in[4];
    a.w1 = (const u16*)d_in[6]; a.b1 = (const u16*)d_in[7]; a.w2 = (const u16*)d_in[8]; a.b2 = (const u16*)d_in[9];
    a.out = (u16*)d_out; a.ws = (unsigned char*)d_ws;
    (void)hipFuncSetAttribute((const void*)fused_decode, hipFuncAttributeMaxDynamicSharedMemorySize, L_BYTES);
    hipLaunchKernelGGL(fused_decode, dim3(NWG), dim3(NTHR), L_BYTES, stream, a);
}
```

```cpp
#include <hip/hip_runtime.h>
#include <hip/hip_bf16.h>
#include <stdint.h>

typedef unsigned short u16;
typedef unsigned v4u __attribute__((ext_vector_type(4)));
typedef unsigned v2u __attribute__((ext_vector_type(2)));
typedef float f32x4 __attribute__((ext_vector_type(4)));
typedef float f32x2 __attribute__((ext_vector_type(2)));
typedef __bf16 bf16x8 __attribute__((ext_vector_type(8)));
typedef __bf16 bf16x2 __attribute__((ext_vector_type(2)));
typedef short v4i16 __attribute__((ext_vector_type(4)));
#define GAS __attribute__((address_space(1)))
#define LAS __attribute__((address_space(3)))

#define STAMPS 0
#define WIN 6
#define CODE_KB 8
#define W1_GATE 14
#define W2_GATE 20
#define WO_GATE 26

constexpr int NWG = 256, NWAVE = 11, NTHR = NWAVE * 64;
constexpr unsigned WS_EPOCH = 0;
constexpr unsigned WS_Q = 4096;
constexpr unsigned WS_H = WS_Q + 8 * 2048 * 8;
constexpr unsigned WS_SC = WS_H + 8 * 896 * 8;
constexpr unsigned WS_O = WS_SC + 64 * 32 * 512;
constexpr unsigned WS_S = WS_O + 32 * 4096;
constexpr unsigned WS_Y = WS_S + 256 * 32 * 16 * 8;
constexpr unsigned WS_END = WS_Y + 256 * 16 * 16 * 8;
constexpr unsigned WS_STAMP = 8u << 20;
constexpr int L_K = 0;
constexpr int L_V = 65536;
constexpr int L_XN = L_V + 7 * 8192;
constexpr int L_WQ = 131072;
constexpr int L_W1 = L_WQ + 16384;
constexpr int L_Q = L_W1 + 8192;
constexpr int L_H = L_Q + 4 * 272;
constexpr int L_OP = L_H + 8 * 272;
constexpr int L_ML = L_OP + 4096;
constexpr int L_FLG = L_ML + 256;
constexpr int F_PROG = L_FLG, F_XN = L_FLG + 4, F_Q = L_FLG + 8, F_O = L_FLG + 12;
constexpr int F_TAG = L_FLG + 16;
constexpr int F_XF = L_FLG + 24, F_S = L_FLG + 28;
constexpr int L_BYTES = L_FLG + 64;
static_assert(L_BYTES <= 163840, "LDS");
constexpr unsigned SPIN_CAP = 1u << 18;

struct Args {
    const u16* x; const u16* kc; const u16* vc; const u16* wq; const u16* wo;
    const u16* w1; const u16* b1; const u16* w2; const u16* b2; u16* out; unsigned char* ws;
};

__device__ __forceinline__ float bf_lo(unsigned u) { return __uint_as_float(u << 16); }
__device__ __forceinline__ float bf_hi(unsigned u) { return __uint_as_float(u & 0xffff0000u); }
__device__ __forceinline__ unsigned pk_bf(float lo, float hi) { bf16x2 v = {(__bf16)lo, (__bf16)hi}; return __builtin_bit_cast(unsigned, v); }
__device__ __forceinline__ float rbf(float f) { return (float)(__bf16)f; }
__device__ __forceinline__ float rcp(float f) { return __builtin_amdgcn_rcpf(f); }
__device__ __forceinline__ float ex2(float f) { return __builtin_amdgcn_exp2f(f); }
__device__ __forceinline__ f32x4 mfma16(v4u a, v4u b, f32x4 c) {
    return __builtin_amdgcn_mfma_f32_16x16x32_bf16(__builtin_bit_cast(bf16x8, a), __builtin_bit_cast(bf16x8, b), c, 0, 0, 0);
}
__device__ __forceinline__ v2u trread(LAS unsigned char* p) { return __builtin_bit_cast(v2u, __builtin_amdgcn_ds_read_tr16_b64_v4i16((LAS v4i16*)p)); }
__device__ __forceinline__ v4u trfrag(LAS unsigned char* p0, LAS unsigned char* p1) { v2u a = trread(p0), b = trread(p1); return (v4u){a.x, a.y, b.x, b.y}; }
template <int CTRL> __device__ __forceinline__ unsigned dppu(unsigned x) { return (unsigned)__builtin_amdgcn_mov_dpp((int)x, CTRL, 0xf, 0xf, true); }
template <int CTRL> __device__ __forceinline__ float dpp(float x) { return __uint_as_float(dppu<CTRL>(__float_as_uint(x))); }
template <int CTRL> __device__ __forceinline__ v4u dpp4(v4u x) { return (v4u){dppu<CTRL>(x.x), dppu<CTRL>(x.y), dppu<CTRL>(x.z), dppu<CTRL>(x.w)}; }
__device__ __forceinline__ float x16_sum(float x) { auto s = __builtin_amdgcn_permlane16_swap(__float_as_uint(x), __float_as_uint(x), false, false); return __uint_as_float(s[0]) + __uint_as_float(s[1]); }
__device__ __forceinline__ float x32_sum(float x) { auto s = __builtin_amdgcn_permlane32_swap(__float_as_uint(x), __float_as_uint(x), false, false); return __uint_as_float(s[0]) + __uint_as_float(s[1]); }
__device__ __forceinline__ float x16_max(float x) { auto s = __builtin_amdgcn_permlane16_swap(__float_as_uint(x), __float_as_uint(x), false, false); return fmaxf(__uint_as_float(s[0]), __uint_as_float(s[1])); }
__device__ __forceinline__ float x32_max(float x) { auto s = __builtin_amdgcn_permlane32_swap(__float_as_uint(x), __float_as_uint(x), false, false); return fmaxf(__uint_as_float(s[0]), __uint_as_float(s[1])); }
__device__ __forceinline__ float wave_sum(float x) {
    x += dpp<0xB1>(x); x += dpp<0x4E>(x); x += dpp<0x141>(x); x += dpp<0x140>(x);
    return x32_sum(x16_sum(x));
}
__device__ __forceinline__ v4u ldg(const u16* p) { return *(const GAS v4u*)p; }
__device__ __forceinline__ v4u ld_sc1(__amdgpu_buffer_rsrc_t rs, unsigned off) { return __builtin_amdgcn_raw_buffer_load_b128(rs, off, 0, 16); }
__device__ __forceinline__ void st_sc1(__amdgpu_buffer_rsrc_t rs, unsigned off, v4u v) { __builtin_amdgcn_raw_buffer_store_b128(v, rs, off, 0, 16); }
#define CFENCE() asm volatile("" ::: "memory")
#define LDS16(off) (*(LAS v4u*)(L + (off)))
#define LDS8(off) (*(LAS v2u*)(L + (off)))
#define LDS4(off) (*(LAS unsigned*)(L + (off)))
#define VLDS4(off) (*(volatile LAS unsigned*)(L + (off)))
#define STAMP(i) do { } while (0)

template <int NT> __device__ __forceinline__ void glds4(const void* base, unsigned dst, unsigned v0, unsigned v1, unsigned v2, unsigned v3) {
    if (NT) asm volatile("s_mov_b32 m0, %1\n\ts_nop 0\n\tglobal_load_lds_dwordx4 %2, %0 nt\n\tglobal_load_lds_dwordx4 %3, %0 offset:1024 nt\n\t"
                         "global_load_lds_dwordx4 %4, %0 offset:2048 nt\n\tglobal_load_lds_dwordx4 %5, %0 offset:3072 nt"
                         : : "s"(base), "s"(dst), "v"(v0), "v"(v1), "v"(v2), "v"(v3) : "memory", "m0");
    else    asm volatile("s_mov_b32 m0, %1\n\ts_nop 0\n\tglobal_load_lds_dwordx4 %2, %0\n\tglobal_load_lds_dwordx4 %3, %0 offset:1024\n\t"
                         "global_load_lds_dwordx4 %4, %0 offset:2048\n\tglobal_load_lds_dwordx4 %5, %0 offset:3072"
                         : : "s"(base), "s"(dst), "v"(v0), "v"(v1), "v"(v2), "v"(v3) : "memory", "m0");
}
__device__ __forceinline__ void glds1(const void* base, unsigned dst, unsigned v0) {
    asm volatile("s_mov_b32 m0, %1\n\ts_nop 0\n\tglobal_load_lds_dwordx4 %2, %0" : : "s"(base), "s"(dst), "v"(v0) : "memory", "m0");
}
template <int SLEEP> __device__ __forceinline__ void lds_wait_ge(LAS unsigned char* L, int off, unsigned need) {
#pragma clang loop unroll(disable)
    for (unsigned spin = 0; spin < SPIN_CAP; ++spin) {
        if ((unsigned)__builtin_amdgcn_readfirstlane(VLDS4(off)) >= need) break;
        if (SLEEP) __builtin_amdgcn_s_sleep(SLEEP);
    }
    CFENCE();
}
__device__ __forceinline__ void lds_arrive(LAS unsigned char* L, int off, int lane) {
    CFENCE();
    if (lane == 0) __hip_atomic_fetch_add((LAS unsigned*)(L + off), 1u, __ATOMIC_RELAXED, __HIP_MEMORY_SCOPE_WORKGROUP);
    CFENCE();
}
template <int CTRL, int BANK> __device__ __forceinline__ unsigned dppsel(unsigned old, unsigned x) { return (unsigned)__builtin_amdgcn_update_dpp((int)old, (int)x, CTRL, 0xf, BANK, false); }
#define TAGOK(v) ((v).y == tagA && (v).w == tagB)
#define KEEP4(a, i) asm volatile("" : "+v"(a[i]), "+v"(a[i + 1]), "+v"(a[i + 2]), "+v"(a[i + 3]) : : "memory")
#define SB() __builtin_amdgcn_sched_barrier(0)
#define VMWAIT(n) asm volatile("s_waitcnt vmcnt(%0)" : : "n"(n) : "memory")

__global__ void __launch_bounds__(NTHR, 1) fused_decode(Args a) {
    extern __shared__ __attribute__((aligned(16))) unsigned char smem[];
    LAS unsigned char* L = (LAS unsigned char*)smem;
    unsigned long long pc;
    asm volatile("s_getpc_b64 %0" : "=s"(pc));
    const int tid = threadIdx.x, lane = tid & 63, wave = __builtin_amdgcn_readfirstlane(tid >> 6);
    const int j = blockIdx.x, xcd = j & 7, r = j >> 3;
    const int n16 = lane & 15, g = lane >> 4, q4 = n16 >> 2, p4 = lane & 3;
    const unsigned lb = (unsigned)(size_t)L;
    const unsigned l16 = (unsigned)lane * 16u;
    const int hkv = xcd, b_att = r >> 2, s_att = r & 3;
    const int cg = xcd * 32 + r;
    const int fg = xcd * 28 + (r < 28 ? r : 27);
    const int rgf = xcd * 2 + (r >> 4), cf = r & 15;
    const int b_fin = j >> 5, cg_fin = j & 31;
    const int rotk = r & 7, rotv = (b_att + xcd) & 7;
    constexpr float LOG2E = 1.4426950408889634f;
    v4u xv = {0u, 0u, 0u, 0u};
    unsigned long long ep = 0;
    if (wave == 2) {
        const unsigned char* cb = (const unsigned char*)(pc & ~255ull);
        glds1(cb + (r % CODE_KB) * 1024, lb + L_W1, l16);
        glds1(cb + ((r + CODE_KB / 2) % CODE_KB) * 1024, lb + L_W1 + 1024, l16);
        if (lane < 16) LDS4(L_FLG + lane * 4) = 0u;
        if (lane < 16) LDS16(L_H + (lane >> 1) * 272 + 224 + (lane & 1) * 16) = (v4u){0u, 0u, 0u, 0u};
        asm volatile("s_waitcnt lgkmcnt(0)" ::: "memory");
    }
    STAMP(0);
    if (wave >= 3) {
        xv = *(const GAS v4u*)((const unsigned char*)a.x + (wave - 3) * 1024 + l16);
    } else if (wave == 1) {
        ep = __hip_atomic_load((GAS unsigned long long*)(a.ws + WS_EPOCH), __ATOMIC_RELAXED, __HIP_MEMORY_SCOPE_AGENT);
        const unsigned q0 = (unsigned)((lane >> 1) * 8192 + (lane & 1) * 16);
        const unsigned char* qb = (const unsigned char*)a.wq + 32 * cg;
#pragma unroll
        for (int k = 0; k < 4; ++k) glds4<0>(qb + k * 1048576, lb + L_WQ + k * 4096, q0, q0 + 261120u, q0 + 2u * 261120u, q0 + 3u * 261120u);
    }
    const __amdgpu_buffer_rsrc_t rs = __builtin_amdgcn_make_buffer_rsrc(a.ws, 0, WS_END, 0x00020000);

    if (wave == 0) {
        __builtin_amdgcn_s_setprio(3);
        const size_t kvrow = (size_t)(b_att * 8 + hkv) * 4096 + 3072;
        const unsigned char* kb = (const unsigned char*)a.kc + (kvrow + 256 * s_att) * 256;
        const unsigned char* vb = (const unsigned char*)a.vc + kvrow * 256 + 64 * s_att;
        unsigned o[4];
#pragma unroll
        for (int i = 0; i < 4; ++i) o[i] = (unsigned)(g * 256 + ((n16 ^ g ^ (4 * i)) << 4));
        KEEP4(o, 0);
#pragma clang loop unroll(disable)
        for (int c = 0; c < 16; ++c) {
            glds4<1>(kb + ((c + 2 * rotk) & 15) * 4096, lb + L_K + c * 4096, o[0], o[1], o[2], o[3]);
            if (c >= WIN - 1) {
                if (c == WIN - 1) { STAMP(7); __builtin_amdgcn_s_barrier(); STAMP(1); }
                VMWAIT(4 * (WIN - 1));
                VLDS4(F_PROG) = (unsigned)(c - (WIN - 2));
            }
        }
        STAMP(2);
#pragma unroll
        for (int i = 0; i < 4; ++i) o[i] = (unsigned)((lane >> 2) * 256 + ((p4 ^ ((g & 1) << 1)) << 4) + i * 3072);
        KEEP4(o, 0);
#pragma clang loop unroll(disable)
        for (int c = 0; c < 16; ++c) {
            if (c == 14) lds_wait_ge<1>(L, F_XF, 2);
            glds4<1>(vb + ((c + 2 * rotv) & 15) * 16384, lb + L_V + c * 4096, o[0], o[1], o[2], o[3]);
            VMWAIT(4 * (WIN - 1));
            VLDS4(F_PROG) = (unsigned)(c + 16 - (WIN - 2));
        }
        STAMP(4);
#pragma unroll
        for (int k = WIN - 2; k >= 0; --k) {
            VMWAIT(4 * k);
            VLDS4(F_PROG) = 32 - k;
        }
        STAMP(5);
    } else if (wave >= 3) {
        const int w = wave - 3;
        STAMP(13);
        __builtin_amdgcn_s_barrier();
        CFENCE();
        STAMP(1);
        {
            float v[8];
#pragma unroll
            for (int k = 0; k < 4; ++k) { v[2 * k] = bf_lo(xv[k]); v[2 * k + 1] = bf_hi(xv[k]); }
            const float s = ((v[0] + v[1]) + (v[2] + v[3])) + ((v[4] + v[5]) + (v[6] + v[7]));
            const float mean = wave_sum(s) * (1.f / 512.f);
            float q = 0.f;
#pragma unroll
            for (int k = 0; k < 8; ++k) { const float d = v[k] - mean; q = fmaf(d, d, q); }
            const float var = rbf(wave_sum(q) * (1.f / 512.f));
            const float mb = rbf(mean);
            const float den = rbf(sqrtf(rbf(var + 1e-5f)));
            const float rc = rcp(den);
            float o[8];
#pragma unroll
            for (int k = 0; k < 8; ++k) { const float t = rbf(v[k] - mb); const float q0 = t * rc; const float e = fmaf(-q0, den, t); o[k] = fmaf(e, rc, q0); }
            const int xo = L_XN + w * 1024 + (((lane & ~3) + 2 * (lane & 1)) ^ (4 * (w & 3))) * 16 + (lane & 2) * 4;
            LDS8(xo) = (v2u){pk_bf(o[0], o[1]), pk_bf(o[2], o[3])};
            LDS8(xo + 16) = (v2u){pk_bf(o[4], o[5]), pk_bf(o[6], o[7])};
        }
        lds_arrive(L, F_XN, lane);
        STAMP(2);
        v4u kf[8];
        const int kblk = s_att * 8 + ((w + rotk) & 7);
        const int vblk = (w + rotv) & 7;
        const unsigned sco = WS_SC + (unsigned)((((b_att * 8 + hkv) * 32 + kblk) * 16 + (n16 & 3) * 4 + g) * 32 + q4 * 16);
        const unsigned so = WS_SC + (unsigned)((((b_att * 8 + hkv) * 32 + 4 * vblk + q4) * 16 + (n16 & 3) * 4 + g) * 32);
        lds_wait_ge<1>(L, F_PROG, 2 * w + 2);
#pragma unroll
        for (int i = 0; i < 8; ++i) kf[i] = LDS16(L_K + w * 8192 + (16 * (i >> 2) + n16) * 256 + (((4 * (i & 3) + g) ^ n16) << 4));
        KEEP4(kf, 0); KEEP4(kf, 4);
        STAMP(3);
        lds_wait_ge<0>(L, F_Q, 1);
        STAMP(5);
        v4u qf[4];
#pragma unroll
        for (int t = 0; t < 4; ++t) qf[t] = LDS16(L_Q + (n16 & 3) * 272 + (32 * t + 8 * g) * 2);
        const unsigned tagA = LDS4(F_TAG), tagB = LDS4(F_TAG + 4);
        SB();
        {
            f32x4 s0 = {0.f, 0.f, 0.f, 0.f}, s1 = {0.f, 0.f, 0.f, 0.f};
#pragma unroll
            for (int t = 0; t < 4; ++t) { s0 = mfma16(kf[t], qf[t], s0); s1 = mfma16(kf[4 + t], qf[t], s1); }
            float sc[4];
#pragma unroll
            for (int k = 0; k < 4; ++k) sc[k] = rbf((q4 & 1) ? s1[k] : s0[k]) * 0.08838834764831845f;
            if (q4 < 2) st_sc1(rs, sco, (v4u){pk_bf(sc[0], sc[1]), tagA, pk_bf(sc[2], sc[3]), tagB});
        }
        STAMP(6);
        v4u P; float ml, l;
        {
            v4u A0, A1;
#pragma clang loop unroll(disable)
            for (unsigned spin = 0;; ++spin) {
                CFENCE();
                A0 = ld_sc1(rs, so); A1 = ld_sc1(rs, so + 16);
                if (__all(TAGOK(A0) && TAGOK(A1)) || spin > SPIN_CAP) break;
            }
            STAMP(7);
            const float sc[8] = {bf_lo(A0.x), bf_hi(A0.x), bf_lo(A0.z), bf_hi(A0.z), bf_lo(A1.x), bf_hi(A1.x), bf_lo(A1.z), bf_hi(A1.z)};
            float m = fmaxf(fmaxf(fmaxf(sc[0], sc[1]), fmaxf(sc[2], sc[3])), fmaxf(fmaxf(sc[4], sc[5]), fmaxf(sc[6], sc[7])));
            m = fmaxf(m, dpp<0x124>(m)); m = fmaxf(m, dpp<0x128>(m));
            m = x32_max(x16_max(m));
            ml = m * LOG2E;
            float p[8];
#pragma unroll
            for (int k = 0; k < 8; ++k) p[k] = ex2(fmaf(sc[k], LOG2E, -ml));
            P = (v4u){pk_bf(p[0], p[1]), pk_bf(p[2], p[3]), pk_bf(p[4], p[5]), pk_bf(p[6], p[7])};
            l = ((p[0] + p[1]) + (p[2] + p[3])) + ((p[4] + p[5]) + (p[6] + p[7]));
            l += dpp<0x124>(l); l += dpp<0x128>(l);
            l = x32_sum(x16_sum(l));
            if (n16 < 4 && g == 0) *(LAS f32x2*)(L + L_ML + n16 * 64 + w * 8) = (f32x2){ml, l};
        }
        v4u P1 = dpp4<0x12C>(P), P2 = dpp4<0x128>(P), P3 = dpp4<0x124>(P);
        asm volatile("" : "+v"(P), "+v"(P1), "+v"(P2), "+v"(P3) : : "memory");
        STAMP(8);
        lds_wait_ge<0>(L, F_PROG, 16 + 2 * w + 2);
        STAMP(4);
        {
            v4u vf[8];
#pragma unroll
            for (int i = 0; i < 8; ++i) {
                const int row = 32 * (i >> 1) + 4 * g + q4, pcs = ((2 * (i & 1) + (p4 >> 1)) ^ ((g & 1) << 1)) * 16 + 8 * (p4 & 1);
                vf[i] = trfrag(L + L_V + w * 8192 + row * 64 + pcs, L + L_V + w * 8192 + (row + 16) * 64 + pcs);
            }
            SB();
            f32x4 d0 = {0.f, 0.f, 0.f, 0.f}, d1 = {0.f, 0.f, 0.f, 0.f};
            d0 = mfma16(vf[0], P, d0); d1 = mfma16(vf[1], P, d1);
            d0 = mfma16(vf[2], P1, d0); d1 = mfma16(vf[3], P1, d1);
            d0 = mfma16(vf[4], P2, d0); d1 = mfma16(vf[5], P2, d1);
            d0 = mfma16(vf[6], P3, d0); d1 = mfma16(vf[7], P3, d1);
            if (n16 < 4) {
                *(LAS f32x4*)(L + L_OP + w * 512 + n16 * 128 + g * 16) = d0;
                *(LAS f32x4*)(L + L_OP + w * 512 + n16 * 128 + 64 + g * 16) = d1;
            }
        }
        lds_arrive(L, F_O, lane);
        STAMP(9);
    } else if (wave == 1) {
        __builtin_amdgcn_s_setprio(2);
        STAMP(9);
        __builtin_amdgcn_s_barrier();
        CFENCE();
        STAMP(1);
        const unsigned tagA = (unsigned)ep + 1u, tagB = (unsigned)(ep >> 32) + 1u;
        if (lane == 0) LDS8(F_TAG) = (v2u){tagA, tagB};
        lds_arrive(L, F_XN, lane);
        VMWAIT(0);
        STAMP(14);
        v4u af[16];
        {
            const int ao = L_WQ + 8 * p4 + (4 * g + q4) * 32;
#pragma unroll
            for (int ks = 0; ks < 16; ++ks) af[ks] = trfrag(L + ao + 1024 * ks, L + ao + 1024 * ks + 512);
            KEEP4(af, 0); KEEP4(af, 4); KEEP4(af, 8); KEEP4(af, 12);
        }
        STAMP(15);
        lds_wait_ge<0>(L, F_XN, 9);
        STAMP(10);
        {
            v4u xf[16];
#pragma unroll
            for (int ks = 0; ks < 16; ++ks) xf[ks] = LDS16(L_XN + (n16 & 7) * 1024 + (((4 * ks + g) ^ (4 * (n16 & 3))) << 4));
            SB();
            f32x4 e0 = {0.f, 0.f, 0.f, 0.f}, e1 = {0.f, 0.f, 0.f, 0.f};
#pragma unroll
            for (int ks = 0; ks < 16; ks += 2) { e0 = mfma16(af[ks], xf[ks], e0); e1 = mfma16(af[ks + 1], xf[ks + 1], e1); }
            e0 += e1;
            if (n16 < 8) st_sc1(rs, WS_Q + (unsigned)((n16 * 2048 + 8 * cg + 2 * g) * 8), (v4u){pk_bf(e0[0], e0[1]), tagA, pk_bf(e0[2], e0[3]), tagB});
        }
        lds_arrive(L, F_XF, lane);
        STAMP(4);
        {
            const unsigned qo = WS_Q + (unsigned)((b_att * 2048 + hkv * 256 + 2 * lane) * 8);
            v4u Q[2];
#pragma clang loop unroll(disable)
            for (unsigned spin = 0;; ++spin) {
                CFENCE();
#pragma unroll
                for (int k = 0; k < 2; ++k) Q[k] = ld_sc1(rs, qo + 1024 * k);
                if (__all(TAGOK(Q[0]) && TAGOK(Q[1])) || spin > SPIN_CAP) break;
            }
#pragma unroll
            for (int k = 0; k < 2; ++k) { const int p = lane + 64 * k; LDS8(L_Q + (p >> 5) * 272 + (p & 31) * 8) = (v2u){Q[k].x, Q[k].z}; }
            CFENCE();
            if (lane == 0) VLDS4(F_Q) = 1u;
        }
        STAMP(5);
        const int kg = hkv * 4 + s_att;
        const unsigned oo = WS_O + (unsigned)(kg * 4096 + (n16 & 7) * 512 + (n16 >> 3) * 256 + g * 16);
        const unsigned sto = WS_S + (unsigned)(((((n16 & 7) * 32 + 4 * b_att) * 32 + kg) * 16 + 4 * g + 2 * (n16 >> 3)) * 8);
        lds_wait_ge<2>(L, F_PROG, WO_GATE);
        {
            const unsigned w0 = (unsigned)((lane >> 3) * 1024 + (((lane & 7) ^ (((lane >> 4) & 3) << 1)) << 4));
            const unsigned char* wb = (const unsigned char*)a.wo + (size_t)(hkv * 512 + 32 * s_att) * 1024 + 128 * b_att;
#pragma unroll
            for (int c = 0; c < 4; ++c) glds4<1>(wb + c * 131072, lb + L_WQ + c * 4096, w0, w0 + 7168u, w0 + 2u * 7168u, w0 + 3u * 7168u);
        }
        STAMP(6);
        lds_wait_ge<0>(L, F_O, 8);
        STAMP(7);
        {
            const f32x4 own = *(LAS f32x4*)(L + L_ML + q4 * 64 + p4 * 16);
            const f32x4 a0 = *(LAS f32x4*)(L + L_OP + (2 * p4) * 512 + q4 * 128 + g * 16), a1 = *(LAS f32x4*)(L + L_OP + (2 * p4) * 512 + q4 * 128 + 64 + g * 16);
            const f32x4 c0 = *(LAS f32x4*)(L + L_OP + (2 * p4 + 1) * 512 + q4 * 128 + g * 16), c1 = *(LAS f32x4*)(L + L_OP + (2 * p4 + 1) * 512 + q4 * 128 + 64 + g * 16);
            SB();
            float M = fmaxf(own[0], own[2]);
            M = fmaxf(M, dpp<0xB1>(M)); M = fmaxf(M, dpp<0x4E>(M));
            const float wa = ex2(own[0] - M), wb = ex2(own[2] - M);
            float o[9];
#pragma unroll
            for (int k = 0; k < 4; ++k) { o[k] = fmaf(wa, a0[k], wb * c0[k]); o[4 + k] = fmaf(wa, a1[k], wb * c1[k]); }
            o[8] = fmaf(wa, own[1], wb * own[3]);
#pragma unroll
            for (int k = 0; k < 9; ++k) { o[k] += dpp<0xB1>(o[k]); o[k] += dpp<0x4E>(o[k]); }
            const float inv = rcp(o[8]);
            const int h = p4 & 1;
            const unsigned r0 = pk_bf((h ? o[4] : o[0]) * inv, (h ? o[5] : o[1]) * inv), r1 = pk_bf((h ? o[6] : o[2]) * inv, (h ? o[7] : o[3]) * inv);
            if (p4 < 2) st_sc1(rs, WS_O + (unsigned)(kg * 4096 + b_att * 512 + q4 * 128 + h * 64 + g * 16), (v4u){r0, tagA, r1, tagB});
        }
        STAMP(8);
        VMWAIT(1);
        {
            const int row = 4 * g + q4;
#pragma unroll
            for (int i = 0; i < 16; ++i) {
                const int o_ = L_WQ + (i >> 2) * 4096 + row * 128 + (((2 * (i & 3) + (p4 >> 1)) ^ (((row >> 1) & 3) << 1)) << 4) + 8 * (p4 & 1);
                af[i] = trfrag(L + o_, L + o_ + 2048);
            }
            KEEP4(af, 0); KEEP4(af, 4); KEEP4(af, 8); KEEP4(af, 12);
        }
        STAMP(11);
        {
            v4u F[4];
#pragma clang loop unroll(disable)
            for (unsigned spin = 0;; ++spin) {
                CFENCE();
#pragma unroll
                for (int k = 0; k < 4; ++k) F[k] = ld_sc1(rs, oo + 64 * k);
                if (__all(TAGOK(F[0]) && TAGOK(F[1]) && TAGOK(F[2]) && TAGOK(F[3])) || spin > SPIN_CAP) break;
            }
            STAMP(12);
            const v4u B0 = {F[0].x, F[0].z, F[1].x, F[1].z}, B1 = {F[2].x, F[2].z, F[3].x, F[3].z};
            const v4u B2 = dpp4<0x128>(B0), B3 = dpp4<0x128>(B1);
            f32x4 d[4];
#pragma unroll
            for (int nt = 0; nt < 4; ++nt) { d[nt] = (f32x4){0.f, 0.f, 0.f, 0.f}; d[nt] = mfma16(af[nt], B0, d[nt]); }
#pragma unroll
            for (int nt = 0; nt < 4; ++nt) d[nt] = mfma16(af[4 + nt], B1, d[nt]);
#pragma unroll
            for (int nt = 0; nt < 4; ++nt) d[nt] = mfma16(af[8 + nt], B2, d[nt]);
#pragma unroll
            for (int nt = 0; nt < 4; ++nt) {
                d[nt] = mfma16(af[12 + nt], B3, d[nt]);
                const unsigned v0 = dppsel<0x128, 0xC>(__float_as_uint(d[nt][0]), __float_as_uint(d[nt][2])), v1 = dppsel<0x128, 0xC>(__float_as_uint(d[nt][1]), __float_as_uint(d[nt][3]));
                st_sc1(rs, sto + nt * 4096, (v4u){v0, tagA, v1, tagB});
            }
        }
        CFENCE();
        if (lane == 0) VLDS4(F_S) = 1u;
        STAMP(13);
    } else {
        __builtin_amdgcn_s_setprio(2);
        const v2u b1v = *(const GAS v2u*)(a.b1 + 8 * fg + 4 * (g & 1));
        const unsigned xfin = *(const GAS unsigned*)(a.x + b_fin * 512 + 16 * cg_fin + 2 * (lane & 7));
        const unsigned b2v = *(const GAS unsigned*)(a.b2 + 16 * cg_fin + 2 * (lane & 7));
        SB();
        STAMP(9);
        __builtin_amdgcn_s_barrier();
        SB();
        CFENCE();
        STAMP(1);
        lds_wait_ge<1>(L, F_XN, 9);
        STAMP(10);
        const unsigned tagA = LDS4(F_TAG), tagB = LDS4(F_TAG + 4);
        v4u xf[16];
#pragma unroll
        for (int ks = 0; ks < 16; ++ks) xf[ks] = LDS16(L_XN + (n16 & 7) * 1024 + (((4 * ks + g) ^ (4 * (n16 & 3))) << 4));
        KEEP4(xf, 0); KEEP4(xf, 4); KEEP4(xf, 8); KEEP4(xf, 12);
        lds_arrive(L, F_XF, lane);
        STAMP(2);
        lds_wait_ge<2>(L, F_PROG, W1_GATE);
        {
            const unsigned u0 = (unsigned)(lane * 3584);
            const unsigned char* ub = (const unsigned char*)a.w1 + 16 * fg;
#pragma unroll
            for (int k = 0; k < 2; ++k) glds4<0>(ub + k * 917504, lb + L_W1 + k * 4096, u0, u0 + 228352u, u0 + 2u * 228352u, u0 + 3u * 228352u);
        }
        VMWAIT(0);
        STAMP(14);
        {
            const int ao = L_W1 + 8 * (p4 & 1) + (4 * g + q4) * 16;
            v4u af[16];
#pragma unroll
            for (int ks = 0; ks < 16; ++ks) af[ks] = trfrag(L + ao + 512 * ks, L + ao + 512 * ks + 256);
            SB();
            f32x4 e0 = {0.f, 0.f, 0.f, 0.f}, e1 = {0.f, 0.f, 0.f, 0.f};
#pragma unroll
            for (int ks = 0; ks < 16; ks += 2) { e0 = mfma16(af[ks], xf[ks], e0); e1 = mfma16(af[ks + 1], xf[ks + 1], e1); }
            e0 += e1;
            STAMP(3);
            float hv[4];
#pragma unroll
            for (int k = 0; k < 4; ++k) {
                const float bb = (k & 1) ? bf_hi(b1v[k >> 1]) : bf_lo(b1v[k >> 1]);
                const float t = rbf(rbf(e0[k]) + bb);
                const float u = t * fmaf(t * t, -2.f * LOG2E * 0.7978845608028654f * 0.044715f, -2.f * LOG2E * 0.7978845608028654f);
                hv[k] = t * rcp(1.f + ex2(u));
            }
            if (n16 < 8 && g < 2 && r < 28) st_sc1(rs, WS_H + (unsigned)((n16 * 896 + 4 * fg + 2 * g) * 8), (v4u){pk_bf(hv[0], hv[1]), tagA, pk_bf(hv[2], hv[3]), tagB});
        }
        STAMP(4);
        lds_wait_ge<2>(L, F_PROG, W2_GATE);
        {
            const unsigned t0 = (unsigned)((lane >> 2) * 1024 + p4 * 16);
            const unsigned char* tb = (const unsigned char*)a.w2 + (size_t)(112 * rgf) * 1024 + 64 * cf;
            glds4<0>(tb, lb + L_W1, t0, t0 + 15360u, t0 + 2u * 15360u, t0 + 3u * 15360u);
            glds4<0>(tb + 65536, lb + L_W1 + 4096, t0, t0 + 15360u, t0 + 2u * 15360u, t0 + 2u * 15360u - 1024u);
        }
        {
            unsigned ho[4]; int hdst[4]; v4u H[4];
#pragma unroll
            for (int k = 0; k < 4; ++k) { int p = lane + 64 * k; if (p >= 224) p -= 64; const int b = p / 28, i = p - 28 * b; ho[k] = WS_H + (unsigned)((b * 896 + 56 * rgf + 2 * i) * 8); hdst[k] = L_H + b * 272 + i * 8; }
#pragma clang loop unroll(disable)
            for (unsigned spin = 0;; ++spin) {
                CFENCE();
#pragma unroll
                for (int k = 0; k < 4; ++k) H[k] = ld_sc1(rs, ho[k]);
                if (__all(TAGOK(H[0]) && TAGOK(H[1]) && TAGOK(H[2]) && TAGOK(H[3])) || spin > SPIN_CAP) break;
            }
#pragma unroll
            for (int k = 0; k < 4; ++k) LDS8(hdst[k]) = (v2u){H[k].x, H[k].z};
        }
        VMWAIT(0);
        STAMP(6);
        {
            v4u hf[4], f0[4], f1[4];
#pragma unroll
            for (int ks = 0; ks < 4; ++ks) {
                const int row = 32 * ks + 8 * g + q4;
                hf[ks] = LDS16(L_H + (n16 & 7) * 272 + (32 * ks + 8 * g) * 2);
                f0[ks] = trfrag(L + L_W1 + row * 64 + 8 * p4, L + L_W1 + (row + 4) * 64 + 8 * p4);
                f1[ks] = trfrag(L + L_W1 + row * 64 + 32 + 8 * p4, L + L_W1 + (row + 4) * 64 + 32 + 8 * p4);
            }
            SB();
            f32x4 d0 = {0.f, 0.f, 0.f, 0.f}, d1 = {0.f, 0.f, 0.f, 0.f};
#pragma unroll
            for (int ks = 0; ks < 4; ++ks) { d0 = mfma16(f0[ks], hf[ks], d0); d1 = mfma16(f1[ks], hf[ks], d1); }
            const int b = n16 & 7, cp = n16 >> 3, f = b * 32 + 2 * cf;
            st_sc1(rs, WS_Y + (unsigned)(((f * 16 + rgf) * 16 + 4 * g + 2 * cp) * 8), (v4u){__float_as_uint(cp ? d0[2] : d0[0]), tagA, __float_as_uint(cp ? d0[3] : d0[1]), tagB});
            st_sc1(rs, WS_Y + (unsigned)((((f + 1) * 16 + rgf) * 16 + 4 * g + 2 * cp) * 8), (v4u){__float_as_uint(cp ? d1[2] : d1[0]), tagA, __float_as_uint(cp ? d1[3] : d1[1]), tagB});
        }
        STAMP(7);
        float ga, gb;
        {
            v4u Y[2];
#pragma clang loop unroll(disable)
            for (unsigned spin = 0;; ++spin) {
                CFENCE();
#pragma unroll
                for (int k = 0; k < 2; ++k) Y[k] = ld_sc1(rs, WS_Y + (unsigned)(j * 2048 + (lane + 64 * k) * 16));
                if (__all(TAGOK(Y[0]) && TAGOK(Y[1])) || spin > SPIN_CAP) break;
            }
            float y0 = __uint_as_float(Y[0].x) + __uint_as_float(Y[1].x), y1 = __uint_as_float(Y[0].z) + __uint_as_float(Y[1].z);
            y0 += dpp<0x128>(y0); y1 += dpp<0x128>(y1);
            y0 = x32_sum(x16_sum(y0)); y1 = x32_sum(x16_sum(y1));
            const float ya = rbf(rbf(y0) + bf_lo(b2v)), yb = rbf(rbf(y1) + bf_hi(b2v));
            ga = rbf(ya * rcp(1.f + ex2(-LOG2E * ya))); gb = rbf(yb * rcp(1.f + ex2(-LOG2E * yb)));
        }
        STAMP(8);
        lds_wait_ge<1>(L, F_S, 1);
        STAMP(11);
        {
            v4u S[4];
#pragma clang loop unroll(disable)
            for (unsigned spin = 0;; ++spin) {
                CFENCE();
#pragma unroll
                for (int k = 0; k < 4; ++k) S[k] = ld_sc1(rs, WS_S + (unsigned)(j * 4096 + (lane + 64 * k) * 16));
                if (__all(TAGOK(S[0]) && TAGOK(S[1]) && TAGOK(S[2]) && TAGOK(S[3])) || spin > SPIN_CAP) break;
            }
            STAMP(12);
            float s0 = (__uint_as_float(S[0].x) + __uint_as_float(S[1].x)) + (__uint_as_float(S[2].x) + __uint_as_float(S[3].x));
            float s1 = (__uint_as_float(S[0].z) + __uint_as_float(S[1].z)) + (__uint_as_float(S[2].z) + __uint_as_float(S[3].z));
            s0 += dpp<0x128>(s0); s1 += dpp<0x128>(s1);
            s0 = x32_sum(x16_sum(s0)); s1 = x32_sum(x16_sum(s1));
            const float oa = bf_lo(xfin) + rbf(rbf(s0) * ga), ob = bf_hi(xfin) + rbf(rbf(s1) * gb);
            if (lane < 8) *(GAS unsigned*)(a.out + b_fin * 512 + 16 * cg_fin + 2 * lane) = pk_bf(oa, ob);
            if (j == 0 && lane == 0) __hip_atomic_store((GAS unsigned long long*)(a.ws + WS_EPOCH), ((unsigned long long)tagB << 32) | tagA, __ATOMIC_RELAXED, __HIP_MEMORY_SCOPE_AGENT);
        }
        STAMP(13);
    }
}

extern "C" void kernel_launch(void* const* d_in, const int* in_sizes, int n_in,
                              void* d_out, int out_size, void* d_ws, size_t ws_size,
                              hipStream_t stream) {
    Args a;
    a.x = (const u16*)d_in[0]; a.kc = (const u16*)d_in[1]; a.vc = (const u16*)d_in[2]; a.wq = (const u16*)d_in[3]; a.wo = (const u16*)d_in[4];
    a.w1 = (const u16*)d_in[6]; a.b1 = (const u16*)d_in[7]; a.w2 = (const u16*)d_in[8]; a.b2 = (const u16*)d_in[9];
    a.out = (u16*)d_out; a.ws = (unsigned char*)d_ws;
    (void)hipFuncSetAttribute((const void*)fused_decode, hipFuncAttributeMaxDynamicSharedMemorySize, L_BYTES);
    hipLaunchKernelGGL(fused_decode, dim3(NWG), dim3(NTHR), L_BYTES, stream, a);
}
```

```cpp
#include <hip/hip_runtime.h>
#include <hip/hip_bf16.h>
#include <stdint.h>

typedef unsigned short u16;
typedef unsigned v4u __attribute__((ext_vector_type(4)));
typedef unsigned v2u __attribute__((ext_vector_type(2)));
typedef float f32x4 __attribute__((ext_vector_type(4)));
typedef float f32x2 __attribute__((ext_vector_type(2)));
typedef __bf16 bf16x8 __attribute__((ext_vector_type(8)));
typedef __bf16 bf16x2 __attribute__((ext_vector_type(2)));
typedef short v4i16 __attribute__((ext_vector_type(4)));
#define GAS __attribute__((address_space(1)))
#define LAS __attribute__((address_space(3)))

#define STAMPS 0
#define WIN 6
#define CODE_LINES 44
#define PSL 0
#define PSL2 4
#define TAILWAIT 0

constexpr int NWG = 256, NWAVE = 11, NTHR = NWAVE * 64;
constexpr unsigned WS_EPOCH = 0;
constexpr unsigned WS_Q = 4096;
constexpr unsigned WS_H = WS_Q + 256 * 2048;
constexpr unsigned WS_O = WS_H + 16 * 4096;
constexpr unsigned WS_S = WS_O + 32 * 16384;
constexpr unsigned WS_Y = WS_S + 256 * 4096;
constexpr unsigned WS_END = WS_Y + 256 * 2048;
constexpr unsigned WS_STAMP = 8u << 20;
constexpr unsigned L_FLG = 64;
constexpr unsigned F_PROG = L_FLG, F_XN = L_FLG + 4, F_Q = L_FLG + 8, F_SC = L_FLG + 12;
constexpr unsigned F_TAG = L_FLG + 16, F_XF = L_FLG + 20, F_S = L_FLG + 24;
constexpr unsigned L_Q = 128;
constexpr unsigned L_H = L_Q + 4 * 272;
constexpr unsigned L_SC = L_H + 8 * 272;
constexpr unsigned L_W1 = 6144;
constexpr unsigned L_WQ = L_W1 + 8192;
constexpr unsigned L_K = L_WQ + 16384;
constexpr unsigned L_V = L_K + 65536;
constexpr unsigned L_XN = L_V + 7 * 8192;
constexpr unsigned L_BYTES = L_V + 65536;
static_assert(L_SC + 2048 <= L_W1 && L_BYTES <= 163840, "LDS");
constexpr unsigned G_WQ = 4, G_W1 = 6, G_K0 = 6, G_V0 = 22, G_W2 = 40, G_WO = 44;
constexpr unsigned SPIN_CAP = 1u << 18;

struct Args {
    const u16* x; const u16* kc; const u16* vc; const u16* wq; const u16* wo;
    const u16* w1; const u16* b1; const u16* w2; const u16* b2; u16* out; unsigned char* ws;
};

__device__ __forceinline__ float bf_lo(unsigned u) { return __uint_as_float(u << 16); }
__device__ __forceinline__ float bf_hi(unsigned u) { return __uint_as_float(u & 0xffff0000u); }
__device__ __forceinline__ unsigned pk_bf(float lo, float hi) { bf16x2 v = {(__bf16)lo, (__bf16)hi}; return __builtin_bit_cast(unsigned, v); }
__device__ __forceinline__ float rbf(float f) { return (float)(__bf16)f; }
__device__ __forceinline__ float rcp(float f) { return __builtin_amdgcn_rcpf(f); }
__device__ __forceinline__ float ex2(float f) { return __builtin_amdgcn_exp2f(f); }
__device__ __forceinline__ float lg2(float f) { return __builtin_amdgcn_logf(f); }
__device__ __forceinline__ f32x4 mfma16(v4u a, v4u b, f32x4 c) {
    return __builtin_amdgcn_mfma_f32_16x16x32_bf16(__builtin_bit_cast(bf16x8, a), __builtin_bit_cast(bf16x8, b), c, 0, 0, 0);
}
__device__ __forceinline__ v2u trread(unsigned p) { return __builtin_bit_cast(v2u, __builtin_amdgcn_ds_read_tr16_b64_v4i16((LAS v4i16*)(size_t)p)); }
__device__ __forceinline__ v4u trfrag(unsigned p0, unsigned p1) { v2u a = trread(p0), b = trread(p1); return (v4u){a.x, a.y, b.x, b.y}; }
template <int CTRL> __device__ __forceinline__ unsigned dppu(unsigned x) { return (unsigned)__builtin_amdgcn_mov_dpp((int)x, CTRL, 0xf, 0xf, true); }
template <int CTRL> __device__ __forceinline__ float dpp(float x) { return __uint_as_float(dppu<CTRL>(__float_as_uint(x))); }
template <int CTRL> __device__ __forceinline__ v4u dpp4(v4u x) { return (v4u){dppu<CTRL>(x.x), dppu<CTRL>(x.y), dppu<CTRL>(x.z), dppu<CTRL>(x.w)}; }
__device__ __forceinline__ float x16_sum(float x) { auto s = __builtin_amdgcn_permlane16_swap(__float_as_uint(x), __float_as_uint(x), false, false); return __uint_as_float(s[0]) + __uint_as_float(s[1]); }
__device__ __forceinline__ float x32_sum(float x) { auto s = __builtin_amdgcn_permlane32_swap(__float_as_uint(x), __float_as_uint(x), false, false); return __uint_as_float(s[0]) + __uint_as_float(s[1]); }
__device__ __forceinline__ float x16_max(float x) { auto s = __builtin_amdgcn_permlane16_swap(__float_as_uint(x), __float_as_uint(x), false, false); return fmaxf(__uint_as_float(s[0]), __uint_as_float(s[1])); }
__device__ __forceinline__ float x32_max(float x) { auto s = __builtin_amdgcn_permlane32_swap(__float_as_uint(x), __float_as_uint(x), false, false); return fmaxf(__uint_as_float(s[0]), __uint_as_float(s[1])); }
__device__ __forceinline__ float wave_sum(float x) {
    x += dpp<0xB1>(x); x += dpp<0x4E>(x); x += dpp<0x141>(x); x += dpp<0x140>(x);
    return x32_sum(x16_sum(x));
}
__device__ __forceinline__ v4u ld_sc1(__amdgpu_buffer_rsrc_t rs, unsigned off, unsigned so) { return __builtin_amdgcn_raw_buffer_load_b128(rs, off, so, 16); }
__device__ __forceinline__ void st_sc1(__amdgpu_buffer_rsrc_t rs, unsigned off, unsigned so, v4u v) { __builtin_amdgcn_raw_buffer_store_b128(v, rs, off, so, 16); }
__device__ __forceinline__ unsigned opq(unsigned v) { asm("" : "+v"(v)); return v; }
#define CFENCE() asm volatile("" ::: "memory")
#define LDS16(off) (*(LAS v4u*)(size_t)(off))
#define LDS8(off) (*(LAS v2u*)(size_t)(off))
#define LDS4(off) (*(LAS unsigned*)(size_t)(off))
#define VLDS4(off) (*(volatile LAS unsigned*)(size_t)(off))
#define STAMP(i) do { } while (0)

template <int NT> __device__ __forceinline__ void glds4(const void* base, unsigned dst, unsigned v0, unsigned v1, unsigned v2, unsigned v3) {
    if (NT) asm volatile("s_mov_b32 m0, %1\n\ts_nop 0\n\tglobal_load_lds_dwordx4 %2, %0 nt\n\tglobal_load_lds_dwordx4 %3, %0 offset:1024 nt\n\t"
                         "global_load_lds_dwordx4 %4, %0 offset:2048 nt\n\tglobal_load_lds_dwordx4 %5, %0 offset:3072 nt"
                         : : "s"(base), "s"(dst), "v"(v0), "v"(v1), "v"(v2), "v"(v3) : "memory", "m0");
    else    asm volatile("s_mov_b32 m0, %1\n\ts_nop 0\n\tglobal_load_lds_dwordx4 %2, %0\n\tglobal_load_lds_dwordx4 %3, %0 offset:1024\n\t"
                         "global_load_lds_dwordx4 %4, %0 offset:2048\n\tglobal_load_lds_dwordx4 %5, %0 offset:3072"
                         : : "s"(base), "s"(dst), "v"(v0), "v"(v1), "v"(v2), "v"(v3) : "memory", "m0");
}
template <int SLEEP> __device__ __forceinline__ void lds_wait_ge(unsigned off, unsigned need) {
#pragma clang loop unroll(disable)
    for (unsigned spin = 0; spin < SPIN_CAP; ++spin) {
        if ((unsigned)__builtin_amdgcn_readfirstlane(VLDS4(off)) >= need) break;
        if (SLEEP) __builtin_amdgcn_s_sleep(SLEEP);
    }
    CFENCE();
}
__device__ __forceinline__ void lds_arrive(unsigned off, unsigned lane) {
    CFENCE();
    if (lane == 0) __hip_atomic_fetch_add((LAS unsigned*)(size_t)(off), 1u, __ATOMIC_RELAXED, __HIP_MEMORY_SCOPE_WORKGROUP);
    CFENCE();
}
#define BAD2(v) (((v).y ^ tag) | ((v).w ^ tag))
#define KEEP4(a, i) asm volatile("" : "+v"(a[i]), "+v"(a[i + 1]), "+v"(a[i + 2]), "+v"(a[i + 3]) : : "memory")
#define SB() __builtin_amdgcn_sched_barrier(0)
#define VMWAIT(n) asm volatile("s_waitcnt vmcnt(%0)" : : "n"(n) : "memory")
#define PROG() do { VMWAIT(4 * (WIN - 1)); ++prog; VLDS4(F_PROG) = prog; if (STAMPS && prog == 38) STAMP(5); } while (0)

__global__ void __launch_bounds__(NTHR, 1) fused_decode(Args a) {
    extern __shared__ __attribute__((aligned(16))) unsigned char smem[];
    unsigned long long pc;
    asm volatile("s_getpc_b64 %0" : "=s"(pc));
    const unsigned tid = threadIdx.x, lane = tid & 63u, wave = __builtin_amdgcn_readfirstlane(tid >> 6);
    const unsigned j = blockIdx.x, xcd = j & 7u, r = j >> 3;
    const unsigned n16 = lane & 15u, g = lane >> 4, q4 = n16 >> 2, p4 = lane & 3u;
    constexpr float LOG2E = 1.4426950408889634f;
    STAMP(0);
    const unsigned hh = (r >> 2) != xcd ? (r >> 2) : ((xcd - 1u) & 7u);
    const unsigned cc = ((r >> 2) != xcd ? xcd - 1u : xcd - 2u) & 7u;
    const unsigned sub = r & 3u;

    if (__builtin_expect(wave == 0, 1)) {
        __builtin_amdgcn_s_setprio(3);
        unsigned prog = 0;
        {
            const unsigned q0 = (lane >> 1) * 8192u + (lane & 1u) * 16u;
            const unsigned char* qb = (const unsigned char*)a.wq + 32u * (((xcd + 1u) & 7u) * 32u + r);
#pragma unroll
            for (int k = 0; k < 4; ++k) glds4<0>(qb + k * 1048576, L_WQ + k * 4096, q0, q0 + 261120u, q0 + 2u * 261120u, q0 + 3u * 261120u);
        }
        STAMP(7);
        {
            const unsigned u0 = lane * 3584u;
            const unsigned char* ub = (const unsigned char*)a.w1 + 16u * (xcd * 28u + (r < 28u ? r : 27u));
#pragma unroll
            for (int k = 0; k < 2; ++k) glds4<0>(ub + k * 917504, L_W1 + k * 4096, u0, u0 + 228352u, u0 + 2u * 228352u, u0 + 3u * 228352u);
        }
        const size_t kvoff = ((size_t)((r >> 2) * 8u + xcd) * 4096u + 3072u + 256u * (r & 3u)) * 256u;
        const unsigned rot = r & 15u;
        unsigned o[4];
#pragma unroll
        for (int i = 0; i < 4; ++i) o[i] = g * 256u + ((n16 ^ g ^ (4u * i)) << 4);
        KEEP4(o, 0);
        const unsigned char* kb = (const unsigned char*)a.kc + kvoff;
#pragma unroll
        for (int c = 0; c < WIN - 6; ++c) glds4<1>(kb + ((c + rot) & 15u) * 4096u, L_K + c * 4096, o[0], o[1], o[2], o[3]);
        __builtin_amdgcn_s_barrier();
        STAMP(1);
#pragma clang loop unroll(disable)
        for (unsigned c = WIN - 6; c < 16; ++c) {
            PROG();
            glds4<1>(kb + ((c + rot) & 15u) * 4096u, L_K + c * 4096, o[0], o[1], o[2], o[3]);
        }
        STAMP(2);
#pragma unroll
        for (int i = 0; i < 2; ++i) o[i] = g * 256u + ((n16 ^ ((4u * i + g) << 1)) << 4);
        KEEP4(o, 0);
        const unsigned char* vb = (const unsigned char*)a.vc + kvoff;
#pragma clang loop unroll(disable)
        for (unsigned c = 0; c < 16; ++c) {
            PROG();
            if (c == 14) lds_wait_ge<1>(F_XF, 2);
            glds4<1>(vb + ((c + rot) & 15u) * 4096u, L_V + c * 4096, o[0], o[1], o[0], o[1]);
        }
        STAMP(4);
        {
            const unsigned t0 = (lane >> 2) * 1024u + p4 * 16u;
            const unsigned char* tb = (const unsigned char*)a.w2 + (size_t)(112u * (2u * hh + (sub >> 1))) * 1024u + 64u * (2u * cc + (sub & 1u));
            const unsigned w0 = (lane >> 3) * 1024u + (((lane & 7u) ^ (((lane >> 4) & 3u) << 1)) << 4);
            const unsigned char* wb = (const unsigned char*)a.wo + (size_t)((4u * hh + sub) * 128u) * 1024u + 128u * cc;
            if (TAILWAIT) {
#pragma unroll
                for (int k = WIN - 1; k >= 0; --k) { VMWAIT(4 * k); ++prog; VLDS4(F_PROG) = prog; }
                STAMP(5);
            }
            if (!TAILWAIT) PROG();
            glds4<0>(tb, L_W1, t0, t0 + 15360u, t0 + 2u * 15360u, t0 + 3u * 15360u);
            if (!TAILWAIT) PROG();
            glds4<0>(tb + 65536, L_W1 + 4096, t0, t0 + 15360u, t0 + 2u * 15360u, t0 + 2u * 15360u - 1024u);
#pragma unroll
            for (int c = 0; c < 4; ++c) {
                if (!TAILWAIT) PROG();
                glds4<1>(wb + c * 32768, L_WQ + c * 4096, w0, w0 + 7168u, w0 + 2u * 7168u, w0 + 3u * 7168u);
            }
        }
#pragma unroll
        for (int k = (TAILWAIT ? 6 : WIN) - 1; k >= 0; --k) {
            VMWAIT(4 * k); ++prog; VLDS4(F_PROG) = prog;
            if (STAMPS && prog == 38) STAMP(5);
        }
        STAMP(6);
        return;
    }
    const __amdgpu_buffer_rsrc_t rs = __builtin_amdgcn_make_buffer_rsrc(a.ws, 0, WS_END, 0x00020000);
    if (__builtin_expect(wave >= 3, 1)) {
        const unsigned w = wave - 3u;
        const v4u xv = *(const GAS v4u*)((const unsigned char*)a.x + w * 1024u + lane * 16u);
        STAMP(13);
        __builtin_amdgcn_s_barrier();
        CFENCE();
        STAMP(1);
        {
            float v[8];
#pragma unroll
            for (int k = 0; k < 4; ++k) { v[2 * k] = bf_lo(xv[k]); v[2 * k + 1] = bf_hi(xv[k]); }
            const float s = ((v[0] + v[1]) + (v[2] + v[3])) + ((v[4] + v[5]) + (v[6] + v[7]));
            const float mean = wave_sum(s) * (1.f / 512.f);
            float q = 0.f;
#pragma unroll
            for (int k = 0; k < 8; ++k) { const float d = v[k] - mean; q = fmaf(d, d, q); }
            const float var = rbf(wave_sum(q) * (1.f / 512.f));
            const float mb = rbf(mean);
            const float den = rbf(sqrtf(rbf(var + 1e-5f)));
            const float rc = rcp(den);
            float o[8];
#pragma unroll
            for (int k = 0; k < 8; ++k) { const float t = rbf(v[k] - mb); const float q0 = t * rc; const float e = fmaf(-q0, den, t); o[k] = fmaf(e, rc, q0); }
            const unsigned xo = L_XN + w * 1024u + (((lane & ~3u) + 2u * (lane & 1u)) ^ (4u * (w & 3u))) * 16u + (lane & 2u) * 4u;
            LDS8(xo) = (v2u){pk_bf(o[0], o[1]), pk_bf(o[2], o[3])};
            LDS8(xo + 16) = (v2u){pk_bf(o[4], o[5]), pk_bf(o[6], o[7])};
        }
        lds_arrive(F_XN, lane);
        STAMP(2);
        v4u kf[8];
        unsigned ka[4];
#pragma unroll
        for (int t = 0; t < 4; ++t) ka[t] = L_K + w * 8192u + n16 * 256u + (((4u * t + g) ^ n16) << 4);
        KEEP4(ka, 0);
        const unsigned va = opq(L_V + (4u * g + q4) * 256u + (((2u * w + (p4 >> 1)) ^ (((4u * g + q4) & 7u) << 1)) << 4) + 8u * (p4 & 1u));
        const unsigned oo = opq(WS_O + ((xcd * 4u + n16) * 32u + r) * 512u + w * 64u + g * 16u);
        lds_wait_ge<1>(F_PROG, G_K0 + 2u * w + 2u);
#pragma unroll
        for (int i = 0; i < 8; ++i) kf[i] = LDS16(ka[i & 3] + 4096 * (i >> 2));
        KEEP4(kf, 0); KEEP4(kf, 4);
        STAMP(3);
        lds_wait_ge<1>(F_Q, 1);
        STAMP(5);
        const unsigned tag = LDS4(F_TAG);
        {
            v4u qf[4];
#pragma unroll
            for (int t = 0; t < 4; ++t) qf[t] = LDS16(L_Q + (n16 & 3u) * 272u + 16u * g + 64 * t);
            SB();
            f32x4 s0 = {0.f, 0.f, 0.f, 0.f}, s1 = {0.f, 0.f, 0.f, 0.f};
#pragma unroll
            for (int t = 0; t < 4; ++t) { s0 = mfma16(kf[t], qf[t], s0); s1 = mfma16(kf[4 + t], qf[t], s1); }
            float sc[8];
#pragma unroll
            for (int k = 0; k < 4; ++k) { sc[k] = rbf(s0[k]) * 0.08838834764831845f; sc[4 + k] = rbf(s1[k]) * 0.08838834764831845f; }
            if (n16 < 4) LDS16(L_SC + w * 256u + n16 * 64u + g * 16u) = (v4u){pk_bf(sc[0], sc[1]), pk_bf(sc[2], sc[3]), pk_bf(sc[4], sc[5]), pk_bf(sc[6], sc[7])};
        }
        lds_arrive(F_SC, lane);
        STAMP(6);
        lds_wait_ge<0>(F_SC, 8);
        STAMP(7);
        v4u P[8]; float lse, inv;
        {
            const unsigned sa = L_SC + q4 * 512u + (n16 & 3u) * 64u + g * 16u;
            const v4u A0 = LDS16(sa), A1 = LDS16(sa + 256);
            float sc[16];
#pragma unroll
            for (int k = 0; k < 4; ++k) { sc[2 * k] = bf_lo(A0[k]); sc[2 * k + 1] = bf_hi(A0[k]); sc[8 + 2 * k] = bf_lo(A1[k]); sc[9 + 2 * k] = bf_hi(A1[k]); }
            float m = sc[0];
#pragma unroll
            for (int k = 1; k < 16; ++k) m = fmaxf(m, sc[k]);
            m = fmaxf(m, dpp<0x124>(m)); m = fmaxf(m, dpp<0x128>(m));
            m = x32_max(x16_max(m));
            const float ml = m * LOG2E;
            float p[16];
#pragma unroll
            for (int k = 0; k < 16; ++k) p[k] = ex2(fmaf(sc[k], LOG2E, -ml));
            P[0] = (v4u){pk_bf(p[0], p[1]), pk_bf(p[2], p[3]), pk_bf(p[4], p[5]), pk_bf(p[6], p[7])};
            P[1] = (v4u){pk_bf(p[8], p[9]), pk_bf(p[10], p[11]), pk_bf(p[12], p[13]), pk_bf(p[14], p[15])};
            float l = (((p[0] + p[1]) + (p[2] + p[3])) + ((p[4] + p[5]) + (p[6] + p[7]))) + (((p[8] + p[9]) + (p[10] + p[11])) + ((p[12] + p[13]) + (p[14] + p[15])));
            l += dpp<0x128>(l); l += dpp<0x124>(l);
            l = x32_sum(x16_sum(l));
            lse = ml + lg2(l); inv = rcp(l);
            P[2] = dpp4<0x12C>(P[0]); P[3] = dpp4<0x12C>(P[1]);
            P[4] = dpp4<0x128>(P[0]); P[5] = dpp4<0x128>(P[1]);
            P[6] = dpp4<0x124>(P[0]); P[7] = dpp4<0x124>(P[1]);
            KEEP4(P, 0); KEEP4(P, 4);
            asm volatile("" : "+v"(lse), "+v"(inv));
        }
        STAMP(8);
        {
            f32x4 d0 = {0.f, 0.f, 0.f, 0.f}, d1 = {0.f, 0.f, 0.f, 0.f};
            v4u vf[4];
            lds_wait_ge<2>(F_PROG, G_V0 + 8);
#pragma unroll
            for (int t = 0; t < 4; ++t) vf[t] = trfrag(va + 8192 * t, va + 8192 * t + 4096);
            d0 = mfma16(vf[0], P[0], d0); d1 = mfma16(vf[1], P[1], d1); d0 = mfma16(vf[2], P[2], d0); d1 = mfma16(vf[3], P[3], d1);
            asm volatile("" : "+v"(d0), "+v"(d1));
            lds_wait_ge<1>(F_PROG, G_V0 + 14);
#pragma unroll
            for (int t = 0; t < 3; ++t) vf[t] = trfrag(va + 8192 * (4 + t), va + 8192 * (4 + t) + 4096);
            d0 = mfma16(vf[0], P[4], d0); d1 = mfma16(vf[1], P[5], d1); d0 = mfma16(vf[2], P[6], d0);
            d0 += d1;
            asm volatile("" : "+v"(d0));
            STAMP(4);
            lds_wait_ge<0>(F_PROG, G_V0 + 16);
            vf[3] = trfrag(va + 8192 * 7, va + 8192 * 7 + 4096);
            d0 = mfma16(vf[3], P[7], d0);
            if (n16 < 4) st_sc1(rs, oo, 0, (v4u){pk_bf(d0[0] * inv, d0[1] * inv), pk_bf(d0[2] * inv, d0[3] * inv), __float_as_uint(lse), tag});
        }
        STAMP(9);
    } else if (wave == 1) {
        const unsigned ep = __hip_atomic_load((GAS unsigned*)(a.ws + WS_EPOCH), __ATOMIC_RELAXED, __HIP_MEMORY_SCOPE_AGENT);
        __builtin_amdgcn_s_setprio(2);
        STAMP(9);
        __builtin_amdgcn_s_barrier();
        CFENCE();
        STAMP(1);
        const unsigned tag = ep + 1u;
        if (lane == 0) LDS4(F_TAG) = tag;
        lds_arrive(F_XN, lane);
        v4u af[16];
        lds_wait_ge<1>(F_PROG, G_WQ);
        STAMP(14);
        {
            const unsigned ao = L_WQ + 8u * p4 + (4u * g + q4) * 32u;
#pragma unroll
            for (int ks = 0; ks < 16; ++ks) af[ks] = trfrag(ao + 1024 * ks, ao + 1024 * ks + 512);
            KEEP4(af, 0); KEEP4(af, 4); KEEP4(af, 8); KEEP4(af, 12);
        }
        STAMP(15);
        lds_wait_ge<0>(F_XN, 9);
        STAMP(10);
        {
            v4u xf[16];
            unsigned xa[4];
#pragma unroll
            for (int k = 0; k < 4; ++k) xa[k] = opq(L_XN + (n16 & 7u) * 1024u + (((4u * k + g) ^ (4u * (n16 & 3u))) << 4));
#pragma unroll
            for (int ks = 0; ks < 16; ++ks) xf[ks] = LDS16(xa[ks & 3] + 256 * (ks >> 2));
            SB();
            f32x4 e0 = {0.f, 0.f, 0.f, 0.f}, e1 = {0.f, 0.f, 0.f, 0.f};
#pragma unroll
            for (int ks = 0; ks < 16; ks += 2) { e0 = mfma16(af[ks], xf[ks], e0); e1 = mfma16(af[ks + 1], xf[ks + 1], e1); }
            e0 += e1;
            const unsigned qo = WS_Q + ((((n16 & 7u) * 4u + (n16 >> 3)) * 8u + ((xcd + 1u) & 7u)) * 128u + r * 4u + g) * 16u;
            const v4u qv = {pk_bf(e0[0], e0[1]), tag, pk_bf(e0[2], e0[3]), tag};
            st_sc1(rs, qo, 0, qv); st_sc1(rs, qo + 32768, 0, qv);
        }
        lds_arrive(F_XF, lane);
        STAMP(4);
        {
            const unsigned qo = WS_Q + j * 2048u + lane * 16u;
            v4u Q[2];
#pragma clang loop unroll(disable)
            for (unsigned spin = 0;; ++spin) {
                CFENCE();
#pragma unroll
                for (int k = 0; k < 2; ++k) Q[k] = ld_sc1(rs, qo + 1024 * k, 0);
                if (__all((BAD2(Q[0]) | BAD2(Q[1])) == 0u) || spin > SPIN_CAP) break;
                if (PSL2) __builtin_amdgcn_s_sleep(PSL2);
            }
#pragma unroll
            for (int k = 0; k < 2; ++k) LDS8(L_Q + (lane >> 5) * 272u + (lane & 31u) * 8u + 544 * k) = (v2u){Q[k].x, Q[k].z};
            CFENCE();
            if (lane == 0) VLDS4(F_Q) = 1u;
        }
        STAMP(5);
        const unsigned oo = WS_O + (hh * 4u + sub) * 16384u + (n16 & 7u) * 2048u + (n16 >> 3) * 512u + g * 16u;
        const unsigned sto = WS_S + (((n16 & 7u) * 32u + cc) * 32u + hh * 4u + sub) * 128u + (2u * g + (n16 >> 3)) * 16u;
        lds_wait_ge<2>(F_PROG, G_WO);
        STAMP(6);
        {
            const unsigned row = 4u * g + q4;
            unsigned wa[4];
#pragma unroll
            for (int k = 0; k < 4; ++k) wa[k] = L_WQ + row * 128u + (((2u * k + (p4 >> 1)) ^ (((row >> 1) & 3u) << 1)) << 4) + 8u * (p4 & 1u);
#pragma unroll
            for (int i = 0; i < 16; ++i) af[i] = trfrag(wa[i & 3] + 4096 * (i >> 2), wa[i & 3] + 4096 * (i >> 2) + 2048);
            KEEP4(af, 0); KEEP4(af, 4); KEEP4(af, 8); KEEP4(af, 12);
        }
        STAMP(11);
        {
            v4u F[16];
#pragma clang loop unroll(disable)
            for (unsigned spin = 0;; ++spin) {
                CFENCE();
#pragma unroll
                for (int k = 0; k < 16; ++k) F[k] = ld_sc1(rs, oo + 64 * (k & 7) + 1024 * (k >> 3), 0);
                unsigned bad = 0;
#pragma unroll
                for (int k = 0; k < 16; ++k) bad |= F[k].w ^ tag;
                if (__all(bad == 0u) || spin > SPIN_CAP) break;
                if (PSL) __builtin_amdgcn_s_sleep(PSL);
            }
            STAMP(12);
            const float l0 = __uint_as_float(F[0].z), l1 = __uint_as_float(F[8].z);
            float M = fmaxf(l0, l1); M = fmaxf(M, dpp<0x128>(M));
            const float e0 = ex2(l0 - M), e1 = ex2(l1 - M);
            float tot = e0 + e1; tot += dpp<0x128>(tot);
            const float it = rcp(tot), w0 = e0 * it, w1 = e1 * it;
            v4u B[8];
#pragma unroll
            for (int k = 0; k < 8; ++k) B[k] = (v4u){F[2 * k].x, F[2 * k].y, F[2 * k + 1].x, F[2 * k + 1].y};
#pragma unroll
            for (int np = 0; np < 2; ++np) {
                f32x4 d[4];
#pragma unroll
                for (int u = 0; u < 4; ++u) d[u] = (f32x4){0.f, 0.f, 0.f, 0.f};
#pragma unroll
                for (int t = 0; t < 4; ++t)
#pragma unroll
                    for (int u = 0; u < 4; ++u) d[u] = mfma16(af[4 * t + 2 * np + (u >> 1)], B[4 * (u & 1) + t], d[u]);
#pragma unroll
                for (int h = 0; h < 2; ++h) {
                    float v[4];
#pragma unroll
                    for (int k = 0; k < 4; ++k) { v[k] = fmaf(w0, d[2 * h][k], w1 * d[2 * h + 1][k]); v[k] += dpp<0x128>(v[k]); }
                    st_sc1(rs, sto + (2 * np + h) * 32768, 0, (v4u){__float_as_uint(n16 < 8 ? v[0] : v[2]), tag, __float_as_uint(n16 < 8 ? v[1] : v[3]), tag});
                }
            }
        }
        CFENCE();
        if (lane == 0) VLDS4(F_S) = 1u;
        STAMP(13);
    } else {
        unsigned cpf = 0;
        if (r < 8) cpf = *(const GAS unsigned*)((pc & ~255ull) + ((r & 1u) * CODE_LINES + (lane < CODE_LINES ? lane : 0u)) * 128u);
        const unsigned fg = xcd * 28u + (r < 28u ? r : 27u);
        const unsigned fo = (r >> 2) * 512u + (xcd * 4u + sub) * 16u + 2u * (lane & 7u);
        const v2u b1v = *(const GAS v2u*)(a.b1 + 8u * fg + 4u * (g & 1u));
        const unsigned xfin = *(const GAS unsigned*)(a.x + fo);
        const unsigned b2v = *(const GAS unsigned*)(a.b2 + (fo & 511u));
        __builtin_amdgcn_s_setprio(2);
        if (lane < 16) LDS4(L_FLG + lane * 4u) = 0u;
        if (lane < 16) LDS16(L_H + (lane >> 1) * 272u + 224u + (lane & 1u) * 16u) = (v4u){0u, 0u, 0u, 0u};
        asm volatile("s_waitcnt lgkmcnt(0)" ::: "memory");
        SB();
        STAMP(9);
        __builtin_amdgcn_s_barrier();
        SB();
        CFENCE();
        STAMP(1);
        v4u af[16];
        lds_wait_ge<1>(F_PROG, G_W1);
        STAMP(14);
        {
            const unsigned ao = L_W1 + 8u * (p4 & 1u) + (4u * g + q4) * 16u;
#pragma unroll
            for (int ks = 0; ks < 16; ++ks) af[ks] = trfrag(ao + 512 * ks, ao + 512 * ks + 256);
            KEEP4(af, 0); KEEP4(af, 4); KEEP4(af, 8); KEEP4(af, 12);
        }
        lds_wait_ge<1>(F_XN, 9);
        STAMP(10);
        const unsigned tag = LDS4(F_TAG);
        {
            v4u xf[16];
            unsigned xa[4];
#pragma unroll
            for (int k = 0; k < 4; ++k) xa[k] = opq(L_XN + (n16 & 7u) * 1024u + (((4u * k + g) ^ (4u * (n16 & 3u))) << 4));
#pragma unroll
            for (int ks = 0; ks < 16; ++ks) xf[ks] = LDS16(xa[ks & 3] + 256 * (ks >> 2));
            SB();
            f32x4 e0 = {0.f, 0.f, 0.f, 0.f}, e1 = {0.f, 0.f, 0.f, 0.f};
#pragma unroll
            for (int ks = 0; ks < 16; ks += 2) { e0 = mfma16(af[ks], xf[ks], e0); e1 = mfma16(af[ks + 1], xf[ks + 1], e1); }
            e0 += e1;
            asm volatile("" : "+v"(e0), "+v"(cpf));
            lds_arrive(F_XF, lane);
            STAMP(3);
            float hv[4];
#pragma unroll
            for (int k = 0; k < 4; ++k) {
                const float bb = (k & 1) ? bf_hi(b1v[k >> 1]) : bf_lo(b1v[k >> 1]);
                const float t = rbf(rbf(e0[k]) + bb);
                const float u = t * fmaf(t * t, -2.f * LOG2E * 0.7978845608028654f * 0.044715f, -2.f * LOG2E * 0.7978845608028654f);
                hv[k] = t * rcp(1.f + ex2(u));
            }
            if (n16 < 8 && g < 2 && r < 28) st_sc1(rs, WS_H + xcd * 8192u + (r >= 14u ? 4096u - 3584u : 0u) + r * 256u + n16 * 32u + g * 16u, 0, (v4u){pk_bf(hv[0], hv[1]), tag, pk_bf(hv[2], hv[3]), tag});
        }
        STAMP(4);
        const unsigned rgf = 2u * hh + (sub >> 1), cf = 2u * cc + (sub & 1u);
        {
            const unsigned ho = WS_H + rgf * 4096u + lane * 16u, ho3 = ho + (lane < 32u ? 3072u : 2048u);
            v4u H[4];
#pragma clang loop unroll(disable)
            for (unsigned spin = 0;; ++spin) {
                CFENCE();
#pragma unroll
                for (int k = 0; k < 3; ++k) H[k] = ld_sc1(rs, ho + 1024 * k, 0);
                H[3] = ld_sc1(rs, ho3, 0);
                if (__all((BAD2(H[0]) | BAD2(H[1]) | BAD2(H[2]) | BAD2(H[3])) == 0u) || spin > SPIN_CAP) break;
                __builtin_amdgcn_s_sleep(8);
            }
            const unsigned hd = L_H + ((lane >> 1) & 7u) * 272u + (lane >> 4) * 16u + (lane & 1u) * 8u;
#pragma unroll
            for (int k = 0; k < 3; ++k) LDS8(hd + 64 * k) = (v2u){H[k].x, H[k].z};
            if (lane < 32) LDS8(hd + 192) = (v2u){H[3].x, H[3].z};
        }
        STAMP(5);
        lds_wait_ge<2>(F_PROG, G_W2);
        STAMP(6);
        {
            v4u hf[4], f0[4], f1[4];
            const unsigned wa = L_W1 + (8u * g + q4) * 64u + 8u * p4;
#pragma unroll
            for (int ks = 0; ks < 4; ++ks) {
                hf[ks] = LDS16(L_H + (n16 & 7u) * 272u + 16u * g + 64 * ks);
                f0[ks] = trfrag(wa + 2048 * ks, wa + 2048 * ks + 256);
                f1[ks] = trfrag(wa + 2048 * ks + 32, wa + 2048 * ks + 288);
            }
            SB();
            f32x4 d0 = {0.f, 0.f, 0.f, 0.f}, d1 = {0.f, 0.f, 0.f, 0.f};
#pragma unroll
            for (int ks = 0; ks < 4; ++ks) { d0 = mfma16(f0[ks], hf[ks], d0); d1 = mfma16(f1[ks], hf[ks], d1); }
            const unsigned cp = n16 >> 3;
            const unsigned yo = WS_Y + ((((n16 & 7u) * 4u + 2u * (sub & 1u)) * 8u + cc) * 16u + rgf) * 128u + (2u * g + cp) * 16u;
            st_sc1(rs, yo, 0, (v4u){__float_as_uint(cp ? d0[2] : d0[0]), tag, __float_as_uint(cp ? d0[3] : d0[1]), tag});
            st_sc1(rs, yo + 16384, 0, (v4u){__float_as_uint(cp ? d1[2] : d1[0]), tag, __float_as_uint(cp ? d1[3] : d1[1]), tag});
            (void)cf;
        }
        STAMP(7);
        float ga, gb;
        {
            v4u Y[2];
#pragma clang loop unroll(disable)
            for (unsigned spin = 0;; ++spin) {
                CFENCE();
#pragma unroll
                for (int k = 0; k < 2; ++k) Y[k] = ld_sc1(rs, WS_Y + j * 2048u + lane * 16u + 1024 * k, 0);
                if (__all((BAD2(Y[0]) | BAD2(Y[1])) == 0u) || spin > SPIN_CAP) break;
                if (PSL2) __builtin_amdgcn_s_sleep(PSL2);
            }
            float y0 = __uint_as_float(Y[0].x) + __uint_as_float(Y[1].x), y1 = __uint_as_float(Y[0].z) + __uint_as_float(Y[1].z);
            y0 += dpp<0x128>(y0); y1 += dpp<0x128>(y1);
            y0 = x32_sum(x16_sum(y0)); y1 = x32_sum(x16_sum(y1));
            const float ya = rbf(rbf(y0) + bf_lo(b2v)), yb = rbf(rbf(y1) + bf_hi(b2v));
            ga = rbf(ya * rcp(1.f + ex2(-LOG2E * ya))); gb = rbf(yb * rcp(1.f + ex2(-LOG2E * yb)));
        }
        STAMP(8);
        lds_wait_ge<1>(F_S, 1);
        STAMP(11);
        {
            v4u S[4];
#pragma clang loop unroll(disable)
            for (unsigned spin = 0;; ++spin) {
                CFENCE();
#pragma unroll
                for (int k = 0; k < 4; ++k) S[k] = ld_sc1(rs, WS_S + j * 4096u + lane * 16u + 1024 * k, 0);
                if (__all((BAD2(S[0]) | BAD2(S[1]) | BAD2(S[2]) | BAD2(S[3])) == 0u) || spin > SPIN_CAP) break;
                if (PSL) __builtin_amdgcn_s_sleep(PSL);
            }
            STAMP(12);
            float s0 = (__uint_as_float(S[0].x) + __uint_as_float(S[1].x)) + (__uint_as_float(S[2].x) + __uint_as_float(S[3].x));
            float s1 = (__uint_as_float(S[0].z) + __uint_as_float(S[1].z)) + (__uint_as_float(S[2].z) + __uint_as_float(S[3].z));
            s0 += dpp<0x128>(s0); s1 += dpp<0x128>(s1);
            s0 = x32_sum(x16_sum(s0)); s1 = x32_sum(x16_sum(s1));
            const float oa = bf_lo(xfin) + rbf(rbf(s0) * ga), ob = bf_hi(xfin) + rbf(rbf(s1) * gb);
            if (lane < 8) *(GAS unsigned*)(a.out + fo) = pk_bf(oa, ob);
            if (j == 0 && lane == 0) __hip_atomic_store((GAS unsigned*)(a.ws + WS_EPOCH), tag, __ATOMIC_RELAXED, __HIP_MEMORY_SCOPE_AGENT);
        }
        STAMP(13);
    }
    (void)smem;
}

extern "C" void kernel_launch(void* const* d_in, const int* in_sizes, int n_in,
                              void* d_out, int out_size, void* d_ws, size_t ws_size,
                              hipStream_t stream) {
    Args a;
    a.x = (const u16*)d_in[0]; a.kc = (const u16*)d_in[1]; a.vc = (const u16*)d_in[2]; a.wq = (const u16*)d_in[3]; a.wo = (const u16*)d_in[4];
    a.w1 = (const u16*)d_in[6]; a.b1 = (const u16*)d_in[7]; a.w2 = (const u16*)d_in[8]; a.b2 = (const u16*)d_in[9];
    a.out = (u16*)d_out; a.ws = (unsigned char*)d_ws;
    (void)hipFuncSetAttribute((const void*)fused_decode, hipFuncAttributeMaxDynamicSharedMemorySize, L_BYTES);
    hipLaunchKernelGGL(fused_decode, dim3(NWG), dim3(NTHR), L_BYTES, stream, a);
}
```

```cpp
#include <hip/hip_runtime.h>
#include <hip/hip_bf16.h>
#include <stdint.h>

typedef unsigned short u16;
typedef unsigned v4u __attribute__((ext_vector_type(4)));
typedef unsigned v2u __attribute__((ext_vector_type(2)));
typedef float f32x4 __attribute__((ext_vector_type(4)));
typedef float f32x2 __attribute__((ext_vector_type(2)));
typedef __bf16 bf16x8 __attribute__((ext_vector_type(8)));
typedef __bf16 bf16x2 __attribute__((ext_vector_type(2)));
typedef short v4i16 __attribute__((ext_vector_type(4)));
#define GAS __attribute__((address_space(1)))
#define LAS __attribute__((address_space(3)))

#define STAMPS 0
#define WIN 6
#define IB 5
#define W1AT 16
#define CODE_LINES 50
#define PSQ 1
#define PSO 0
#define PSS 0
#define WNT 1
#define DRY 1

constexpr int NWG = 256, NWAVE = 11, NTHR = NWAVE * 64;
constexpr unsigned WS_EPOCH = 0;
constexpr unsigned WS_Q = 4096;
constexpr unsigned WS_H = WS_Q + 256 * 8192;
constexpr unsigned WS_O = WS_H + 448 * 512;
constexpr unsigned WS_S = WS_O + 32 * 16384;
constexpr unsigned WS_Y = WS_S + 256 * 4096;
constexpr unsigned WS_END = WS_Y + 256 * 4096;
constexpr unsigned WS_STAMP = 8u << 20;
constexpr unsigned L_FLG = 64;
constexpr unsigned F_PROG = L_FLG, F_XN = L_FLG + 4, F_Q = L_FLG + 8, F_SC = L_FLG + 12;
constexpr unsigned F_TAG = L_FLG + 16, F_XF = L_FLG + 20, F_S = L_FLG + 24, F_QP = L_FLG + 28, F_W1 = L_FLG + 32;
constexpr unsigned L_Q = 128;
constexpr unsigned L_H = L_Q + 4 * 272;
constexpr unsigned L_SC = L_H + 8 * 144 + 64;
constexpr unsigned L_W1 = 6144;
constexpr unsigned L_WQ = L_W1 + 8192;
constexpr unsigned L_K = L_WQ + 16384;
constexpr unsigned L_V = L_K + 65536;
constexpr unsigned L_XN = L_V + 7 * 8192;
constexpr unsigned L_BYTES = L_V + 65536;
static_assert(L_SC % 16 == 0 && L_SC + 2048 <= L_W1 && L_BYTES <= 163840, "LDS");
static_assert(4 + IB >= WIN && 4 + IB <= 15 && W1AT >= IB && W1AT <= 31, "loader");
constexpr unsigned G_KV(unsigned c) { return 4u + c + (c > W1AT ? 2u : 0u); }
constexpr unsigned G_WQ = 4, G_W1 = 4 + W1AT + 2, G_W2 = 40, G_WO = 44;
constexpr unsigned SPIN_CAP = 1u << 18;

struct Args {
    const u16* x; const u16* kc; const u16* vc; const u16* wq; const u16* wo;
    const u16* w1; const u16* b1; const u16* w2; const u16* b2; u16* out; unsigned char* ws;
};

__device__ __forceinline__ float bf_lo(unsigned u) { return __uint_as_float(u << 16); }
__device__ __forceinline__ float bf_hi(unsigned u) { return __uint_as_float(u & 0xffff0000u); }
__device__ __forceinline__ unsigned pk_bf(float lo, float hi) { bf16x2 v = {(__bf16)lo, (__bf16)hi}; return __builtin_bit_cast(unsigned, v); }
__device__ __forceinline__ float rbf(float f) { return (float)(__bf16)f; }
__device__ __forceinline__ float rcp(float f) { return __builtin_amdgcn_rcpf(f); }
__device__ __forceinline__ float ex2(float f) { return __builtin_amdgcn_exp2f(f); }
__device__ __forceinline__ float lg2(float f) { return __builtin_amdgcn_logf(f); }
__device__ __forceinline__ f32x4 mfma16(v4u a, v4u b, f32x4 c) {
    return __builtin_amdgcn_mfma_f32_16x16x32_bf16(__builtin_bit_cast(bf16x8, a), __builtin_bit_cast(bf16x8, b), c, 0, 0, 0);
}
__device__ __forceinline__ v2u trread(unsigned p) { return __builtin_bit_cast(v2u, __builtin_amdgcn_ds_read_tr16_b64_v4i16((LAS v4i16*)(size_t)p)); }
__device__ __forceinline__ v4u trfrag(unsigned p0, unsigned p1) { v2u a = trread(p0), b = trread(p1); return (v4u){a.x, a.y, b.x, b.y}; }
template <int CTRL> __device__ __forceinline__ unsigned dppu(unsigned x) { return (unsigned)__builtin_amdgcn_mov_dpp((int)x, CTRL, 0xf, 0xf, true); }
template <int CTRL> __device__ __forceinline__ float dpp(float x) { return __uint_as_float(dppu<CTRL>(__float_as_uint(x))); }
template <int CTRL> __device__ __forceinline__ v4u dpp4(v4u x) { return (v4u){dppu<CTRL>(x.x), dppu<CTRL>(x.y), dppu<CTRL>(x.z), dppu<CTRL>(x.w)}; }
__device__ __forceinline__ float x16_sum(float x) { auto s = __builtin_amdgcn_permlane16_swap(__float_as_uint(x), __float_as_uint(x), false, false); return __uint_as_float(s[0]) + __uint_as_float(s[1]); }
__device__ __forceinline__ float x32_sum(float x) { auto s = __builtin_amdgcn_permlane32_swap(__float_as_uint(x), __float_as_uint(x), false, false); return __uint_as_float(s[0]) + __uint_as_float(s[1]); }
__device__ __forceinline__ float x16_max(float x) { auto s = __builtin_amdgcn_permlane16_swap(__float_as_uint(x), __float_as_uint(x), false, false); return fmaxf(__uint_as_float(s[0]), __uint_as_float(s[1])); }
__device__ __forceinline__ float x32_max(float x) { auto s = __builtin_amdgcn_permlane32_swap(__float_as_uint(x), __float_as_uint(x), false, false); return fmaxf(__uint_as_float(s[0]), __uint_as_float(s[1])); }
__device__ __forceinline__ float wave_sum(float x) {
    x += dpp<0xB1>(x); x += dpp<0x4E>(x); x += dpp<0x141>(x); x += dpp<0x140>(x);
    return x32_sum(x16_sum(x));
}
__device__ __forceinline__ v4u ld_sc1(__amdgpu_buffer_rsrc_t rs, unsigned off, unsigned so) { return __builtin_amdgcn_raw_buffer_load_b128(rs, off, so, 16); }
__device__ __forceinline__ void st_sc1(__amdgpu_buffer_rsrc_t rs, unsigned off, unsigned so, v4u v) { __builtin_amdgcn_raw_buffer_store_b128(v, rs, off, so, 16); }
__device__ __forceinline__ unsigned opq(unsigned v) { asm("" : "+v"(v)); return v; }
#define CFENCE() asm volatile("" ::: "memory")
#define LDS16(off) (*(LAS v4u*)(size_t)(off))
#define LDS8(off) (*(LAS v2u*)(size_t)(off))
#define LDS4(off) (*(LAS unsigned*)(size_t)(off))
#define VLDS4(off) (*(volatile LAS unsigned*)(size_t)(off))
#define STAMP(i) do { } while (0)
#define STAMP_END() do { } while (0)

template <int NT> __device__ __forceinline__ void glds4(const void* base, unsigned dst, unsigned v0, unsigned v1, unsigned v2, unsigned v3) {
    if (NT) asm volatile("s_mov_b32 m0, %1\n\ts_nop 0\n\tglobal_load_lds_dwordx4 %2, %0 nt\n\tglobal_load_lds_dwordx4 %3, %0 offset:1024 nt\n\t"
                         "global_load_lds_dwordx4 %4, %0 offset:2048 nt\n\tglobal_load_lds_dwordx4 %5, %0 offset:3072 nt"
                         : : "s"(base), "s"(dst), "v"(v0), "v"(v1), "v"(v2), "v"(v3) : "memory", "m0");
    else    asm volatile("s_mov_b32 m0, %1\n\ts_nop 0\n\tglobal_load_lds_dwordx4 %2, %0\n\tglobal_load_lds_dwordx4 %3, %0 offset:1024\n\t"
                         "global_load_lds_dwordx4 %4, %0 offset:2048\n\tglobal_load_lds_dwordx4 %5, %0 offset:3072"
                         : : "s"(base), "s"(dst), "v"(v0), "v"(v1), "v"(v2), "v"(v3) : "memory", "m0");
}
template <int NT> __device__ __forceinline__ void glds1(const void* base, unsigned dst, unsigned v0) {
    if (NT) asm volatile("s_mov_b32 m0, %1\n\ts_nop 0\n\tglobal_load_lds_dwordx4 %2, %0 nt" : : "s"(base), "s"(dst), "v"(v0) : "memory", "m0");
    else    asm volatile("s_mov_b32 m0, %1\n\ts_nop 0\n\tglobal_load_lds_dwordx4 %2, %0" : : "s"(base), "s"(dst), "v"(v0) : "memory", "m0");
}
template <int SLEEP> __device__ __forceinline__ void lds_wait_ge(unsigned off, unsigned need) {
#pragma clang loop unroll(disable)
    for (unsigned spin = 0; spin < SPIN_CAP; ++spin) {
        if ((unsigned)__builtin_amdgcn_readfirstlane(VLDS4(off)) >= need) break;
        if (SLEEP) __builtin_amdgcn_s_sleep(SLEEP);
    }
    CFENCE();
}
template <unsigned OFF> __device__ __forceinline__ void lds_arrive() {
    asm volatile("s_mov_b64 exec, 1\n\tds_add_u32 %0, %1 offset:%2\n\ts_mov_b64 exec, -1" : : "v"(0u), "v"(1u), "n"(OFF) : "memory");
}
#define KEEP4(a, i) asm volatile("" : "+v"(a[i]), "+v"(a[i + 1]), "+v"(a[i + 2]), "+v"(a[i + 3]) : : "memory")
#define SB() __builtin_amdgcn_sched_barrier(0)
#define VMWAIT(n) asm volatile("s_waitcnt vmcnt(%0)" : : "n"(n) : "memory")
#define PROG() do { VMWAIT(4 * (WIN - 1)); ++prog; VLDS4(F_PROG) = prog; if (STAMPS && prog == G_KV(32)) STAMP(5); if (STAMPS && prog == G_WQ) STAMP(1); if (STAMPS && prog == G_KV(16)) STAMP(2); } while (0)
__device__ __forceinline__ v4u enc4(f32x4 v, unsigned tag) {
    v4u o;
#pragma unroll
    for (int k = 0; k < 4; ++k) o[k] = ((__float_as_uint(v[k]) + 0x80u) & 0xffffff00u) | ((tag >> (8 * k)) & 0xffu);
    return o;
}
__device__ __forceinline__ unsigned bad4(v4u w, unsigned tag) {
    return (__builtin_amdgcn_perm(w.y, w.x, 0x0c0c0400u) | __builtin_amdgcn_perm(w.w, w.z, 0x04000c0cu)) ^ tag;
}
__device__ __forceinline__ f32x4 dec4(v4u w) {
    return (f32x4){__uint_as_float(w.x & 0xffffff00u), __uint_as_float(w.y & 0xffffff00u), __uint_as_float(w.z & 0xffffff00u), __uint_as_float(w.w & 0xffffff00u)};
}
__device__ __forceinline__ bool any_lane(unsigned v) { return __builtin_amdgcn_ballot_w64(v != 0u) != 0ull; }

__global__ void __launch_bounds__(NTHR, 1) fused_decode(Args a) {
    extern __shared__ __attribute__((aligned(16))) unsigned char smem[];
    unsigned long long pc;
    asm volatile("s_getpc_b64 %0" : "=s"(pc));
    const unsigned tid = threadIdx.x, lane = tid & 63u, wave = __builtin_amdgcn_readfirstlane(tid >> 6);
    const unsigned j = blockIdx.x, xcd = j & 7u, r = j >> 3;
    const unsigned n16 = lane & 15u, g = lane >> 4, q4 = n16 >> 2, p4 = lane & 3u;
    constexpr float LOG2E = 1.4426950408889634f;
    STAMP(0);
    unsigned cpf = 0, ep = 0;
    v4u xv = {0u, 0u, 0u, 0u};
    if (wave != 0) {
        if (wave == 2) {
            if (r < 8) cpf = *(const GAS unsigned*)((pc & ~255ull) + ((r & 1u) * CODE_LINES + (lane < CODE_LINES ? lane : 0u)) * 128u);
            if (lane < 16) LDS4(L_FLG + lane * 4u) = 0u;
            asm volatile("s_waitcnt lgkmcnt(0)" ::: "memory");
        }
        if (wave >= 3) xv = *(const GAS v4u*)((const unsigned char*)a.x + (wave - 3u) * 1024u + lane * 16u);
        if (wave == 1) ep = __hip_atomic_load((GAS unsigned*)(a.ws + WS_EPOCH), __ATOMIC_RELAXED, __HIP_MEMORY_SCOPE_AGENT);
        __builtin_amdgcn_s_barrier();
        CFENCE();
    }
    const unsigned hh = (r >> 2) != xcd ? (r >> 2) : ((xcd - 1u) & 7u);
    const unsigned cc = ((r >> 2) != xcd ? xcd - 1u : xcd - 2u) & 7u;
    const unsigned sub = r & 3u;
    const unsigned c32 = 28u * (xcd >> 2) + (r < 28u ? r : 27u), k1 = xcd & 3u;
    const unsigned rg = 16u * (1u - (xcd >> 2)) + (r >> 1), cg8 = ((xcd - 1u) & 3u) + 4u * (r & 1u);

    if (wave == 0) {
        __builtin_amdgcn_s_setprio(3);
        const unsigned sw = ((lane & 7u) ^ (((lane >> 4) & 3u) << 1)) << 4;
        {
            const unsigned q0 = (lane >> 3) * 8192u + sw;
            const unsigned char* qb = (const unsigned char*)a.wq + (r >> 3) * 1048576u + ((xcd + 1u) & 7u) * 1024u + (r & 7u) * 128u;
#pragma clang loop unroll(disable)
            for (unsigned k = 0; k < 4; ++k) glds4<WNT>(qb + k * 262144u, L_WQ + k * 4096u, q0, q0 + 64512u, q0 + 2u * 64512u, q0 + 3u * 64512u);
        }
        STAMP(7);
        const size_t kvoff = ((size_t)((r >> 2) * 8u + xcd) * 4096u + 3072u + 256u * (r & 3u)) * 256u;
        const unsigned rot = r & 15u;
        const unsigned char* kb = (const unsigned char*)a.kc + kvoff;
        unsigned o[4];
#pragma unroll
        for (int i = 0; i < 4; ++i) o[i] = g * 256u + ((n16 ^ g ^ (4u * i)) << 4);
        KEEP4(o, 0);
        unsigned prog = 4 + IB - WIN;
#pragma clang loop unroll(disable)
        for (unsigned c = 0; c < 32; ++c) {
            if (c == 2) { __builtin_amdgcn_s_barrier(); STAMP(3); }
            if (c == W1AT) {
                const unsigned u0 = (lane >> 2) * 3584u + ((p4 ^ (((lane >> 4) & 1u) << 1)) << 4);
                const unsigned char* ub = (const unsigned char*)a.w1 + k1 * 458752u + c32 * 64u;
#pragma unroll
                for (unsigned k = 0; k < 2; ++k) {
                    const unsigned kk = (k ^ r) & 1u;
                    PROG();
                    glds4<WNT>(ub + kk * 229376u, L_W1 + kk * 4096u, u0, u0 + 56320u, u0 + 2u * 56320u, u0 + 3u * 56320u);
                }
            }
            if (c == 16) {
                o[0] = o[2] = g * 256u + ((n16 ^ (g << 1)) << 4);
                o[1] = o[3] = g * 256u + ((n16 ^ ((4u + g) << 1)) << 4);
                kb = (const unsigned char*)a.vc + kvoff;
            }
            if (c >= IB) PROG();
            if (c == 30) lds_wait_ge<1>(F_XF, 2);
            glds4<1>(kb + ((c + rot) & 15u) * 4096u, L_K + c * 4096u, o[0], o[1], o[2], o[3]);
        }
        STAMP(4);
        {
            const unsigned w0 = (lane >> 3) * 1024u + sw;
            const unsigned char* tb = (const unsigned char*)a.w2 + rg * 57344u + cg8 * 128u;
            const unsigned char* wb = (const unsigned char*)a.wo + (4u * hh + sub) * 131072u + 128u * cc;
            PROG();
            lds_wait_ge<1>(F_W1, 1);
            glds4<WNT>(tb, L_W1, w0, w0 + 7168u, w0 + 2u * 7168u, w0 + 3u * 7168u);
            PROG();
            glds4<WNT>(tb + 32768, L_W1 + 4096, w0, w0 + 7168u, w0 + 2u * 7168u, w0 + 2u * 7168u - 1024u);
#pragma clang loop unroll(disable)
            for (unsigned c = 0; c < 4; ++c) {
                PROG();
                glds4<WNT>(wb + c * 32768u, L_WQ + c * 4096u, w0, w0 + 7168u, w0 + 2u * 7168u, w0 + 3u * 7168u);
            }
        }
#pragma unroll
        for (int k = WIN - 1; k >= 0; --k) {
            VMWAIT(4 * k); ++prog; VLDS4(F_PROG) = prog;
            if (STAMPS && prog == G_KV(32)) STAMP(5);
        }
        STAMP(6);
    }
    const __amdgpu_buffer_rsrc_t rs = __builtin_amdgcn_make_buffer_rsrc(a.ws, 0, WS_END, 0x00020000);
    if (wave >= 3) {
        const unsigned w = wave - 3u;
        STAMP(13);
        {
            float v[8];
#pragma unroll
            for (int k = 0; k < 4; ++k) { v[2 * k] = bf_lo(xv[k]); v[2 * k + 1] = bf_hi(xv[k]); }
            const float s = ((v[0] + v[1]) + (v[2] + v[3])) + ((v[4] + v[5]) + (v[6] + v[7]));
            const float mean = wave_sum(s) * (1.f / 512.f);
            float q = 0.f;
#pragma unroll
            for (int k = 0; k < 8; ++k) { const float d = v[k] - mean; q = fmaf(d, d, q); }
            const float var = rbf(wave_sum(q) * (1.f / 512.f));
            const float mb = rbf(mean);
            const float den = rbf(sqrtf(rbf(var + 1e-5f)));
            const float rc = rcp(den);
            float o[8];
#pragma unroll
            for (int k = 0; k < 8; ++k) { const float t = rbf(v[k] - mb); const float q0 = t * rc; const float e = fmaf(-q0, den, t); o[k] = fmaf(e, rc, q0); }
            const unsigned xo = L_XN + w * 1024u + (((lane & ~3u) + 2u * (lane & 1u)) ^ (4u * (w & 3u))) * 16u + (lane & 2u) * 4u;
            LDS8(xo) = (v2u){pk_bf(o[0], o[1]), pk_bf(o[2], o[3])};
            LDS8(xo + 16) = (v2u){pk_bf(o[4], o[5]), pk_bf(o[6], o[7])};
        }
        lds_arrive<F_XN>();
        STAMP(2);
        unsigned ka[4];
#pragma unroll
        for (int t = 0; t < 4; ++t) ka[t] = L_K + w * 8192u + n16 * 256u + (((4u * t + g) ^ n16) << 4);
        KEEP4(ka, 0);
        const unsigned va = opq(L_V + (4u * g + q4) * 256u + (((2u * w + (p4 >> 1)) ^ (((4u * g + q4) & 7u) << 1)) << 4) + 8u * (p4 & 1u));
        const unsigned oo = opq(WS_O + ((xcd * 4u + n16) * 32u + r) * 512u + w * 64u + g * 16u);
        const unsigned qo = opq(WS_Q + j * 8192u + w * 1024u + lane * 16u);
        const unsigned kneed = 2u * w + 2u > W1AT ? 2u * w + 8u : 2u * w + 6u;
        unsigned pm = DRY ? 0u : ~0u;
#pragma clang loop unroll(disable)
        for (;;) {
            asm volatile("" : "+s"(pm));
            lds_wait_ge<1>(F_QP, 1u & pm);
            const unsigned tag = LDS4(F_TAG);
            v4u kf[8];
            {
                v4u Q;
                asm volatile("" : "=v"(Q));
                unsigned bad = pm;
                bool kd = false;
#pragma clang loop unroll(disable)
                for (unsigned spin = 0;; ++spin) {
                    CFENCE();
                    if (bad) Q = ld_sc1(rs, qo, 0);
                    if (!kd && (unsigned)__builtin_amdgcn_readfirstlane(VLDS4(F_PROG)) >= (kneed & pm)) {
                        CFENCE();
#pragma unroll
                        for (int i = 0; i < 8; ++i) kf[i] = LDS16(ka[i & 3] + 4096 * (i >> 2));
                        KEEP4(kf, 0); KEEP4(kf, 4);
                        kd = true;
                        STAMP(3);
                    }
                    if (bad) bad = bad4(Q, tag);
                    if (!any_lane(bad) || spin > SPIN_CAP) break;
                    if (PSQ) __builtin_amdgcn_s_sleep(PSQ);
                }
                STAMP(10);
                const f32x4 qv = dec4(Q);
                float qs[4];
#pragma unroll
                for (int k = 0; k < 4; ++k) qs[k] = x32_sum(x16_sum(qv[k]));
                if (lane < 16) LDS8(L_Q + (w >> 1) * 272u + (w & 1u) * 128u + lane * 8u) = (v2u){pk_bf(qs[0], qs[1]), pk_bf(qs[2], qs[3])};
                lds_arrive<F_Q>();
                if (!kd) {
                    lds_wait_ge<0>(F_PROG, kneed);
#pragma unroll
                    for (int i = 0; i < 8; ++i) kf[i] = LDS16(ka[i & 3] + 4096 * (i >> 2));
                    KEEP4(kf, 0); KEEP4(kf, 4);
                    STAMP(3);
                }
            }
            lds_wait_ge<0>(F_Q, DRY ? 8u + (8u & pm) : 8u);
            STAMP(5);
            {
                v4u qf[4];
#pragma unroll
                for (int t = 0; t < 4; ++t) qf[t] = LDS16(L_Q + (n16 & 3u) * 272u + 16u * g + 64 * t);
                SB();
                f32x4 s0 = {0.f, 0.f, 0.f, 0.f}, s1 = {0.f, 0.f, 0.f, 0.f};
#pragma unroll
                for (int t = 0; t < 4; ++t) { s0 = mfma16(kf[t], qf[t], s0); s1 = mfma16(kf[4 + t], qf[t], s1); }
                float sc[8];
#pragma unroll
                for (int k = 0; k < 4; ++k) { sc[k] = rbf(s0[k]) * 0.08838834764831845f; sc[4 + k] = rbf(s1[k]) * 0.08838834764831845f; }
                if (n16 < 4) LDS16(L_SC + w * 256u + n16 * 64u + g * 16u) = (v4u){pk_bf(sc[0], sc[1]), pk_bf(sc[2], sc[3]), pk_bf(sc[4], sc[5]), pk_bf(sc[6], sc[7])};
            }
            lds_arrive<F_SC>();
            STAMP(6);
            lds_wait_ge<0>(F_SC, DRY ? 8u + (8u & pm) : 8u);
            STAMP(7);
            v4u P[8]; float lse, inv;
            {
                const unsigned sa = L_SC + q4 * 512u + (n16 & 3u) * 64u + g * 16u;
                const v4u A0 = LDS16(sa), A1 = LDS16(sa + 256);
                float sc[16];
#pragma unroll
                for (int k = 0; k < 4; ++k) { sc[2 * k] = bf_lo(A0[k]); sc[2 * k + 1] = bf_hi(A0[k]); sc[8 + 2 * k] = bf_lo(A1[k]); sc[9 + 2 * k] = bf_hi(A1[k]); }
                float m = sc[0];
#pragma unroll
                for (int k = 1; k < 16; ++k) m = fmaxf(m, sc[k]);
                m = fmaxf(m, dpp<0x124>(m)); m = fmaxf(m, dpp<0x128>(m));
                m = x32_max(x16_max(m));
                const float ml = m * LOG2E;
                float p[16];
#pragma unroll
                for (int k = 0; k < 16; ++k) p[k] = ex2(fmaf(sc[k], LOG2E, -ml));
                P[0] = (v4u){pk_bf(p[0], p[1]), pk_bf(p[2], p[3]), pk_bf(p[4], p[5]), pk_bf(p[6], p[7])};
                P[1] = (v4u){pk_bf(p[8], p[9]), pk_bf(p[10], p[11]), pk_bf(p[12], p[13]), pk_bf(p[14], p[15])};
                float l = (((p[0] + p[1]) + (p[2] + p[3])) + ((p[4] + p[5]) + (p[6] + p[7]))) + (((p[8] + p[9]) + (p[10] + p[11])) + ((p[12] + p[13]) + (p[14] + p[15])));
                l += dpp<0x128>(l); l += dpp<0x124>(l);
                l = x32_sum(x16_sum(l));
                lse = ml + lg2(l); inv = rcp(l);
                P[2] = dpp4<0x12C>(P[0]); P[3] = dpp4<0x12C>(P[1]);
                P[4] = dpp4<0x128>(P[0]); P[5] = dpp4<0x128>(P[1]);
                P[6] = dpp4<0x124>(P[0]); P[7] = dpp4<0x124>(P[1]);
                KEEP4(P, 0); KEEP4(P, 4);
                asm volatile("" : "+v"(lse), "+v"(inv));
            }
            STAMP(8);
            {
                f32x4 d0 = {0.f, 0.f, 0.f, 0.f}, d1 = {0.f, 0.f, 0.f, 0.f};
                v4u vf[4];
                lds_wait_ge<2>(F_PROG, G_KV(24) & pm);
#pragma unroll
                for (int t = 0; t < 4; ++t) vf[t] = trfrag(va + 8192 * t, va + 8192 * t + 4096);
                d0 = mfma16(vf[0], P[0], d0); d1 = mfma16(vf[1], P[1], d1); d0 = mfma16(vf[2], P[2], d0); d1 = mfma16(vf[3], P[3], d1);
                asm volatile("" : "+v"(d0), "+v"(d1));
                lds_wait_ge<1>(F_PROG, G_KV(30) & pm);
#pragma unroll
                for (int t = 0; t < 3; ++t) vf[t] = trfrag(va + 8192 * (4 + t), va + 8192 * (4 + t) + 4096);
                d0 = mfma16(vf[0], P[4], d0); d1 = mfma16(vf[1], P[5], d1); d0 = mfma16(vf[2], P[6], d0);
                d0 += d1;
                asm volatile("" : "+v"(d0));
                STAMP(4);
                lds_wait_ge<0>(F_PROG, G_KV(32) & pm);
                vf[3] = trfrag(va + 8192 * 7, va + 8192 * 7 + 4096);
                d0 = mfma16(vf[3], P[7], d0);
                v4u ov = {pk_bf(d0[0] * inv, d0[1] * inv), pk_bf(d0[2] * inv, d0[3] * inv), __float_as_uint(lse), tag};
                asm volatile("" : "+v"(ov));
                if (n16 < (4u & pm)) st_sc1(rs, oo, 0, ov);
            }
            if (pm) break;
            pm = ~0u;
            STAMP(11);
        }
        STAMP(9);
    }
    if (wave == 1) {
        __builtin_amdgcn_s_setprio(2);
        STAMP(9);
        unsigned wa[4];
        {
            const unsigned row = 4u * g + q4;
#pragma unroll
            for (int k = 0; k < 4; ++k) wa[k] = L_WQ + row * 128u + (((2u * k + (p4 >> 1)) ^ (((row >> 1) & 3u) << 1)) << 4) + 8u * (p4 & 1u);
            KEEP4(wa, 0);
        }
        unsigned tag = 0;
        v4u af[16];
        unsigned pm = DRY ? 0u : ~0u;
#pragma clang loop unroll(disable)
        for (;;) {
            asm volatile("" : "+s"(pm));
            if (pm) {
                asm volatile("" : "+v"(ep));
                tag = ep + 0x9E3779B1u;
                if (lane == 0) LDS4(F_TAG) = tag;
                lds_arrive<F_XN>();
            }
            lds_wait_ge<1>(F_PROG, G_WQ & pm);
            STAMP(14);
#pragma unroll
            for (int i = 0; i < 16; ++i) af[i] = trfrag(wa[i & 3] + 4096 * (i >> 2), wa[i & 3] + 4096 * (i >> 2) + 2048);
            KEEP4(af, 0); KEEP4(af, 4); KEEP4(af, 8); KEEP4(af, 12);
            STAMP(15);
            lds_wait_ge<0>(F_XN, 9u & pm);
            STAMP(10);
            v4u xf[4];
#pragma unroll
            for (int k = 0; k < 4; ++k) xf[k] = LDS16(L_XN + (n16 & 7u) * 1024u + (r >> 3) * 256u + (((4u * k + g) ^ (4u * (n16 & 3u))) << 4));
            SB();
            f32x4 d[4];
#pragma unroll
            for (int t = 0; t < 4; ++t) d[t] = (f32x4){0.f, 0.f, 0.f, 0.f};
#pragma unroll
            for (int k = 0; k < 4; ++k)
#pragma unroll
                for (int t = 0; t < 4; ++t) d[t] = mfma16(af[4 * k + t], xf[k], d[t]);
            const unsigned qo = opq(WS_Q + ((xcd + 1u) & 7u) * 8192u + (n16 & 7u) * 262144u + (r & 7u) * 1024u + (r >> 3) * 256u + (n16 >> 3) * 64u + g * 16u);
            v4u q0 = enc4(n16 < 8 ? d[0] : d[1], tag), q1 = enc4(n16 < 8 ? d[2] : d[3], tag);
            asm volatile("" : "+v"(q0), "+v"(q1));
            if (pm) {
#pragma unroll
                for (int s = 0; s < 4; ++s) { st_sc1(rs, qo, s * 65536, q0); st_sc1(rs, qo + 128, s * 65536, q1); }
                break;
            }
            pm = ~0u;
            STAMP(3);
        }
        CFENCE();
        if (lane == 0) VLDS4(F_QP) = 1u;
        lds_arrive<F_XF>();
        STAMP(4);
        const unsigned oo = opq(WS_O + (hh * 4u + sub) * 16384u + (n16 & 7u) * 2048u + (n16 >> 3) * 512u + g * 16u);
        const unsigned sto = WS_S + cc * 4096u + (n16 & 7u) * 131072u + (n16 >> 3) * 32768u + (hh * 4u + sub) * 128u + g * 16u;
        pm = DRY ? 0u : ~0u;
#pragma clang loop unroll(disable)
        for (;;) {
            asm volatile("" : "+s"(pm));
            lds_wait_ge<2>(F_PROG, G_WO & pm);
            STAMP(6);
#pragma unroll
            for (int i = 0; i < 16; ++i) af[i] = trfrag(wa[i & 3] + 4096 * (i >> 2), wa[i & 3] + 4096 * (i >> 2) + 2048);
            KEEP4(af, 0); KEEP4(af, 4); KEEP4(af, 8); KEEP4(af, 12);
            STAMP(11);
            v4u F[16];
#pragma unroll
            for (int k = 0; k < 16; ++k) asm volatile("" : "=v"(F[k]));
            unsigned bad = pm;
#pragma clang loop unroll(disable)
            for (unsigned spin = 0;; ++spin) {
                unsigned pa = oo;
                asm volatile("" : "+v"(pa) : : "memory");
                if (bad) {
#pragma unroll
                    for (int k = 0; k < 16; ++k) F[k] = ld_sc1(rs, pa + 64 * (k & 7) + 1024 * (k >> 3), 0);
                    bad = 0;
#pragma unroll
                    for (int k = 0; k < 16; ++k) bad |= F[k].w ^ tag;
                }
                if (!any_lane(bad) || spin > SPIN_CAP) break;
                if (PSO) __builtin_amdgcn_s_sleep(PSO);
            }
            STAMP(12);
            const float l0 = __uint_as_float(F[0].z), l1 = __uint_as_float(F[8].z);
            float M = fmaxf(l0, l1); M = fmaxf(M, dpp<0x128>(M));
            const float e0 = ex2(l0 - M), e1 = ex2(l1 - M);
            float tot = e0 + e1; tot += dpp<0x128>(tot);
            const float it = rcp(tot), w0 = e0 * it, w1 = e1 * it;
            v4u B[8];
#pragma unroll
            for (int k = 0; k < 8; ++k) B[k] = (v4u){F[2 * k].x, F[2 * k].y, F[2 * k + 1].x, F[2 * k + 1].y};
#pragma unroll
            for (int np = 0; np < 2; ++np) {
                f32x4 d[4];
#pragma unroll
                for (int u = 0; u < 4; ++u) d[u] = (f32x4){0.f, 0.f, 0.f, 0.f};
#pragma unroll
                for (int t = 0; t < 4; ++t)
#pragma unroll
                    for (int u = 0; u < 4; ++u) d[u] = mfma16(af[4 * t + 2 * np + (u >> 1)], B[4 * (u & 1) + t], d[u]);
                f32x4 v;
#pragma unroll
                for (int k = 0; k < 4; ++k) {
                    const float v0 = fmaf(w0, d[0][k], w1 * d[1][k]), v1 = fmaf(w0, d[2][k], w1 * d[3][k]);
                    v[k] = (n16 < 8 ? v0 : v1) + dpp<0x128>(n16 < 8 ? v1 : v0);
                }
                v4u sv = enc4(v, tag);
                asm volatile("" : "+v"(sv));
                if (pm) st_sc1(rs, sto, np * 65536, sv);
            }
            if (pm) break;
            pm = ~0u;
            STAMP(5);
        }
        CFENCE();
        if (lane == 0) VLDS4(F_S) = 1u;
        STAMP(13);
    }
    if (wave == 2) {
        const unsigned fo = (r >> 2) * 512u + (xcd * 4u + sub) * 16u + 4u * p4;
        const unsigned bo = 56u * rg + 4u * (lane >> 3);
        const v2u b1a = *(const GAS v2u*)(a.b1 + bo), b1b = *(const GAS v2u*)(a.b1 + bo + (lane < 48u ? 32u : 0u));
        const v2u xfin = *(const GAS v2u*)(a.x + fo);
        const v2u b2v = *(const GAS v2u*)(a.b2 + (fo & 511u));
        unsigned tw = __builtin_amdgcn_raw_buffer_load_b32(rs, 4096u + lane * 1048576u, 0, 16);
        unsigned tw2 = *(const GAS unsigned*)(a.out + fo);
        __builtin_amdgcn_s_setprio(2);
        STAMP(9);
        lds_wait_ge<1>(F_XN, 9);
        STAMP(10);
        const unsigned tag = LDS4(F_TAG);
        {
            v4u xf[4], af[8];
#pragma unroll
            for (int k = 0; k < 4; ++k) xf[k] = LDS16(L_XN + (n16 & 7u) * 1024u + k1 * 256u + (((4u * k + g) ^ (4u * (n16 & 3u))) << 4));
            KEEP4(xf, 0);
            lds_arrive<F_XF>();
            const unsigned ao = L_W1 + (4u * g + q4) * 64u + (((p4 >> 1) ^ (2u * (g & 1u))) << 4) + 8u * (p4 & 1u);
            lds_wait_ge<2>(F_PROG, G_W1);
            STAMP(14);
#pragma unroll
            for (int k = 0; k < 4; ++k) {
                af[2 * k] = trfrag(ao + 2048 * k, ao + 2048 * k + 1024);
                af[2 * k + 1] = trfrag((ao ^ 32u) + 2048 * k, (ao ^ 32u) + 2048 * k + 1024);
            }
            KEEP4(af, 0); KEEP4(af, 4);
            if (lane == 0) VLDS4(F_W1) = 1u;
            f32x4 e0 = {0.f, 0.f, 0.f, 0.f}, e1 = {0.f, 0.f, 0.f, 0.f};
#pragma unroll
            for (int k = 0; k < 4; ++k) { e0 = mfma16(af[2 * k], xf[k], e0); e1 = mfma16(af[2 * k + 1], xf[k], e1); }
            asm volatile("" : "+v"(cpf), "+v"(tw), "+v"(tw2));
            if (r < 28) st_sc1(rs, WS_H + (8u * c32 + 4u * (n16 >> 3) + g) * 512u + k1 * 128u + (n16 & 7u) * 16u, 0, enc4(n16 < 8 ? e0 : e1, tag));
        }
        STAMP(4);
        {
            const unsigned ha = opq(WS_H + rg * 7168u + (lane >> 3) * 512u + (lane & 7u) * 16u), hb = opq(ha + (lane < 48u ? 4096u : 0u));
            v4u H[8];
#pragma unroll
            for (int k = 0; k < 8; ++k) asm volatile("" : "=v"(H[k]));
            unsigned bad = 1;
#pragma clang loop unroll(disable)
            for (unsigned spin = 0;; ++spin) {
                unsigned pa = ha, pb = hb;
                asm volatile("" : "+v"(pa), "+v"(pb) : : "memory");
                if (bad) {
#pragma unroll
                    for (int k = 0; k < 4; ++k) { H[k] = ld_sc1(rs, pa + 128 * k, 0); H[4 + k] = ld_sc1(rs, pb + 128 * k, 0); }
                    bad = 0;
#pragma unroll
                    for (int k = 0; k < 8; ++k) bad |= bad4(H[k], tag);
                }
                if (!any_lane(bad) || spin > SPIN_CAP) break;
                __builtin_amdgcn_s_sleep(8);
            }
            STAMP(3);
            const unsigned hd = L_H + (lane & 7u) * 144u + ((lane >> 3) & 3u) * 16u + (lane >> 5) * 8u;
#pragma unroll
            for (int i = 0; i < 2; ++i) {
                const f32x4 e = (dec4(H[4 * i]) + dec4(H[4 * i + 1])) + (dec4(H[4 * i + 2]) + dec4(H[4 * i + 3]));
                const v2u bv = i ? b1b : b1a;
                float hv[4];
#pragma unroll
                for (int k = 0; k < 4; ++k) {
                    const float bb = (k & 1) ? bf_hi(bv[k >> 1]) : bf_lo(bv[k >> 1]);
                    const float t = rbf(rbf(e[k]) + bb);
                    const float u = t * fmaf(t * t, -2.f * LOG2E * 0.7978845608028654f * 0.044715f, -2.f * LOG2E * 0.7978845608028654f);
                    hv[k] = t * rcp(1.f + ex2(u));
                }
                const bool z = i && lane >= 48u;
                LDS8(hd + 64 * i) = (v2u){z ? 0u : pk_bf(hv[0], hv[1]), z ? 0u : pk_bf(hv[2], hv[3])};
            }
        }
        STAMP(5);
        lds_wait_ge<2>(F_PROG, G_W2);
        STAMP(6);
        {
            v4u hf[2], af[8];
            const unsigned row = 4u * g + q4;
#pragma unroll
            for (int k = 0; k < 2; ++k) hf[k] = LDS16(L_H + (n16 & 7u) * 144u + 16u * g + 64 * k);
#pragma unroll
            for (int t = 0; t < 4; ++t) {
                const unsigned wa = L_W1 + row * 128u + (((2u * t + (p4 >> 1)) ^ (((row >> 1) & 3u) << 1)) << 4) + 8u * (p4 & 1u);
                af[t] = trfrag(wa, wa + 2048); af[4 + t] = trfrag(wa + 4096, wa + 6144);
            }
            SB();
            f32x4 d[4];
#pragma unroll
            for (int t = 0; t < 4; ++t) { d[t] = mfma16(af[t], hf[0], (f32x4){0.f, 0.f, 0.f, 0.f}); d[t] = mfma16(af[4 + t], hf[1], d[t]); }
            const unsigned yo = WS_Y + cg8 * 4096u + (n16 & 7u) * 131072u + (n16 >> 3) * 32768u + rg * 128u + g * 16u;
            st_sc1(rs, yo, 0, enc4(n16 < 8 ? d[0] : d[1], tag));
            st_sc1(rs, yo, 65536, enc4(n16 < 8 ? d[2] : d[3], tag));
        }
        STAMP(7);
        float gt[4] = {0.f, 0.f, 0.f, 0.f};
        unsigned po = WS_Y + j * 4096u + (lane >> 2) * 128u + p4 * 16u, fin = 0;
#pragma clang loop unroll(disable)
        for (;;) {
            asm volatile("" : "+s"(fin));
            lds_wait_ge<1>(F_S, fin);
            STAMP(11);
            v4u Y0, Y1;
            asm volatile("" : "=v"(Y0), "=v"(Y1));
            unsigned bad = 1;
#pragma clang loop unroll(disable)
            for (unsigned spin = 0;; ++spin) {
                asm volatile("" : "+v"(po) : : "memory");
                if (bad) {
                    Y0 = ld_sc1(rs, po, 0); Y1 = ld_sc1(rs, po + 2048, 0);
                    bad = bad4(Y0, tag) | bad4(Y1, tag);
                }
                if (!any_lane(bad) || spin > SPIN_CAP) break;
                if (PSS) __builtin_amdgcn_s_sleep(PSS);
            }
            STAMP(12);
            const f32x4 y = dec4(Y0) + dec4(Y1);
            float v[4], o[4];
#pragma unroll
            for (int k = 0; k < 4; ++k) {
                float t = y[k];
                t += dpp<0x124>(t); t += dpp<0x128>(t);
                v[k] = rbf(x32_sum(x16_sum(t)));
                const float xx = (k & 1) ? bf_hi(xfin[k >> 1]) : bf_lo(xfin[k >> 1]);
                o[k] = xx + rbf(v[k] * gt[k]);
            }
            v2u ov = {pk_bf(o[0], o[1]), pk_bf(o[2], o[3])};
            asm volatile("" : "+v"(ov));
            if (fin) {
                if (lane < 4) *(GAS v2u*)(a.out + fo) = ov;
                break;
            }
#pragma unroll
            for (int k = 0; k < 4; ++k) {
                const float bb = (k & 1) ? bf_hi(b2v[k >> 1]) : bf_lo(b2v[k >> 1]);
                const float ya = rbf(v[k] + bb);
                gt[k] = rbf(ya * rcp(1.f + ex2(-LOG2E * ya)));
            }
            po += WS_S - WS_Y; fin = 1;
            STAMP(8);
        }
        if (j == 0 && lane == 0) __hip_atomic_store((GAS unsigned*)(a.ws + WS_EPOCH), tag, __ATOMIC_RELAXED, __HIP_MEMORY_SCOPE_AGENT);
        STAMP(13);
    }
    STAMP_END();
    (void)smem;
}

extern "C" void kernel_launch(void* const* d_in, const int* in_sizes, int n_in,
                              void* d_out, int out_size, void* d_ws, size_t ws_size,
                              hipStream_t stream) {
    Args a;
    a.x = (const u16*)d_in[0]; a.kc = (const u16*)d_in[1]; a.vc = (const u16*)d_in[2]; a.wq = (const u16*)d_in[3]; a.wo = (const u16*)d_in[4];
    a.w1 = (const u16*)d_in[6]; a.b1 = (const u16*)d_in[7]; a.w2 = (const u16*)d_in[8]; a.b2 = (const u16*)d_in[9];
    a.out = (u16*)d_out; a.ws = (unsigned char*)d_ws;
    (void)hipFuncSetAttribute((const void*)fused_decode, hipFuncAttributeMaxDynamicSharedMemorySize, L_BYTES);
    hipLaunchKernelGGL(fused_decode, dim3(NWG), dim3(NTHR), L_BYTES, stream, a);
}
```

```cpp
#include <hip/hip_runtime.h>
#include <hip/hip_bf16.h>
#include <stdint.h>

typedef unsigned short u16;
typedef unsigned v4u __attribute__((ext_vector_type(4)));
typedef unsigned v2u __attribute__((ext_vector_type(2)));
typedef float f32x4 __attribute__((ext_vector_type(4)));
typedef float f32x2 __attribute__((ext_vector_type(2)));
typedef __bf16 bf16x8 __attribute__((ext_vector_type(8)));
typedef __bf16 bf16x2 __attribute__((ext_vector_type(2)));
typedef short v4i16 __attribute__((ext_vector_type(4)));
#define GAS __attribute__((address_space(1)))
#define LAS __attribute__((address_space(3)))

#define STAMPS 0
#define WIN 7
#define IB 8
#define W1G 2
#define W2G 14
#define WOG 25
#define WOGE 25
#define QGL 9
#define QGE 12
#define HGATE 20
#define CODE_LINES 48
#define PSQ 1
#define PSH 8
#define STO 10
#define STS 10
#define FSN 8
#define WNT 1

constexpr int NWG = 256, NWAVE = 11, NTHR = NWAVE * 64;
constexpr unsigned WS_EPOCH = 0;
constexpr unsigned WS_Q = 4096;
constexpr unsigned WS_H = WS_Q + 64 * 8192;
constexpr unsigned WS_O = WS_H + 448 * 512;
constexpr unsigned WS_S = WS_O + 32 * 16384;
constexpr unsigned WS_Y = WS_S + 256 * 4096;
constexpr unsigned WS_END = WS_Y + 256 * 4096;
constexpr unsigned WS_STAMP = 8u << 20;
constexpr unsigned L_FLG = 64;
constexpr unsigned F_PROG = L_FLG, F_XN = L_FLG + 4, F_Q = L_FLG + 8, F_SC = L_FLG + 12;
constexpr unsigned F_XF = L_FLG + 20, F_S = L_FLG + 24, F_WO = L_FLG + 28;
constexpr unsigned L_Q = 128;
constexpr unsigned L_H = L_Q + 4 * 272;
constexpr unsigned L_SC = L_H + 8 * 144 + 64;
constexpr unsigned L_W1 = 6144;
constexpr unsigned L_WQ = L_W1 + 8192;
constexpr unsigned L_K = L_WQ + 16384;
constexpr unsigned L_V = L_K + 65536;
constexpr unsigned L_XN = L_V + 7 * 8192;
constexpr unsigned L_BYTES = L_V + 65536;
static_assert(L_SC % 16 == 0 && L_SC + 2048 <= L_W1 && L_BYTES <= 163840, "LDS");
static_assert(IB >= WIN && IB <= 15 && IB > 2, "loader");
constexpr unsigned OOR = 0x40000000u;
constexpr unsigned SPIN_CAP = 1u << 18;

struct Args {
    const u16* x; const u16* kc; const u16* vc; const u16* wq; const u16* wo;
    const u16* w1; const u16* b1; const u16* w2; const u16* b2; u16* out; unsigned char* ws;
};

__device__ __forceinline__ float bf_lo(unsigned u) { return __uint_as_float(u << 16); }
__device__ __forceinline__ float bf_hi(unsigned u) { return __uint_as_float(u & 0xffff0000u); }
__device__ __forceinline__ unsigned pk_bf(float lo, float hi) { bf16x2 v = {(__bf16)lo, (__bf16)hi}; return __builtin_bit_cast(unsigned, v); }
__device__ __forceinline__ float rbf(float f) { return (float)(__bf16)f; }
__device__ __forceinline__ float rcp(float f) { return __builtin_amdgcn_rcpf(f); }
__device__ __forceinline__ float ex2(float f) { return __builtin_amdgcn_exp2f(f); }
__device__ __forceinline__ float lg2(float f) { return __builtin_amdgcn_logf(f); }
__device__ __forceinline__ f32x4 mfma16(v4u a, v4u b, f32x4 c) {
    return __builtin_amdgcn_mfma_f32_16x16x32_bf16(__builtin_bit_cast(bf16x8, a), __builtin_bit_cast(bf16x8, b), c, 0, 0, 0);
}
__device__ __forceinline__ v2u trread(unsigned p) { return __builtin_bit_cast(v2u, __builtin_amdgcn_ds_read_tr16_b64_v4i16((LAS v4i16*)(size_t)p)); }
__device__ __forceinline__ v4u trfrag(unsigned p0, unsigned p1) { v2u a = trread(p0), b = trread(p1); return (v4u){a.x, a.y, b.x, b.y}; }
template <int CTRL> __device__ __forceinline__ unsigned dppu(unsigned x) { return (unsigned)__builtin_amdgcn_mov_dpp((int)x, CTRL, 0xf, 0xf, true); }
template <int CTRL> __device__ __forceinline__ float dpp(float x) { return __uint_as_float(dppu<CTRL>(__float_as_uint(x))); }
template <int CTRL> __device__ __forceinline__ v4u dpp4(v4u x) { return (v4u){dppu<CTRL>(x.x), dppu<CTRL>(x.y), dppu<CTRL>(x.z), dppu<CTRL>(x.w)}; }
__device__ __forceinline__ float x16_sum(float x) { auto s = __builtin_amdgcn_permlane16_swap(__float_as_uint(x), __float_as_uint(x), false, false); return __uint_as_float(s[0]) + __uint_as_float(s[1]); }
__device__ __forceinline__ float x32_sum(float x) { auto s = __builtin_amdgcn_permlane32_swap(__float_as_uint(x), __float_as_uint(x), false, false); return __uint_as_float(s[0]) + __uint_as_float(s[1]); }
__device__ __forceinline__ float x16_max(float x) { auto s = __builtin_amdgcn_permlane16_swap(__float_as_uint(x), __float_as_uint(x), false, false); return fmaxf(__uint_as_float(s[0]), __uint_as_float(s[1])); }
__device__ __forceinline__ float x32_max(float x) { auto s = __builtin_amdgcn_permlane32_swap(__float_as_uint(x), __float_as_uint(x), false, false); return fmaxf(__uint_as_float(s[0]), __uint_as_float(s[1])); }
__device__ __forceinline__ float wave_sum(float x) {
    x += dpp<0xB1>(x); x += dpp<0x4E>(x); x += dpp<0x141>(x); x += dpp<0x140>(x);
    return x32_sum(x16_sum(x));
}
__device__ __forceinline__ v4u ld_sc1(__amdgpu_buffer_rsrc_t rs, unsigned off, unsigned so) { return __builtin_amdgcn_raw_buffer_load_b128(rs, off, so, 16); }
__device__ __forceinline__ void st_sc1(__amdgpu_buffer_rsrc_t rs, unsigned off, unsigned so, v4u v) { __builtin_amdgcn_raw_buffer_store_b128(v, rs, off, so, 16); }
__device__ __forceinline__ unsigned opq(unsigned v) { asm("" : "+v"(v)); return v; }
#define CFENCE() asm volatile("" ::: "memory")
#define LDS16(off) (*(LAS v4u*)(size_t)(off))
#define LDS8(off) (*(LAS v2u*)(size_t)(off))
#define LDS4(off) (*(LAS unsigned*)(size_t)(off))
#define VLDS4(off) (*(volatile LAS unsigned*)(size_t)(off))
#define STAMP(i) do { } while (0)
#define STAMP_END() do { } while (0)

template <int NT> __device__ __forceinline__ void glds4(const void* base, unsigned dst, unsigned v0, unsigned v1, unsigned v2, unsigned v3) {
    if (NT) asm volatile("s_mov_b32 m0, %1\n\ts_nop 0\n\tglobal_load_lds_dwordx4 %2, %0 nt\n\tglobal_load_lds_dwordx4 %3, %0 offset:1024 nt\n\t"
                         "global_load_lds_dwordx4 %4, %0 offset:2048 nt\n\tglobal_load_lds_dwordx4 %5, %0 offset:3072 nt"
                         : : "s"(base), "s"(dst), "v"(v0), "v"(v1), "v"(v2), "v"(v3) : "memory", "m0");
    else    asm volatile("s_mov_b32 m0, %1\n\ts_nop 0\n\tglobal_load_lds_dwordx4 %2, %0\n\tglobal_load_lds_dwordx4 %3, %0 offset:1024\n\t"
                         "global_load_lds_dwordx4 %4, %0 offset:2048\n\tglobal_load_lds_dwordx4 %5, %0 offset:3072"
                         : : "s"(base), "s"(dst), "v"(v0), "v"(v1), "v"(v2), "v"(v3) : "memory", "m0");
}
template <int NT> __device__ __forceinline__ void glds1(const void* base, unsigned dst, unsigned v0) {
    if (NT) asm volatile("s_mov_b32 m0, %1\n\ts_nop 0\n\tglobal_load_lds_dwordx4 %2, %0 nt" : : "s"(base), "s"(dst), "v"(v0) : "memory", "m0");
    else    asm volatile("s_mov_b32 m0, %1\n\ts_nop 0\n\tglobal_load_lds_dwordx4 %2, %0" : : "s"(base), "s"(dst), "v"(v0) : "memory", "m0");
}
template <int SLEEP> __device__ __forceinline__ void lds_wait_ge(unsigned off, unsigned need) {
#pragma clang loop unroll(disable)
    for (unsigned spin = 0; spin < SPIN_CAP; ++spin) {
        if ((unsigned)__builtin_amdgcn_readfirstlane(VLDS4(off)) >= need) break;
        if (SLEEP) __builtin_amdgcn_s_sleep(SLEEP);
    }
    CFENCE();
}
template <unsigned OFF> __device__ __forceinline__ void lds_arrive() {
    asm volatile("s_mov_b64 exec, 1\n\tds_add_u32 %0, %1 offset:%2\n\ts_mov_b64 exec, -1" : : "v"(0u), "v"(1u), "n"(OFF) : "memory");
}
#define KEEP4(a, i) asm volatile("" : "+v"(a[i]), "+v"(a[i + 1]), "+v"(a[i + 2]), "+v"(a[i + 3]) : : "memory")
#define SB() __builtin_amdgcn_sched_barrier(0)
#define VMWAIT(n) asm volatile("s_waitcnt vmcnt(%0)" : : "n"(n) : "memory")
#define PROG() do { VMWAIT(4 * (WIN - 1)); ++prog; VLDS4(F_PROG) = prog; if (STAMPS && prog == 16) STAMP(2); } while (0)
__device__ __forceinline__ v4u enc4(f32x4 v, unsigned tag) {
    v4u o;
#pragma unroll
    for (int k = 0; k < 4; ++k) o[k] = ((__float_as_uint(v[k]) + 0x80u) & 0xffffff00u) | ((tag >> (8 * k)) & 0xffu);
    return o;
}
__device__ __forceinline__ unsigned bad4(v4u w, unsigned tag) {
    return (__builtin_amdgcn_perm(w.y, w.x, 0x0c0c0400u) | __builtin_amdgcn_perm(w.w, w.z, 0x04000c0cu)) ^ tag;
}
__device__ __forceinline__ f32x4 dec4(v4u w) {
    return (f32x4){__uint_as_float(w.x & 0xffffff00u), __uint_as_float(w.y & 0xffffff00u), __uint_as_float(w.z & 0xffffff00u), __uint_as_float(w.w & 0xffffff00u)};
}
__device__ __forceinline__ bool any_lane(unsigned v) { return __builtin_amdgcn_ballot_w64(v != 0u) != 0ull; }

__global__ void __launch_bounds__(NTHR, 1) fused_decode(Args a) {
    extern __shared__ __attribute__((aligned(16))) unsigned char smem[];
    unsigned long long pc;
    asm volatile("s_getpc_b64 %0" : "=s"(pc));
    const unsigned tid = threadIdx.x, lane = tid & 63u, wave = __builtin_amdgcn_readfirstlane(tid >> 6);
    const unsigned j = blockIdx.x, xcd = j & 7u, r = j >> 3;
    const unsigned n16 = lane & 15u, g = lane >> 4, q4 = n16 >> 2, p4 = lane & 3u;
    constexpr float LOG2E = 1.4426950408889634f;
    STAMP(0);
    const unsigned sw = ((lane & 7u) ^ (((lane >> 4) & 3u) << 1)) << 4;
    unsigned cpf = 0, tag = 0;
    v4u xv = {0u, 0u, 0u, 0u};
    if (wave != 0) {
        if (wave == 1) {
            const unsigned q0 = (lane >> 3) * 8192u + sw;
            const unsigned char* qb = (const unsigned char*)a.wq + (r >> 3) * 1048576u + (xcd ^ 4u) * 1024u + (r & 7u) * 128u;
#pragma clang loop unroll(disable)
            for (unsigned k = 0; k < 4; ++k) glds4<WNT>(qb + k * 262144u, L_WQ + k * 4096u, q0, q0 + 64512u, q0 + 2u * 64512u, q0 + 3u * 64512u);
        }
        if (wave == 2) {
            if (r < 8) cpf = *(const GAS unsigned*)((pc & ~255ull) + ((r & 1u) * CODE_LINES + (lane < CODE_LINES ? lane : 0u)) * 128u);
            if (lane < 16) LDS4(L_FLG + lane * 4u) = 0u;
            asm volatile("s_waitcnt lgkmcnt(0)" ::: "memory");
        }
        if (wave >= 3) xv = *(const GAS v4u*)((const unsigned char*)a.x + (wave - 3u) * 1024u + lane * 16u);
        tag = *(const __attribute__((address_space(4))) unsigned*)(a.ws + WS_EPOCH) + 0x9E3779B1u;
        __builtin_amdgcn_s_barrier();
        CFENCE();
    }
    const unsigned xp = (xcd & 4u) | ((xcd - 1u) & 3u);
    const unsigned hh = (r >> 2) != xcd ? xp : (xcd ^ 2u);
    const unsigned cc = (r >> 2) != xcd ? (r >> 2) : xp;
    const unsigned sub = r & 3u;
    const unsigned c32 = 28u * (xcd >> 2) + (r < 28u ? r : 27u), k1 = xcd & 3u;
    const unsigned rg = 16u * (1u - (xcd >> 2)) + (r >> 1), cg8 = ((xcd - 1u) & 3u) + 4u * (r & 1u);

    if (wave == 0) {
        __builtin_amdgcn_s_setprio(3);
        const size_t kvoff = ((size_t)((r >> 2) * 8u + xcd) * 4096u + 3072u + 256u * (r & 3u)) * 256u;
        const unsigned rot = r & 15u;
        const unsigned char* kb = (const unsigned char*)a.kc + kvoff;
        unsigned o[4];
#pragma unroll
        for (int i = 0; i < 4; ++i) o[i] = g * 256u + ((n16 ^ g ^ (4u * i)) << 4);
        KEEP4(o, 0);
        unsigned prog = IB - WIN;
#pragma clang loop unroll(disable)
        for (unsigned c = 0; c < 32; ++c) {
            if (c == 2) { __builtin_amdgcn_s_barrier(); STAMP(3); }
            if (c == 16) {
                o[0] = o[2] = g * 256u + ((n16 ^ (g << 1)) << 4);
                o[1] = o[3] = g * 256u + ((n16 ^ ((4u + g) << 1)) << 4);
                kb = (const unsigned char*)a.vc + kvoff;
            }
            if (c >= IB) PROG();
            if (c == 30) lds_wait_ge<1>(F_XF, 2);
            glds4<1>(kb + ((c + rot) & 15u) * 4096u, L_K + c * 4096u, o[0], o[1], o[2], o[3]);
        }
        STAMP(4);
#pragma unroll
        for (int k = WIN - 1; k >= 0; --k) { VMWAIT(4 * k); VLDS4(F_PROG) = 32 - k; }
        STAMP(5);
    }
    const __amdgpu_buffer_rsrc_t rs = __builtin_amdgcn_make_buffer_rsrc(a.ws, 0, WS_END, 0x00020000);
    unsigned wa[4];
    {
        const unsigned row = 4u * g + q4;
#pragma unroll
        for (int k = 0; k < 4; ++k) wa[k] = L_WQ + row * 128u + (((2u * k + (p4 >> 1)) ^ (((row >> 1) & 3u) << 1)) << 4) + 8u * (p4 & 1u);
    }
    if (wave >= 3) {
        const unsigned w = wave - 3u;
        STAMP(13);
        {
            float v[8];
#pragma unroll
            for (int k = 0; k < 4; ++k) { v[2 * k] = bf_lo(xv[k]); v[2 * k + 1] = bf_hi(xv[k]); }
            const float s = ((v[0] + v[1]) + (v[2] + v[3])) + ((v[4] + v[5]) + (v[6] + v[7]));
            const float mean = wave_sum(s) * (1.f / 512.f);
            float q = 0.f;
#pragma unroll
            for (int k = 0; k < 8; ++k) { const float d = v[k] - mean; q = fmaf(d, d, q); }
            const float var = rbf(wave_sum(q) * (1.f / 512.f));
            const float mb = rbf(mean);
            const float den = rbf(sqrtf(rbf(var + 1e-5f)));
            const float rc = rcp(den);
            float o[8];
#pragma unroll
            for (int k = 0; k < 8; ++k) { const float t = rbf(v[k] - mb); const float q0 = t * rc; const float e = fmaf(-q0, den, t); o[k] = fmaf(e, rc, q0); }
            const unsigned xo = L_XN + w * 1024u + (((lane & ~3u) + 2u * (lane & 1u)) ^ (4u * (w & 3u))) * 16u + (lane & 2u) * 4u;
            LDS8(xo) = (v2u){pk_bf(o[0], o[1]), pk_bf(o[2], o[3])};
            LDS8(xo + 16) = (v2u){pk_bf(o[4], o[5]), pk_bf(o[6], o[7])};
        }
        lds_arrive<F_XN>();
        STAMP(2);
        unsigned ka[4];
#pragma unroll
        for (int t = 0; t < 4; ++t) ka[t] = L_K + w * 8192u + n16 * 256u + (((4u * t + g) ^ n16) << 4);
        KEEP4(ka, 0);
        const unsigned va = opq(L_V + (4u * g + q4) * 256u + (((2u * w + (p4 >> 1)) ^ (((4u * g + q4) & 7u) << 1)) << 4) + 8u * (p4 & 1u));
        const unsigned oo = opq(WS_O + ((xcd * 4u + n16) * 32u + r) * 512u + w * 64u + g * 16u);
        const unsigned qo = opq(WS_Q + ((r >> 2) * 8u + xcd) * 8192u + w * 1024u + lane * 16u);
        const unsigned kneed = 2u * w + 2u;
        lds_wait_ge<1>(F_PROG, (xcd & 4u) ? QGL : QGE);
        v4u kf[8];
        {
            v4u Q;
            asm volatile("" : "=v"(Q));
            unsigned bad = 1;
            bool kd = false;
#pragma clang loop unroll(disable)
            for (unsigned spin = 0;; ++spin) {
                CFENCE();
                if (bad) Q = ld_sc1(rs, qo, 0);
                if (!kd && (unsigned)__builtin_amdgcn_readfirstlane(VLDS4(F_PROG)) >= kneed) {
                    CFENCE();
#pragma unroll
                    for (int i = 0; i < 8; ++i) kf[i] = LDS16(ka[i & 3] + 4096 * (i >> 2));
                    KEEP4(kf, 0); KEEP4(kf, 4);
                    kd = true;
                    STAMP(3);
                }
                if (bad) bad = bad4(Q, tag);
                if (!any_lane(bad) || spin > SPIN_CAP) break;
                if (PSQ) __builtin_amdgcn_s_sleep(PSQ);
            }
            STAMP(10);
            const f32x4 qv = dec4(Q);
            float qs[4];
#pragma unroll
            for (int k = 0; k < 4; ++k) qs[k] = x32_sum(x16_sum(qv[k]));
            if (lane < 16) LDS8(L_Q + (w >> 1) * 272u + (w & 1u) * 128u + lane * 8u) = (v2u){pk_bf(qs[0], qs[1]), pk_bf(qs[2], qs[3])};
            lds_arrive<F_Q>();
            if (!kd) {
                lds_wait_ge<0>(F_PROG, kneed);
#pragma unroll
                for (int i = 0; i < 8; ++i) kf[i] = LDS16(ka[i & 3] + 4096 * (i >> 2));
                KEEP4(kf, 0); KEEP4(kf, 4);
                STAMP(3);
            }
        }
        lds_wait_ge<0>(F_Q, 8u);
        STAMP(5);
        {
            v4u qf[4];
#pragma unroll
            for (int t = 0; t < 4; ++t) qf[t] = LDS16(L_Q + (n16 & 3u) * 272u + 16u * g + 64 * t);
            SB();
            f32x4 s0 = {0.f, 0.f, 0.f, 0.f}, s1 = {0.f, 0.f, 0.f, 0.f};
#pragma unroll
            for (int t = 0; t < 4; ++t) { s0 = mfma16(kf[t], qf[t], s0); s1 = mfma16(kf[4 + t], qf[t], s1); }
            float sc[8];
#pragma unroll
            for (int k = 0; k < 4; ++k) { sc[k] = rbf(s0[k]) * 0.08838834764831845f; sc[4 + k] = rbf(s1[k]) * 0.08838834764831845f; }
            if (n16 < 4) LDS16(L_SC + w * 256u + n16 * 64u + g * 16u) = (v4u){pk_bf(sc[0], sc[1]), pk_bf(sc[2], sc[3]), pk_bf(sc[4], sc[5]), pk_bf(sc[6], sc[7])};
        }
        lds_arrive<F_SC>();
        STAMP(6);
        lds_wait_ge<0>(F_SC, 8u);
        STAMP(7);
        v4u P[8]; float lse, inv;
        {
            const unsigned sa = L_SC + q4 * 512u + (n16 & 3u) * 64u + g * 16u;
            const v4u A0 = LDS16(sa), A1 = LDS16(sa + 256);
            float sc[16];
#pragma unroll
            for (int k = 0; k < 4; ++k) { sc[2 * k] = bf_lo(A0[k]); sc[2 * k + 1] = bf_hi(A0[k]); sc[8 + 2 * k] = bf_lo(A1[k]); sc[9 + 2 * k] = bf_hi(A1[k]); }
            float m = sc[0];
#pragma unroll
            for (int k = 1; k < 16; ++k) m = fmaxf(m, sc[k]);
            m = fmaxf(m, dpp<0x124>(m)); m = fmaxf(m, dpp<0x128>(m));
            m = x32_max(x16_max(m));
            const float ml = m * LOG2E;
            float p[16];
#pragma unroll
            for (int k = 0; k < 16; ++k) p[k] = ex2(fmaf(sc[k], LOG2E, -ml));
            P[0] = (v4u){pk_bf(p[0], p[1]), pk_bf(p[2], p[3]), pk_bf(p[4], p[5]), pk_bf(p[6], p[7])};
            P[1] = (v4u){pk_bf(p[8], p[9]), pk_bf(p[10], p[11]), pk_bf(p[12], p[13]), pk_bf(p[14], p[15])};
            float l = (((p[0] + p[1]) + (p[2] + p[3])) + ((p[4] + p[5]) + (p[6] + p[7]))) + (((p[8] + p[9]) + (p[10] + p[11])) + ((p[12] + p[13]) + (p[14] + p[15])));
            l += dpp<0x128>(l); l += dpp<0x124>(l);
            l = x32_sum(x16_sum(l));
            lse = ml + lg2(l); inv = rcp(l);
            P[2] = dpp4<0x12C>(P[0]); P[3] = dpp4<0x12C>(P[1]);
            P[4] = dpp4<0x128>(P[0]); P[5] = dpp4<0x128>(P[1]);
            P[6] = dpp4<0x124>(P[0]); P[7] = dpp4<0x124>(P[1]);
            KEEP4(P, 0); KEEP4(P, 4);
            asm volatile("" : "+v"(lse), "+v"(inv));
        }
        STAMP(8);
        {
            f32x4 d0 = {0.f, 0.f, 0.f, 0.f}, d1 = {0.f, 0.f, 0.f, 0.f};
            v4u vf[4];
            lds_wait_ge<2>(F_PROG, 24);
#pragma unroll
            for (int t = 0; t < 4; ++t) vf[t] = trfrag(va + 8192 * t, va + 8192 * t + 4096);
            d0 = mfma16(vf[0], P[0], d0); d1 = mfma16(vf[1], P[1], d1); d0 = mfma16(vf[2], P[2], d0); d1 = mfma16(vf[3], P[3], d1);
            asm volatile("" : "+v"(d0), "+v"(d1));
            lds_wait_ge<1>(F_PROG, 30);
#pragma unroll
            for (int t = 0; t < 3; ++t) vf[t] = trfrag(va + 8192 * (4 + t), va + 8192 * (4 + t) + 4096);
            d0 = mfma16(vf[0], P[4], d0); d1 = mfma16(vf[1], P[5], d1); d0 = mfma16(vf[2], P[6], d0);
            d0 += d1;
            asm volatile("" : "+v"(d0));
            STAMP(4);
            lds_wait_ge<0>(F_PROG, 32);
            vf[3] = trfrag(va + 8192 * 7, va + 8192 * 7 + 4096);
            d0 = mfma16(vf[3], P[7], d0);
            v4u ov = {pk_bf(d0[0] * inv, d0[1] * inv), pk_bf(d0[2] * inv, d0[3] * inv), __float_as_uint(lse), tag};
            if (n16 < 4u) st_sc1(rs, oo, 0, ov);
        }
        CFENCE();
        STAMP(9);
        {
            const unsigned po = opq(WS_O + (hh * 4u + sub) * 16384u + w * 2048u + n16 * 128u + g * 16u);
            const unsigned sto = WS_S + cc * 4096u + w * 131072u + n16 * 32768u + (hh * 4u + sub) * 128u + g * 16u;
            v4u A0, A1, B0, B1, af[16];
            unsigned R[5], pa = po;
            asm volatile("" : "=v"(R[0]), "=v"(R[1]), "=v"(R[2]), "=v"(R[3]), "=v"(R[4]));
            bool wd = false;
            A0 = ld_sc1(rs, pa, 0); A1 = ld_sc1(rs, pa + 64, 0);
            CFENCE();
            if (STO) __builtin_amdgcn_s_sleep(STO);
#define OCHK(X0, X1) { const bool ok = X0.w == tag && X1.w == tag && pa == po; R[0] = ok ? X0.x : R[0]; R[1] = ok ? X0.y : R[1]; R[2] = ok ? X1.x : R[2]; R[3] = ok ? X1.y : R[3]; R[4] = ok ? X0.z : R[4]; pa = ok ? OOR : pa; }
#pragma clang loop unroll(disable)
            for (unsigned spin = 0;; ++spin) {
                B0 = ld_sc1(rs, pa, 0); B1 = ld_sc1(rs, pa + 64, 0);
                CFENCE();
                if (!wd && (unsigned)__builtin_amdgcn_readfirstlane(VLDS4(F_WO)) != 0u) {
                    CFENCE();
#pragma unroll
                    for (int i = 0; i < 16; ++i) af[i] = trfrag(wa[i & 3] + 4096 * (i >> 2), wa[i & 3] + 4096 * (i >> 2) + 2048);
                    KEEP4(af, 0); KEEP4(af, 4); KEEP4(af, 8); KEEP4(af, 12);
                    wd = true;
                    STAMP(11);
                }
                OCHK(A0, A1);
                if (!any_lane(pa ^ OOR)) break;
                A0 = ld_sc1(rs, pa, 0); A1 = ld_sc1(rs, pa + 64, 0);
                CFENCE();
                OCHK(B0, B1);
                if (!any_lane(pa ^ OOR) || spin > SPIN_CAP) break;
            }
            STAMP(12);
            const float l = __uint_as_float(R[4]);
            float M = fmaxf(l, dpp<0x124>(l)); M = fmaxf(M, dpp<0x128>(M));
            const float e = ex2(l - M);
            float tot = e + dpp<0x124>(e); tot += dpp<0x128>(tot);
            const float wt = e * rcp(tot);
            v4u ob;
#pragma unroll
            for (int k = 0; k < 4; ++k) {
                float lo = bf_lo(R[k]) * wt, hi = bf_hi(R[k]) * wt;
                lo += dpp<0x124>(lo); hi += dpp<0x124>(hi);
                lo += dpp<0x128>(lo); hi += dpp<0x128>(hi);
                ob[k] = pk_bf(lo, hi);
            }
            const v4u Bf[4] = {dpp4<0x00>(ob), dpp4<0x55>(ob), dpp4<0xAA>(ob), dpp4<0xFF>(ob)};
            if (!wd) {
                lds_wait_ge<0>(F_WO, 1);
#pragma unroll
                for (int i = 0; i < 16; ++i) af[i] = trfrag(wa[i & 3] + 4096 * (i >> 2), wa[i & 3] + 4096 * (i >> 2) + 2048);
                KEEP4(af, 0); KEEP4(af, 4); KEEP4(af, 8); KEEP4(af, 12);
                STAMP(11);
            }
            f32x4 d[4];
#pragma unroll
            for (int u = 0; u < 4; ++u) d[u] = (f32x4){0.f, 0.f, 0.f, 0.f};
#pragma unroll
            for (int t = 0; t < 4; ++t)
#pragma unroll
                for (int u = 0; u < 4; ++u) d[u] = mfma16(af[4 * t + u], Bf[t], d[u]);
            const f32x4 v = (n16 & 2u) ? ((n16 & 1u) ? d[3] : d[2]) : ((n16 & 1u) ? d[1] : d[0]);
            if (n16 < 4u) st_sc1(rs, sto, 0, enc4(v, tag));
            CFENCE();
            lds_arrive<F_S>();
            STAMP(14);
            asm volatile("" : : "v"(A0), "v"(A1), "v"(B0), "v"(B1));
        }
    }
    if (wave == 1) {
        __builtin_amdgcn_s_setprio(2);
        STAMP(9);
        KEEP4(wa, 0);
        v4u af[16];
        VMWAIT(0);
        STAMP(14);
#pragma unroll
        for (int i = 0; i < 16; ++i) af[i] = trfrag(wa[i & 3] + 4096 * (i >> 2), wa[i & 3] + 4096 * (i >> 2) + 2048);
        KEEP4(af, 0); KEEP4(af, 4); KEEP4(af, 8); KEEP4(af, 12);
        STAMP(15);
        lds_wait_ge<0>(F_XN, 8u);
        STAMP(10);
        v4u xf[4];
#pragma unroll
        for (int k = 0; k < 4; ++k) xf[k] = LDS16(L_XN + (n16 & 7u) * 1024u + (r >> 3) * 256u + (((4u * k + g) ^ (4u * (n16 & 3u))) << 4));
        SB();
        f32x4 d[4];
#pragma unroll
        for (int t = 0; t < 4; ++t) d[t] = (f32x4){0.f, 0.f, 0.f, 0.f};
#pragma unroll
        for (int k = 0; k < 4; ++k)
#pragma unroll
            for (int t = 0; t < 4; ++t) d[t] = mfma16(af[4 * k + t], xf[k], d[t]);
        const unsigned qo = opq(WS_Q + (xcd ^ 4u) * 8192u + (n16 & 7u) * 65536u + (r & 7u) * 1024u + (r >> 3) * 256u + (n16 >> 3) * 64u + g * 16u);
        st_sc1(rs, qo, 0, enc4(n16 < 8 ? d[0] : d[1], tag));
        st_sc1(rs, qo + 128, 0, enc4(n16 < 8 ? d[2] : d[3], tag));
        CFENCE();
        lds_arrive<F_XF>();
        STAMP(4);
        const unsigned w0 = (lane >> 3) * 1024u + sw;
        const unsigned char* wb = (const unsigned char*)a.wo + (4u * hh + sub) * 131072u + 128u * cc;
        lds_wait_ge<2>(F_PROG, (xcd & 4u) ? WOG : WOGE);
#pragma clang loop unroll(disable)
        for (unsigned c = 0; c < 4; ++c) glds4<WNT>(wb + c * 32768u, L_WQ + c * 4096u, w0, w0 + 7168u, w0 + 2u * 7168u, w0 + 3u * 7168u);
        STAMP(5);
        VMWAIT(0);
        VLDS4(F_WO) = 1u;
        STAMP(6);
    }
    if (wave == 2) {
        const unsigned fo = (r >> 2) * 512u + (xcd * 4u + sub) * 16u + 4u * p4;
        const unsigned bo = 56u * rg + 4u * (lane >> 3);
        const v2u b1a = *(const GAS v2u*)(a.b1 + bo), b1b = *(const GAS v2u*)(a.b1 + bo + (lane < 48u ? 32u : 0u));
        const v2u xfin = *(const GAS v2u*)(a.x + fo);
        const v2u b2v = *(const GAS v2u*)(a.b2 + (fo & 511u));
        unsigned tw = __builtin_amdgcn_raw_buffer_load_b32(rs, 4096u + j * 128u + lane * 1048576u, 0, 16);
        unsigned tw2 = *(const GAS unsigned*)(a.out + fo);
        __builtin_amdgcn_s_setprio(2);
        STAMP(9);
        {
            const unsigned u0 = (lane >> 2) * 3584u + ((p4 ^ (((lane >> 4) & 1u) << 1)) << 4);
            const unsigned char* ub = (const unsigned char*)a.w1 + k1 * 458752u + c32 * 64u;
            if (W1G) lds_wait_ge<1>(F_PROG, W1G);
#pragma unroll
            for (unsigned k = 0; k < 2; ++k) {
                const unsigned kk = (k ^ r) & 1u;
                glds4<WNT>(ub + kk * 229376u, L_W1 + kk * 4096u, u0, u0 + 56320u, u0 + 2u * 56320u, u0 + 3u * 56320u);
            }
        }
        lds_wait_ge<1>(F_XN, 8);
        STAMP(10);
        {
            v4u xf[4], af[8];
#pragma unroll
            for (int k = 0; k < 4; ++k) xf[k] = LDS16(L_XN + (n16 & 7u) * 1024u + k1 * 256u + (((4u * k + g) ^ (4u * (n16 & 3u))) << 4));
            KEEP4(xf, 0);
            lds_arrive<F_XF>();
            const unsigned ao = L_W1 + (4u * g + q4) * 64u + (((p4 >> 1) ^ (2u * (g & 1u))) << 4) + 8u * (p4 & 1u);
            VMWAIT(0);
            STAMP(14);
#pragma unroll
            for (int k = 0; k < 4; ++k) {
                af[2 * k] = trfrag(ao + 2048 * k, ao + 2048 * k + 1024);
                af[2 * k + 1] = trfrag((ao ^ 32u) + 2048 * k, (ao ^ 32u) + 2048 * k + 1024);
            }
            KEEP4(af, 0); KEEP4(af, 4);
            f32x4 e0 = {0.f, 0.f, 0.f, 0.f}, e1 = {0.f, 0.f, 0.f, 0.f};
#pragma unroll
            for (int k = 0; k < 4; ++k) { e0 = mfma16(af[2 * k], xf[k], e0); e1 = mfma16(af[2 * k + 1], xf[k], e1); }
            asm volatile("" : "+v"(cpf), "+v"(tw), "+v"(tw2));
            if (r < 28) st_sc1(rs, WS_H + (8u * c32 + 4u * (n16 >> 3) + g) * 512u + k1 * 128u + (n16 & 7u) * 16u, 0, enc4(n16 < 8 ? e0 : e1, tag));
        }
        STAMP(4);
        {
            const unsigned w0 = (lane >> 3) * 1024u + sw;
            const unsigned char* tb = (const unsigned char*)a.w2 + rg * 57344u + cg8 * 128u;
            lds_wait_ge<2>(F_PROG, W2G);
            glds4<WNT>(tb, L_W1, w0, w0 + 7168u, w0 + 2u * 7168u, w0 + 3u * 7168u);
            glds4<WNT>(tb + 32768, L_W1 + 4096, w0, w0 + 7168u, w0 + 2u * 7168u, w0 + 2u * 7168u - 1024u);
        }
        {
            const unsigned ha = opq(WS_H + rg * 7168u + (lane >> 3) * 512u + (lane & 7u) * 16u), hb = opq(ha + (lane < 48u ? 4096u : 0u));
            lds_wait_ge<2>(F_PROG, HGATE);
            v4u H[8];
#pragma unroll
            for (int k = 0; k < 8; ++k) asm volatile("" : "=v"(H[k]));
            unsigned bad = 1;
#pragma clang loop unroll(disable)
            for (unsigned spin = 0;; ++spin) {
                unsigned pa = ha, pb = hb;
                asm volatile("" : "+v"(pa), "+v"(pb) : : "memory");
                if (bad) {
#pragma unroll
                    for (int k = 0; k < 4; ++k) { H[k] = ld_sc1(rs, pa + 128 * k, 0); H[4 + k] = ld_sc1(rs, pb + 128 * k, 0); }
                    bad = 0;
#pragma unroll
                    for (int k = 0; k < 8; ++k) bad |= bad4(H[k], tag);
                }
                if (!any_lane(bad) || spin > SPIN_CAP) break;
                __builtin_amdgcn_s_sleep(PSH);
            }
            STAMP(3);
            const unsigned hd = L_H + (lane & 7u) * 144u + ((lane >> 3) & 3u) * 16u + (lane >> 5) * 8u;
#pragma unroll
            for (int i = 0; i < 2; ++i) {
                const f32x4 e = (dec4(H[4 * i]) + dec4(H[4 * i + 1])) + (dec4(H[4 * i + 2]) + dec4(H[4 * i + 3]));
                const v2u bv = i ? b1b : b1a;
                float hv[4];
#pragma unroll
                for (int k = 0; k < 4; ++k) {
                    const float bb = (k & 1) ? bf_hi(bv[k >> 1]) : bf_lo(bv[k >> 1]);
                    const float t = rbf(rbf(e[k]) + bb);
                    const float u = t * fmaf(t * t, -2.f * LOG2E * 0.7978845608028654f * 0.044715f, -2.f * LOG2E * 0.7978845608028654f);
                    hv[k] = t * rcp(1.f + ex2(u));
                }
                const bool z = i && lane >= 48u;
                LDS8(hd + 64 * i) = (v2u){z ? 0u : pk_bf(hv[0], hv[1]), z ? 0u : pk_bf(hv[2], hv[3])};
            }
        }
        STAMP(5);
        VMWAIT(0);
        STAMP(6);
        {
            v4u hf[2], af[8];
            const unsigned row = 4u * g + q4;
#pragma unroll
            for (int k = 0; k < 2; ++k) hf[k] = LDS16(L_H + (n16 & 7u) * 144u + 16u * g + 64 * k);
#pragma unroll
            for (int t = 0; t < 4; ++t) {
                const unsigned wb = L_W1 + row * 128u + (((2u * t + (p4 >> 1)) ^ (((row >> 1) & 3u) << 1)) << 4) + 8u * (p4 & 1u);
                af[t] = trfrag(wb, wb + 2048); af[4 + t] = trfrag(wb + 4096, wb + 6144);
            }
            SB();
            f32x4 d[4];
#pragma unroll
            for (int t = 0; t < 4; ++t) { d[t] = mfma16(af[t], hf[0], (f32x4){0.f, 0.f, 0.f, 0.f}); d[t] = mfma16(af[4 + t], hf[1], d[t]); }
            const unsigned yo = WS_Y + cg8 * 4096u + (n16 & 7u) * 131072u + (n16 >> 3) * 32768u + rg * 128u + g * 16u;
            st_sc1(rs, yo, 0, enc4(n16 < 8 ? d[0] : d[1], tag));
            st_sc1(rs, yo, 65536, enc4(n16 < 8 ? d[2] : d[3], tag));
        }
        STAMP(7);
        float gt[4] = {0.f, 0.f, 0.f, 0.f};
        unsigned po = WS_Y + j * 4096u + (lane >> 2) * 128u + p4 * 16u, fin = 0;
#pragma clang loop unroll(disable)
        for (;;) {
            asm volatile("" : "+s"(fin));
            lds_wait_ge<1>(fin ? F_S : F_PROG, fin ? FSN : 32u);
            STAMP(11);
            v4u A0, A1, B0, B1;
            f32x4 y;
            asm volatile("" : "=v"(y), "+v"(po) : : "memory");
            unsigned pa = po;
            A0 = ld_sc1(rs, pa, 0); A1 = ld_sc1(rs, pa + 2048, 0);
            CFENCE();
            if (STS) __builtin_amdgcn_s_sleep(STS);
#define SCHK(X0, X1) { const bool ok = (bad4(X0, tag) | bad4(X1, tag)) == 0u && pa == po; const f32x4 t = dec4(X0) + dec4(X1); y = ok ? t : y; pa = ok ? OOR : pa; }
#pragma clang loop unroll(disable)
            for (unsigned spin = 0;; ++spin) {
                B0 = ld_sc1(rs, pa, 0); B1 = ld_sc1(rs, pa + 2048, 0);
                CFENCE();
                SCHK(A0, A1);
                if (!any_lane(pa ^ OOR)) break;
                A0 = ld_sc1(rs, pa, 0); A1 = ld_sc1(rs, pa + 2048, 0);
                CFENCE();
                SCHK(B0, B1);
                if (!any_lane(pa ^ OOR) || spin > SPIN_CAP) break;
            }
            STAMP(12);
            float v[4], o[4];
#pragma unroll
            for (int k = 0; k < 4; ++k) {
                float t = y[k];
                t += dpp<0x124>(t); t += dpp<0x128>(t);
                v[k] = rbf(x32_sum(x16_sum(t)));
                const float xx = (k & 1) ? bf_hi(xfin[k >> 1]) : bf_lo(xfin[k >> 1]);
                o[k] = xx + rbf(v[k] * gt[k]);
            }
            v2u ov = {pk_bf(o[0], o[1]), pk_bf(o[2], o[3])};
            asm volatile("" : "+v"(ov));
            if (fin) {
                if (lane < 4) *(GAS v2u*)(a.out + fo) = ov;
                if (j == 0 && lane == 0) __hip_atomic_store((GAS unsigned*)(a.ws + WS_EPOCH), tag, __ATOMIC_RELAXED, __HIP_MEMORY_SCOPE_AGENT);
                CFENCE();
            }
            asm volatile("" : : "v"(A0), "v"(A1), "v"(B0), "v"(B1));
            if (fin) break;
#pragma unroll
            for (int k = 0; k < 4; ++k) {
                const float bb = (k & 1) ? bf_hi(b2v[k >> 1]) : bf_lo(b2v[k >> 1]);
                const float ya = rbf(v[k] + bb);
                gt[k] = rbf(ya * rcp(1.f + ex2(-LOG2E * ya)));
            }
            po += WS_S - WS_Y; fin = 1;
            STAMP(8);
        }
        STAMP(13);
    }
    STAMP_END();
    (void)smem;
}

extern "C" void kernel_launch(void* const* d_in, const int* in_sizes, int n_in,
                              void* d_out, int out_size, void* d_ws, size_t ws_size,
                              hipStream_t stream) {
    Args a;
    a.x = (const u16*)d_in[0]; a.kc = (const u16*)d_in[1]; a.vc = (const u16*)d_in[2]; a.wq = (const u16*)d_in[3]; a.wo = (const u16*)d_in[4];
    a.w1 = (const u16*)d_in[6]; a.b1 = (const u16*)d_in[7]; a.w2 = (const u16*)d_in[8]; a.b2 = (const u16*)d_in[9];
    a.out = (u16*)d_out; a.ws = (unsigned char*)d_ws;
    (void)hipFuncSetAttribute((const void*)fused_decode, hipFuncAttributeMaxDynamicSharedMemorySize, L_BYTES);
    hipLaunchKernelGGL(fused_decode, dim3(NWG), dim3(NTHR), L_BYTES, stream, a);
}
```

```cpp
#include <hip/hip_runtime.h>
#include <hip/hip_bf16.h>
#include <stdint.h>

typedef unsigned short u16;
typedef unsigned v4u __attribute__((ext_vector_type(4)));
typedef unsigned v2u __attribute__((ext_vector_type(2)));
typedef float f32x4 __attribute__((ext_vector_type(4)));
typedef float f32x2 __attribute__((ext_vector_type(2)));
typedef __bf16 bf16x8 __attribute__((ext_vector_type(8)));
typedef __bf16 bf16x2 __attribute__((ext_vector_type(2)));
typedef short v4i16 __attribute__((ext_vector_type(4)));
#define GAS __attribute__((address_space(1)))
#define LAS __attribute__((address_space(3)))

#define STAMPS 0
#define WE 7
#define WL 7
#define IBX 1
#define W1G 2
#define W2G 23
#define HG 18
#define DRY 1
#define WOG 25
#define QGL 9
#define QGE 10
#define CODE_LINES 20
#define PSQ 4
#define PSH 4
#define ODL 12
#define PSO 0
#define YDL 16
#define SDL 12
#define PSS 0
#define WNT 1
#define TWP 1
#define QSL 0

constexpr int NWG = 256, NWAVE = 11, NTHR = NWAVE * 64;
constexpr unsigned WS_EPOCH = 0;
constexpr unsigned WS_Q = 4096;
constexpr unsigned WS_H = WS_Q + 64 * 8192;
constexpr unsigned WS_O = WS_H + 28 * 8192;
constexpr unsigned WS_S = WS_O + 32 * 16384;
constexpr unsigned WS_Y = WS_S + 256 * 2048;
constexpr unsigned WS_END = WS_Y + 256 * 2048;
constexpr unsigned WS_STAMP = 8u << 20;
constexpr unsigned L_FLG = 64;
constexpr unsigned F_PROG = L_FLG, F_XN = L_FLG + 4, F_Q = L_FLG + 8, F_SC = L_FLG + 12;
constexpr unsigned F_TAG = L_FLG + 16, F_S = L_FLG + 24, F_WO = L_FLG + 28, F_O = L_FLG + 32;
constexpr unsigned F_XF = L_FLG + 40, F_W = L_FLG + 48;
constexpr unsigned L_Q = 128;
constexpr unsigned L_O = L_Q + 4 * 272;
constexpr unsigned L_SC = L_O + 8 * 272;
constexpr unsigned L_W2 = 6144;
constexpr unsigned L_WO = L_W2 + 8192;
constexpr unsigned L_K = L_WO + 16384;
constexpr unsigned L_V = L_K + 65536;
constexpr unsigned L_XN = L_W2;
constexpr unsigned L_PQ = L_V + 12 * 4096;
constexpr unsigned L_P1 = L_WO;
constexpr unsigned L_BYTES = L_V + 65536;
static_assert(L_SC % 16 == 0 && L_SC + 2048 <= L_W2 && L_BYTES <= 163840, "LDS");
static_assert(WE + IBX <= 15 && WL + IBX <= 15 && WE > 2 && WL > 2, "loader");
constexpr unsigned OOR = 0x40000000u;
constexpr unsigned SPIN_CAP = 1u << 18;

struct Args {
    const u16* x; const u16* kc; const u16* vc; const u16* wq; const u16* wo;
    const u16* w1; const u16* b1; const u16* w2; const u16* b2; u16* out; unsigned char* ws;
};

__device__ __forceinline__ float bf_lo(unsigned u) { return __uint_as_float(u << 16); }
__device__ __forceinline__ float bf_hi(unsigned u) { return __uint_as_float(u & 0xffff0000u); }
__device__ __forceinline__ unsigned pk_bf(float lo, float hi) { bf16x2 v = {(__bf16)lo, (__bf16)hi}; return __builtin_bit_cast(unsigned, v); }
__device__ __forceinline__ float rbf(float f) { return (float)(__bf16)f; }
__device__ __forceinline__ float rcp(float f) { return __builtin_amdgcn_rcpf(f); }
__device__ __forceinline__ float ex2(float f) { return __builtin_amdgcn_exp2f(f); }
__device__ __forceinline__ float lg2(float f) { return __builtin_amdgcn_logf(f); }
__device__ __forceinline__ f32x4 mfma16(v4u a, v4u b, f32x4 c) {
    return __builtin_amdgcn_mfma_f32_16x16x32_bf16(__builtin_bit_cast(bf16x8, a), __builtin_bit_cast(bf16x8, b), c, 0, 0, 0);
}
__device__ __forceinline__ v2u trread(unsigned p) { return __builtin_bit_cast(v2u, __builtin_amdgcn_ds_read_tr16_b64_v4i16((LAS v4i16*)(size_t)p)); }
__device__ __forceinline__ v4u trfrag(unsigned p0, unsigned p1) { v2u a = trread(p0), b = trread(p1); return (v4u){a.x, a.y, b.x, b.y}; }
template <int CTRL> __device__ __forceinline__ unsigned dppu(unsigned x) { return (unsigned)__builtin_amdgcn_mov_dpp((int)x, CTRL, 0xf, 0xf, true); }
template <int CTRL> __device__ __forceinline__ float dpp(float x) { return __uint_as_float(dppu<CTRL>(__float_as_uint(x))); }
template <int CTRL> __device__ __forceinline__ v4u dpp4(v4u x) { return (v4u){dppu<CTRL>(x.x), dppu<CTRL>(x.y), dppu<CTRL>(x.z), dppu<CTRL>(x.w)}; }
__device__ __forceinline__ float x16_sum(float x) { auto s = __builtin_amdgcn_permlane16_swap(__float_as_uint(x), __float_as_uint(x), false, false); return __uint_as_float(s[0]) + __uint_as_float(s[1]); }
__device__ __forceinline__ float x32_sum(float x) { auto s = __builtin_amdgcn_permlane32_swap(__float_as_uint(x), __float_as_uint(x), false, false); return __uint_as_float(s[0]) + __uint_as_float(s[1]); }
__device__ __forceinline__ float x16_max(float x) { auto s = __builtin_amdgcn_permlane16_swap(__float_as_uint(x), __float_as_uint(x), false, false); return fmaxf(__uint_as_float(s[0]), __uint_as_float(s[1])); }
__device__ __forceinline__ float x32_max(float x) { auto s = __builtin_amdgcn_permlane32_swap(__float_as_uint(x), __float_as_uint(x), false, false); return fmaxf(__uint_as_float(s[0]), __uint_as_float(s[1])); }
__device__ __forceinline__ float wave_sum(float x) {
    x += dpp<0xB1>(x); x += dpp<0x4E>(x); x += dpp<0x141>(x); x += dpp<0x140>(x);
    return x32_sum(x16_sum(x));
}
__device__ __forceinline__ v4u ld_sc1(__amdgpu_buffer_rsrc_t rs, unsigned off, unsigned so) { return __builtin_amdgcn_raw_buffer_load_b128(rs, off, so, 16); }
__device__ __forceinline__ void st_sc1(__amdgpu_buffer_rsrc_t rs, unsigned off, unsigned so, v4u v) { __builtin_amdgcn_raw_buffer_store_b128(v, rs, off, so, 16); }
__device__ __forceinline__ unsigned opq(unsigned v) { asm("" : "+v"(v)); return v; }
#define CFENCE() asm volatile("" ::: "memory")
#define LDS16(off) (*(LAS v4u*)(size_t)(off))
#define LDS8(off) (*(LAS v2u*)(size_t)(off))
#define LDS4(off) (*(LAS unsigned*)(size_t)(off))
#define VLDS4(off) (*(volatile LAS unsigned*)(size_t)(off))
#define STAMP(role, i) do { } while (0)
#define STAMP_END() do { } while (0)

template <int NT> __device__ __forceinline__ void glds4(const void* base, unsigned dst, unsigned v0, unsigned v1, unsigned v2, unsigned v3) {
    if (NT) asm volatile("s_mov_b32 m0, %1\n\ts_nop 0\n\tglobal_load_lds_dwordx4 %2, %0 nt\n\tglobal_load_lds_dwordx4 %3, %0 offset:1024 nt\n\t"
                         "global_load_lds_dwordx4 %4, %0 offset:2048 nt\n\tglobal_load_lds_dwordx4 %5, %0 offset:3072 nt"
                         : : "s"(base), "s"(dst), "v"(v0), "v"(v1), "v"(v2), "v"(v3) : "memory", "m0");
    else    asm volatile("s_mov_b32 m0, %1\n\ts_nop 0\n\tglobal_load_lds_dwordx4 %2, %0\n\tglobal_load_lds_dwordx4 %3, %0 offset:1024\n\t"
                         "global_load_lds_dwordx4 %4, %0 offset:2048\n\tglobal_load_lds_dwordx4 %5, %0 offset:3072"
                         : : "s"(base), "s"(dst), "v"(v0), "v"(v1), "v"(v2), "v"(v3) : "memory", "m0");
}
template <int SLEEP> __device__ __forceinline__ void lds_wait_ge(unsigned off, unsigned need) {
#pragma clang loop unroll(disable)
    for (unsigned spin = 0; spin < SPIN_CAP; ++spin) {
        if ((unsigned)__builtin_amdgcn_readfirstlane(VLDS4(off)) >= need) break;
        if (SLEEP) __builtin_amdgcn_s_sleep(SLEEP);
    }
    CFENCE();
}
template <unsigned OFF> __device__ __forceinline__ void lds_arrive() {
    asm volatile("s_mov_b64 exec, 1\n\tds_add_u32 %0, %1 offset:%2\n\ts_mov_b64 exec, -1" : : "v"(0u), "v"(1u), "n"(OFF) : "memory");
}
__device__ __forceinline__ void lds_arrive_at(unsigned off) {
    asm volatile("s_mov_b64 exec, 1\n\tds_add_u32 %0, %1\n\ts_mov_b64 exec, -1" : : "v"(off), "v"(1u) : "memory");
}
#define KEEP4(a, i) asm volatile("" : "+v"(a[i]), "+v"(a[i + 1]), "+v"(a[i + 2]), "+v"(a[i + 3]) : : "memory")
#define SB() __builtin_amdgcn_sched_barrier(0)
#define VMWAIT(n) asm volatile("s_waitcnt vmcnt(%0)" : : "n"(n) : "memory")
#define PROG() do { if (WE == WL || early) VMWAIT(4 * (WE - 1)); else VMWAIT(4 * (WL - 1)); ++prog; VLDS4(F_PROG) = prog; if (STAMPS && prog == 16) STAMP(0, 2); } while (0)
__device__ __forceinline__ v4u enc4(f32x4 v, unsigned tag) {
    v4u o;
#pragma unroll
    for (int k = 0; k < 4; ++k) o[k] = ((__float_as_uint(v[k]) + 0x80u) & 0xffffff00u) | ((tag >> (8 * k)) & 0xffu);
    return o;
}
__device__ __forceinline__ unsigned bad4(v4u w, unsigned tag) {
    return (__builtin_amdgcn_perm(w.y, w.x, 0x0c0c0400u) | __builtin_amdgcn_perm(w.w, w.z, 0x04000c0cu)) ^ tag;
}
__device__ __forceinline__ f32x4 dec4(v4u w) {
    return (f32x4){__uint_as_float(w.x & 0xffffff00u), __uint_as_float(w.y & 0xffffff00u), __uint_as_float(w.z & 0xffffff00u), __uint_as_float(w.w & 0xffffff00u)};
}
__device__ __forceinline__ bool any_lane(unsigned v) { return __builtin_amdgcn_ballot_w64(v != 0u) != 0ull; }
__device__ __forceinline__ v2u hi16(v4u w) { return (v2u){__builtin_amdgcn_perm(w.y, w.x, 0x07060302u), __builtin_amdgcn_perm(w.w, w.z, 0x07060302u)}; }
__device__ __forceinline__ float x32_pair(float a, float b) { auto s = __builtin_amdgcn_permlane32_swap(__float_as_uint(a), __float_as_uint(b), false, false); return __uint_as_float(s[0]) + __uint_as_float(s[1]); }

__global__ void __launch_bounds__(NTHR, 1) fused_decode(Args a) {
    extern __shared__ __attribute__((aligned(16))) unsigned char smem[];
    unsigned long long pc;
    asm volatile("s_getpc_b64 %0" : "=s"(pc));
    const unsigned tid = threadIdx.x, lane = tid & 63u, wave = __builtin_amdgcn_readfirstlane(tid >> 6);
    const unsigned j = blockIdx.x, xcd = j & 7u, r = j >> 3;
    const unsigned n16 = lane & 15u, g = lane >> 4, q4 = n16 >> 2, p4 = lane & 3u;
    constexpr float LOG2E = 1.4426950408889634f;
    if (wave == 0) STAMP(0, 0);
    const bool early = xcd < 4u, up = early && r < 28u;
    const unsigned r7 = (r * 37u) >> 8, i7 = r - 7u * r7, cmb = r < 28u ? r7 : r & 3u;
    const unsigned cq = r < 28u ? (i7 < 3u ? i7 : i7 + 1u) : 3u;
    const unsigned kq = ((xcd & 1u) << 1) | (cmb >> 1), hq = ((xcd ^ 4u) & 6u) | (cmb & 1u);
    const unsigned sw = ((lane & 7u) ^ (((lane >> 4) & 3u) << 1)) << 4;
    unsigned cpf = 0, ep = 0;
    v4u xv = {0u, 0u, 0u, 0u};
    if (wave != 0) {
        if (wave == 1) {
            const unsigned q0 = (lane >> 3) * 8192u + sw;
            const unsigned char* qb = (const unsigned char*)a.wq + kq * 1048576u + hq * 1024u + cq * 128u;
#pragma clang loop unroll(disable)
            for (unsigned k = 0; k < 4; ++k) glds4<WNT>(qb + k * 262144u, L_PQ + k * 4096u, q0, q0 + 64512u, q0 + 2u * 64512u, q0 + 3u * 64512u);
        }
        if (wave == 2) {
            ep = __hip_atomic_load((GAS unsigned*)(a.ws + WS_EPOCH), __ATOMIC_RELAXED, __HIP_MEMORY_SCOPE_AGENT);
            if (r >= 28u) cpf = *(const GAS unsigned*)((pc & ~255ull) + ((r & 3u) * CODE_LINES + (lane < CODE_LINES ? lane : 0u)) * 128u);
            if (lane < 16) LDS4(L_FLG + lane * 4u) = 0u;
            asm volatile("s_waitcnt lgkmcnt(0)" ::: "memory");
        }
        if (wave >= 3) xv = *(const GAS v4u*)((const unsigned char*)a.x + (wave - 3u) * 1024u + lane * 16u);
        __builtin_amdgcn_s_barrier();
        CFENCE();
    }
    const unsigned xp = (xcd & 4u) | ((xcd - 1u) & 3u);
    const unsigned hh = (r >> 2) != xcd ? xp : (xcd ^ 2u);
    const unsigned cc = (r >> 2) != xcd ? (r >> 2) : xp;
    const unsigned ho = hh * 4u + (r & 3u);
    const unsigned w0 = (lane >> 3) * 1024u + sw;
    const unsigned row = 4u * g + q4;

    if (wave == 0) {
        __builtin_amdgcn_s_setprio(3);
        const size_t kvoff = ((size_t)((r >> 2) * 8u + xcd) * 4096u + 3072u + 256u * (r & 3u)) * 256u;
        const unsigned rot = r & 15u;
        const unsigned char* kb = (const unsigned char*)a.kc + kvoff;
        unsigned o[4];
#pragma unroll
        for (int i = 0; i < 4; ++i) o[i] = g * 256u + ((n16 ^ g ^ (4u * i)) << 4);
        KEEP4(o, 0);
        unsigned prog = IBX;
        const unsigned ib = (early ? WE : WL) + IBX;
#pragma clang loop unroll(disable)
        for (unsigned c = 0; c < 32; ++c) {
            if (c == 2) __builtin_amdgcn_s_barrier();
            if (c == 16) {
                o[0] = o[2] = g * 256u + ((n16 ^ (g << 1)) << 4);
                o[1] = o[3] = g * 256u + ((n16 ^ ((4u + g) << 1)) << 4);
                kb = (const unsigned char*)a.vc + kvoff;
            }
            if (c >= ib) PROG();
            if (c == 28) lds_wait_ge<1>(F_XF, 4);
            glds4<1>(kb + ((c + rot) & 15u) * 4096u, L_K + c * 4096u, o[0], o[1], o[2], o[3]);
        }
#pragma unroll
        for (int k = (WE > WL ? WE : WL) - 1; k >= 0; --k) { VMWAIT(4 * k); if (WE == WL || 32u - k > prog) VLDS4(F_PROG) = 32 - k; }
        STAMP(0, 5);
    }
    const __amdgpu_buffer_rsrc_t rs = __builtin_amdgcn_make_buffer_rsrc(a.ws, 0, WS_END, 0x00020000);
    if (wave >= 3) {
        const unsigned w = wave - 3u;
        {
            float v[8];
#pragma unroll
            for (int k = 0; k < 4; ++k) { v[2 * k] = bf_lo(xv[k]); v[2 * k + 1] = bf_hi(xv[k]); }
            const float s = ((v[0] + v[1]) + (v[2] + v[3])) + ((v[4] + v[5]) + (v[6] + v[7]));
            const float mean = wave_sum(s) * (1.f / 512.f);
            float q = 0.f;
#pragma unroll
            for (int k = 0; k < 8; ++k) { const float d = v[k] - mean; q = fmaf(d, d, q); }
            const float var = rbf(wave_sum(q) * (1.f / 512.f));
            const float mb = rbf(mean);
            const float den = rbf(sqrtf(rbf(var + 1e-5f)));
            const float rc = rcp(den);
            float o[8];
#pragma unroll
            for (int k = 0; k < 8; ++k) { const float t = rbf(v[k] - mb); const float q0 = t * rc; const float e = fmaf(-q0, den, t); o[k] = fmaf(e, rc, q0); }
            const unsigned xo = L_XN + w * 1024u + (((lane & ~3u) + 2u * (lane & 1u)) ^ (4u * (w & 3u))) * 16u + (lane & 2u) * 4u;
            LDS8(xo) = (v2u){pk_bf(o[0], o[1]), pk_bf(o[2], o[3])};
            LDS8(xo + 16) = (v2u){pk_bf(o[4], o[5]), pk_bf(o[6], o[7])};
        }
        lds_arrive<F_XN>();
        STAMP(3, 2);
        const unsigned va = opq(L_V + row * 256u + (((2u * w + (p4 >> 1)) ^ ((row & 7u) << 1)) << 4) + 8u * (p4 & 1u));
        const unsigned wa = opq(L_WO + row * 128u + (((2u * (w & 3u) + (p4 >> 1)) ^ (((row >> 1) & 3u) << 1)) << 4) + 8u * (p4 & 1u));
        unsigned pass = 0;
#pragma clang loop unroll(disable)
        for (;;) {
        asm volatile("" : "+s"(pass));
        if (pass == (w >> 2) && (w < 4u || up)) {
            const unsigned ia = wa + (pass ? 0u : L_PQ - L_WO);
            v4u af[4], xf[4];
            __builtin_amdgcn_s_setprio(2);
            lds_wait_ge<0>(F_W + 4u * pass, 1u);
#pragma unroll
            for (int k = 0; k < 4; ++k) af[k] = trfrag(ia + 4096 * k, ia + 4096 * k + 2048);
            KEEP4(af, 0);
            lds_wait_ge<0>(F_XN, 9u);
#pragma unroll
            for (int k = 0; k < 4; ++k) xf[k] = LDS16(L_XN + (n16 & 7u) * 1024u + (pass ? xcd : kq) * 256u + (((4u * k + g) ^ (4u * (n16 & 3u))) << 4));
            const unsigned tg = LDS4(F_TAG);
            SB();
            f32x4 d0 = mfma16(af[0], xf[0], (f32x4){0.f, 0.f, 0.f, 0.f}), d1 = mfma16(af[1], xf[1], (f32x4){0.f, 0.f, 0.f, 0.f});
            d0 = mfma16(af[2], xf[2], d0); d1 = mfma16(af[3], xf[3], d1);
            const unsigned so = pass ? WS_H + r * 8192u + xcd * 2048u + (w & 2u) * 512u + (w & 1u) * 64u : WS_Q + hq * 8192u + kq * 2048u + cq * 256u + w * 64u;
            if (n16 < 8u) st_sc1(rs, n16 * (pass ? 128u : 65536u) + g * 16u, so, enc4(d0 + d1, tg));
            CFENCE();
            lds_arrive_at(F_XF + 4u * pass);
            __builtin_amdgcn_s_setprio(0);
            if (pass) STAMP(3, 6); else STAMP(3, 4);
        }
        if (!DRY && !pass) { pass = 1; continue; }
        const unsigned pm = 0u - pass, dr = OOR & ~pm;
        constexpr unsigned NA = DRY ? 16u : 8u;
        {
            v4u kf[8], qf[4];
            lds_wait_ge<2>(F_PROG, (2u * w + 2u) & pm);
#pragma unroll
            for (int t = 0; t < 4; ++t) {
                const unsigned ka = L_K + w * 8192u + n16 * 256u + (((4u * t + g) ^ n16) << 4);
                kf[t] = LDS16(ka); kf[4 + t] = LDS16(ka + 4096);
            }
            KEEP4(kf, 0); KEEP4(kf, 4);
            STAMP(3, 3);
            lds_wait_ge<QSL>(F_Q, pass);
            STAMP(3, 5);
#pragma unroll
            for (int t = 0; t < 4; ++t) qf[t] = LDS16(L_Q + (n16 & 3u) * 272u + 16u * g + 64 * t);
            SB();
            f32x4 s0 = {0.f, 0.f, 0.f, 0.f}, s1 = {0.f, 0.f, 0.f, 0.f};
#pragma unroll
            for (int t = 0; t < 4; ++t) { s0 = mfma16(kf[t], qf[t], s0); s1 = mfma16(kf[4 + t], qf[t], s1); }
            float sc[8];
#pragma unroll
            for (int k = 0; k < 4; ++k) { sc[k] = rbf(s0[k]) * 0.08838834764831845f; sc[4 + k] = rbf(s1[k]) * 0.08838834764831845f; }
            if (n16 < 4) LDS16(L_SC + w * 256u + n16 * 64u + g * 16u) = (v4u){pk_bf(sc[0], sc[1]), pk_bf(sc[2], sc[3]), pk_bf(sc[4], sc[5]), pk_bf(sc[6], sc[7])};
        }
        lds_arrive<F_SC>();
        const unsigned tag = LDS4(F_TAG);
        lds_wait_ge<0>(F_SC, NA & pm);
        STAMP(3, 7);
        v4u P[8]; float lse, inv;
        {
            const unsigned sa = L_SC + q4 * 512u + (n16 & 3u) * 64u + g * 16u;
            const v4u A0 = LDS16(sa), A1 = LDS16(sa + 256);
            float sc[16];
#pragma unroll
            for (int k = 0; k < 4; ++k) { sc[2 * k] = bf_lo(A0[k]); sc[2 * k + 1] = bf_hi(A0[k]); sc[8 + 2 * k] = bf_lo(A1[k]); sc[9 + 2 * k] = bf_hi(A1[k]); }
            float m = sc[0];
#pragma unroll
            for (int k = 1; k < 16; ++k) m = fmaxf(m, sc[k]);
            m = fmaxf(m, dpp<0x124>(m)); m = fmaxf(m, dpp<0x128>(m));
            m = x32_max(x16_max(m));
            const float ml = m * LOG2E;
            float p[16];
#pragma unroll
            for (int k = 0; k < 16; ++k) p[k] = ex2(fmaf(sc[k], LOG2E, -ml));
            P[0] = (v4u){pk_bf(p[0], p[1]), pk_bf(p[2], p[3]), pk_bf(p[4], p[5]), pk_bf(p[6], p[7])};
            P[1] = (v4u){pk_bf(p[8], p[9]), pk_bf(p[10], p[11]), pk_bf(p[12], p[13]), pk_bf(p[14], p[15])};
            float l = (((p[0] + p[1]) + (p[2] + p[3])) + ((p[4] + p[5]) + (p[6] + p[7]))) + (((p[8] + p[9]) + (p[10] + p[11])) + ((p[12] + p[13]) + (p[14] + p[15])));
            l += dpp<0x128>(l); l += dpp<0x124>(l);
            l = x32_sum(x16_sum(l));
            lse = ml + lg2(l); inv = rcp(l);
            P[2] = dpp4<0x12C>(P[0]); P[3] = dpp4<0x12C>(P[1]);
            P[4] = dpp4<0x128>(P[0]); P[5] = dpp4<0x128>(P[1]);
            P[6] = dpp4<0x124>(P[0]); P[7] = dpp4<0x124>(P[1]);
            KEEP4(P, 0); KEEP4(P, 4);
            asm volatile("" : "+v"(lse), "+v"(inv));
        }
        STAMP(3, 8);
        {
            f32x4 d0 = {0.f, 0.f, 0.f, 0.f}, d1 = {0.f, 0.f, 0.f, 0.f};
            v4u vf[4];
            lds_wait_ge<2>(F_PROG, 24u & pm);
#pragma unroll
            for (int t = 0; t < 4; ++t) vf[t] = trfrag(va + 8192 * t, va + 8192 * t + 4096);
            d0 = mfma16(vf[0], P[0], d0); d1 = mfma16(vf[1], P[1], d1); d0 = mfma16(vf[2], P[2], d0); d1 = mfma16(vf[3], P[3], d1);
            asm volatile("" : "+v"(d0), "+v"(d1));
            lds_wait_ge<1>(F_PROG, 30u & pm);
#pragma unroll
            for (int t = 0; t < 3; ++t) vf[t] = trfrag(va + 8192 * (4 + t), va + 8192 * (4 + t) + 4096);
            d0 = mfma16(vf[0], P[4], d0); d1 = mfma16(vf[1], P[5], d1); d0 = mfma16(vf[2], P[6], d0);
            d0 += d1;
            asm volatile("" : "+v"(d0));
            lds_wait_ge<0>(F_PROG, 32u & pm);
            vf[3] = trfrag(va + 8192 * 7, va + 8192 * 7 + 4096);
            d0 = mfma16(vf[3], P[7], d0);
            v4u ov = {pk_bf(d0[0] * inv, d0[1] * inv), pk_bf(d0[2] * inv, d0[3] * inv), __float_as_uint(lse), tag};
            if (n16 < 4u) st_sc1(rs, n16 * 16384u + g * 16u, (WS_O + (xcd * 128u + r) * 512u + w * 64u) | dr, ov);
        }
        CFENCE();
        STAMP(3, 9);
        {
            const unsigned po = (WS_O + ho * 16384u + w * 2048u) | dr;
            v4u A0, A1, af[4];
            if (ODL && pass) __builtin_amdgcn_s_sleep(ODL);
            CFENCE();
            A0 = ld_sc1(rs, lane * 16u, po); A1 = ld_sc1(rs, lane * 16u + 1024u, po);
            lds_wait_ge<0>(F_WO, pass);
#pragma unroll
            for (int t = 0; t < 4; ++t) af[t] = trfrag(wa + 4096 * t, wa + 4096 * t + 2048);
            KEEP4(af, 0);
#pragma clang loop unroll(disable)
            for (unsigned spin = 0;; ++spin) {
                const unsigned bad = ((A0.w ^ tag) | (A1.w ^ tag)) & pm;
                if (!any_lane(bad) || spin > SPIN_CAP) break;
                if (PSO) __builtin_amdgcn_s_sleep(PSO);
                CFENCE();
                if (bad) { A0 = ld_sc1(rs, lane * 16u, po); A1 = ld_sc1(rs, lane * 16u + 1024u, po); }
            }
            STAMP(3, 12);
            const float l0 = __uint_as_float(A0.z), l1 = __uint_as_float(A1.z);
            const float M = x32_max(fmaxf(l0, l1));
            const float e0 = ex2(l0 - M), e1 = ex2(l1 - M);
            const float it = rcp(x32_sum(e0 + e1)), c0 = e0 * it, c1w = e1 * it;
            const float lo = x32_pair(fmaf(c0, bf_lo(A0.x), c1w * bf_lo(A1.x)), fmaf(c0, bf_lo(A0.y), c1w * bf_lo(A1.y)));
            const float hi = x32_pair(fmaf(c0, bf_hi(A0.x), c1w * bf_hi(A1.x)), fmaf(c0, bf_hi(A0.y), c1w * bf_hi(A1.y)));
            LDS4(L_O + w * 272u + ((lane >> 3) & 3u) * 64u + p4 * 16u + ((lane >> 2) & 1u) * 8u + (lane >> 5) * 4u) = pk_bf(lo, hi);
            lds_arrive<F_O>();
            lds_wait_ge<0>(F_O, NA & pm);
            STAMP(3, 13);
            v4u bf[4];
#pragma unroll
            for (int t = 0; t < 4; ++t) bf[t] = LDS16(L_O + (n16 & 7u) * 272u + 16u * g + 64 * t);
            SB();
            f32x4 d0 = mfma16(af[0], bf[0], (f32x4){0.f, 0.f, 0.f, 0.f}), d1 = mfma16(af[1], bf[1], (f32x4){0.f, 0.f, 0.f, 0.f});
            d0 = mfma16(af[2], bf[2], d0); d1 = mfma16(af[3], bf[3], d1);
            if (q4 == (w >> 2)) st_sc1(rs, n16 * 65536u + g * 16u, (WS_S + ((w & 3u) * 8u + cc) * 2048u + ho * 64u) | dr, enc4(d0 + d1, tag));
            CFENCE();
            lds_arrive<F_S>();
            STAMP(3, 14);
        }
        if (pass) break;
        STAMP(3, 1);
        pass = 1;
        }
    }
    if (wave == 1 || wave == 2) {
        const bool isq = wave == 1;
        const unsigned fo = (r >> 2) * 512u + (xcd * 4u + (r & 3u)) * 16u + 4u * p4;
        const unsigned rb = r < 28u ? r : 27u, cg = (xcd + 1u + rb % 7u) & 7u;
        v2u xfin = {0u, 0u}, b2v = {0u, 0u}, b1v[2] = {{0u, 0u}, {0u, 0u}};
        __builtin_amdgcn_s_setprio(2);
        if (!isq) {
            xfin = *(const GAS v2u*)(a.x + fo);
            b2v = *(const GAS v2u*)(a.b2 + (fo & 511u));
            b1v[0] = *(const GAS v2u*)(a.b1 + rb * 64u + (n16 >> 3) * 16u + g * 4u);
            b1v[1] = *(const GAS v2u*)(a.b1 + rb * 64u + (n16 >> 3) * 16u + g * 4u + 32u);
            unsigned tw = 0, tw2 = 0;
            if (TWP) {
                tw = __builtin_amdgcn_raw_buffer_load_b32(rs, 4096u + j * 128u + lane * 262144u, 0, 16);
                tw2 = *(const GAS unsigned*)(a.out + fo);
            }
            if (lane == 0) LDS4(F_TAG) = ep + 0x9E3779B1u;
            lds_arrive<F_XN>();
            asm volatile("" : "+v"(cpf), "+v"(tw), "+v"(tw2), "+v"(xfin), "+v"(b2v), "+v"(b1v[0]), "+v"(b1v[1]));
            if (up) {
                const unsigned u0 = (lane >> 3) * 3584u + sw;
                const unsigned char* ub = (const unsigned char*)a.w1 + xcd * 458752u + r * 128u;
                lds_wait_ge<1>(F_PROG, W1G);
#pragma clang loop unroll(disable)
                for (unsigned k = 0; k < 4; ++k) glds4<WNT>(ub + k * 114688u, L_P1 + k * 4096u, u0, u0 + 27648u, u0 + 2u * 27648u, u0 + 3u * 27648u);
            }
        }
        VMWAIT(0);
        VLDS4(F_W + (isq ? 0u : 4u)) = 1u;
        STAMP(1, 14);
        f32x4 e[2];
        unsigned tag, pass = DRY ? __builtin_amdgcn_readfirstlane(wave - 1u) : 1u;
#pragma clang loop unroll(disable)
        for (;;) {
            asm volatile("" : "+s"(pass));
            const unsigned pm = isq || r < 28u ? 0u - pass : 0u;
            lds_wait_ge<2>(F_XN, 9u & pm);
            lds_wait_ge<2>(F_PROG, (isq ? (early ? QGE : QGL) : HG) & pm);
            tag = LDS4(F_TAG);
            STAMP(1, 1);
            const unsigned so = (isq ? WS_Q + ((r >> 2) * 8u + xcd) * 8192u : WS_H + rb * 8192u) | (OOR & ~pm);
            const unsigned ga = isq ? lane * 16u : (n16 & 7u) * 128u + (n16 >> 3) * 64u + g * 16u;
            v4u G[8];
#pragma unroll
            for (int k = 0; k < 8; ++k) asm volatile("" : "=v"(G[k]));
            unsigned bad = 1;
#pragma clang loop unroll(disable)
            for (unsigned spin = 0;; ++spin) {
                unsigned pa = ga, pb = ga + 4096u;
                asm volatile("" : "+v"(pa), "+v"(pb) : : "memory");
                if (bad) {
#pragma unroll
                    for (int k = 0; k < 4; ++k) { G[k] = ld_sc1(rs, pa + 1024 * k, so); G[4 + k] = ld_sc1(rs, pb + 1024 * k, so); }
                    bad = 0;
#pragma unroll
                    for (int k = 0; k < 8; ++k) bad |= bad4(G[k], tag);
                    bad &= pm;
                }
                if (STAMPS && spin == 0) STAMP(1, 2);
                if (!any_lane(bad) || spin > SPIN_CAP) break;
                if (PSQ) __builtin_amdgcn_s_sleep(PSQ);
            }
#pragma unroll
            for (int i = 0; i < 2; ++i) e[i] = (dec4(G[i]) + dec4(G[2 + i])) + (dec4(G[4 + i]) + dec4(G[6 + i]));
            if (isq) {
                const unsigned qd = L_Q + (lane >> 5) * 272u + (lane & 31u) * 8u;
                LDS8(qd) = (v2u){pk_bf(e[0][0], e[0][1]), pk_bf(e[0][2], e[0][3])};
                LDS8(qd + 544) = (v2u){pk_bf(e[1][0], e[1][1]), pk_bf(e[1][2], e[1][3])};
                CFENCE();
                VLDS4(F_Q) = pass;
            }
            if (pass) break;
            pass = 1;
        }
        STAMP(1, 11);
        if (isq) {
            const unsigned char* wb = (const unsigned char*)a.wo + ho * 131072u + 128u * cc;
            lds_wait_ge<2>(F_PROG, WOG);
            lds_wait_ge<1>(F_XF + 4, up ? 4u : 0u);
#pragma clang loop unroll(disable)
            for (unsigned c = 0; c < 4; ++c) glds4<WNT>(wb + c * 32768u, L_WO + c * 4096u, w0, w0 + 7168u, w0 + 2u * 7168u, w0 + 3u * 7168u);
            VMWAIT(0);
            VLDS4(F_WO) = 1u;
            STAMP(1, 6);
        } else {
        if (r < 28u) {
            const unsigned char* tb = (const unsigned char*)a.w2 + rb * 65536u + cg * 128u;
            v4u hf[2], af[8];
#pragma unroll
            for (int i = 0; i < 2; ++i) {
                float hv[4];
#pragma unroll
                for (int k = 0; k < 4; ++k) {
                    const float t = rbf(rbf(e[i][k]) + ((k & 1) ? bf_hi(b1v[i][k >> 1]) : bf_lo(b1v[i][k >> 1])));
                    const float u = t * fmaf(t * t, -2.f * LOG2E * 0.7978845608028654f * 0.044715f, -2.f * LOG2E * 0.7978845608028654f);
                    hv[k] = t * rcp(1.f + ex2(u));
                }
                const v2u own = {pk_bf(hv[0], hv[1]), pk_bf(hv[2], hv[3])}, oth = {dppu<0x128>(own.x), dppu<0x128>(own.y)};
                hf[i] = n16 < 8 ? (v4u){own.x, own.y, oth.x, oth.y} : (v4u){oth.x, oth.y, own.x, own.y};
            }
            asm volatile("" : "+v"(hf[0]), "+v"(hf[1]));
            lds_wait_ge<2>(F_PROG, W2G);
            lds_wait_ge<1>(F_XF, 4u);
            lds_wait_ge<1>(F_XF + 4, up ? 4u : 0u);
            glds4<WNT>(tb, L_W2, w0, w0 + 7168u, w0 + 2u * 7168u, w0 + 3u * 7168u);
            glds4<WNT>(tb + 32768, L_W2 + 4096, w0, w0 + 7168u, w0 + 2u * 7168u, w0 + 3u * 7168u);
            VMWAIT(0);
            STAMP(2, 3);
#pragma unroll
            for (int t = 0; t < 4; ++t) {
                const unsigned wt = L_W2 + row * 128u + (((2u * t + (p4 >> 1)) ^ (((row >> 1) & 3u) << 1)) << 4) + 8u * (p4 & 1u);
                af[t] = trfrag(wt, wt + 2048); af[4 + t] = trfrag(wt + 4096, wt + 6144);
            }
            f32x4 d[4];
#pragma unroll
            for (int t = 0; t < 4; ++t) { d[t] = mfma16(af[t], hf[0], (f32x4){0.f, 0.f, 0.f, 0.f}); d[t] = mfma16(af[4 + t], hf[1], d[t]); }
            const unsigned yo = WS_Y + (n16 & 7u) * 65536u + (n16 >> 3) * 16384u + cg * 2048u + rb * 64u + g * 16u;
            st_sc1(rs, yo, 0, enc4(n16 < 8 ? d[0] : d[1], tag));
            st_sc1(rs, yo, 32768, enc4(n16 < 8 ? d[2] : d[3], tag));
        }
        STAMP(2, 7);
        float gt[4] = {0.f, 0.f, 0.f, 0.f};
        unsigned po = WS_Y + j * 2048u + lane * 16u, pb = lane < 48u ? po + 1024u : OOR, fin = 0;
#pragma clang loop unroll(disable)
        for (;;) {
            asm volatile("" : "+s"(fin));
            lds_wait_ge<1>(fin ? F_S : F_PROG, fin ? (DRY ? 16u : 8u) : 32u);
            if (fin) __builtin_amdgcn_s_sleep(SDL); else __builtin_amdgcn_s_sleep(YDL);
            v4u A0, A1;
            asm volatile("" : "=v"(A0), "=v"(A1), "+v"(po), "+v"(pb));
            unsigned bad = 1;
#pragma clang loop unroll(disable)
            for (unsigned spin = 0;; ++spin) {
                CFENCE();
                if (bad) { A0 = ld_sc1(rs, po, 0); A1 = ld_sc1(rs, pb, 0); bad = bad4(A0, tag) | (pb != OOR ? bad4(A1, tag) : 0u); }
                if (!any_lane(bad) || spin > SPIN_CAP) break;
                if (PSS) __builtin_amdgcn_s_sleep(PSS);
            }
            STAMP(2, 12);
            const f32x4 y = dec4(A0) + dec4(A1);
            float v[4], o[4];
#pragma unroll
            for (int k = 0; k < 4; ++k) {
                float t = y[k];
                t += dpp<0x124>(t); t += dpp<0x128>(t);
                v[k] = rbf(x32_sum(x16_sum(t)));
                const float xx = (k & 1) ? bf_hi(xfin[k >> 1]) : bf_lo(xfin[k >> 1]);
                o[k] = xx + rbf(v[k] * gt[k]);
            }
            if (fin) {
                if (lane < 4) *(GAS v2u*)(a.out + fo) = (v2u){pk_bf(o[0], o[1]), pk_bf(o[2], o[3])};
                if (j == 0 && lane == 0) __hip_atomic_store((GAS unsigned*)(a.ws + WS_EPOCH), tag, __ATOMIC_RELAXED, __HIP_MEMORY_SCOPE_AGENT);
                break;
            }
#pragma unroll
            for (int k = 0; k < 4; ++k) {
                const float bb = (k & 1) ? bf_hi(b2v[k >> 1]) : bf_lo(b2v[k >> 1]);
                const float ya = rbf(v[k] + bb);
                gt[k] = rbf(ya * rcp(1.f + ex2(-LOG2E * ya)));
            }
            po -= WS_Y - WS_S; pb = po + 1024u; fin = 1;
            STAMP(2, 8);
        }
        STAMP(2, 13);
        }
    }
    STAMP_END();
    (void)smem;
}

extern "C" void kernel_launch(void* const* d_in, const int* in_sizes, int n_in,
                              void* d_out, int out_size, void* d_ws, size_t ws_size,
                              hipStream_t stream) {
    Args a;
    a.x = (const u16*)d_in[0]; a.kc = (const u16*)d_in[1]; a.vc = (const u16*)d_in[2]; a.wq = (const u16*)d_in[3]; a.wo = (const u16*)d_in[4];
    a.w1 = (const u16*)d_in[6]; a.b1 = (const u16*)d_in[7]; a.w2 = (const u16*)d_in[8]; a.b2 = (const u16*)d_in[9];
    a.out = (u16*)d_out; a.ws = (unsigned char*)d_ws;
    (void)hipFuncSetAttribute((const void*)fused_decode, hipFuncAttributeMaxDynamicSharedMemorySize, L_BYTES);
    hipLaunchKernelGGL(fused_decode, dim3(NWG), dim3(NTHR), L_BYTES, stream, a);
}
```

```cpp
#include <hip/hip_runtime.h>
#include <hip/hip_bf16.h>
#include <stdint.h>

typedef unsigned short u16;
typedef unsigned v4u __attribute__((ext_vector_type(4)));
typedef unsigned v2u __attribute__((ext_vector_type(2)));
typedef float f32x4 __attribute__((ext_vector_type(4)));
typedef float f32x2 __attribute__((ext_vector_type(2)));
typedef __bf16 bf16x8 __attribute__((ext_vector_type(8)));
typedef __bf16 bf16x2 __attribute__((ext_vector_type(2)));
typedef short v4i16 __attribute__((ext_vector_type(4)));
#define GAS __attribute__((address_space(1)))
#define LAS __attribute__((address_space(3)))

#define STAMPS 0
#define WE 7
#define WL 7
#define IBX 1
#define W2G 8
#define HG 14
#define WOG 25
#define QGL 9
#define QGE 10
#define IWARM 0
#define YG 27u
#define PSQ 4
#define PSH 4
#define ODL 12
#define PSO 0
#define YDL 0
#define SDL 12
#define PSS 0
#define WNT 1
#define TWP 1
#define SNT 0
#define LNT 0
#define QSL 0

constexpr int NWG = 256, NWAVE = 11, NTHR = NWAVE * 64;
constexpr unsigned WS_EPOCH = 0;
constexpr unsigned WS_Q = 4096;
constexpr unsigned WS_H = WS_Q + 64 * 2048;
constexpr unsigned WS_O = WS_H + 56 * 1024;
constexpr unsigned WS_S = WS_O + 32 * 16384;
constexpr unsigned WS_Y = WS_S + 256 * 2048;
constexpr unsigned WS_END = WS_Y + 256 * 512;
constexpr unsigned WS_STAMP = 8u << 20;
constexpr unsigned L_FLG = 64;
constexpr unsigned F_PROG = L_FLG, F_XN = L_FLG + 4, F_Q = L_FLG + 8, F_SC = L_FLG + 12;
constexpr unsigned F_TAG = L_FLG + 16, F_KF = L_FLG + 20, F_S = L_FLG + 24, F_WO = L_FLG + 28, F_O = L_FLG + 32;
constexpr unsigned F_XF = L_FLG + 36, F_W = L_FLG + 40;
constexpr unsigned L_Q = 128;
constexpr unsigned L_O = L_Q + 4 * 272;
constexpr unsigned L_SC = L_O + 8 * 272;
constexpr unsigned L_ML = L_SC + 2048;
constexpr unsigned L_XN = 6144;
constexpr unsigned L_WO = L_XN + 8192;
constexpr unsigned L_K = L_WO + 16384;
constexpr unsigned L_V = L_K + 65536;
constexpr unsigned L_P = L_V + 8 * 4096;
constexpr unsigned L_W2 = L_K;
constexpr unsigned L_BYTES = L_V + 65536;
static_assert(L_SC % 16 == 0 && L_ML + 256 <= L_XN && L_BYTES <= 163840, "LDS");
static_assert(WE + IBX <= 15 && WL + IBX <= 15 && WE > 2 && WL > 2, "loader");
constexpr unsigned OOR = 0x40000000u;
constexpr unsigned SPIN_CAP = 1u << 18;

struct Args {
    const u16* x; const u16* kc; const u16* vc; const u16* wq; const u16* wo;
    const u16* w1; const u16* b1; const u16* w2; const u16* b2; u16* out; unsigned char* ws;
};

__device__ __forceinline__ float bf_lo(unsigned u) { return __uint_as_float(u << 16); }
__device__ __forceinline__ float bf_hi(unsigned u) { return __uint_as_float(u & 0xffff0000u); }
__device__ __forceinline__ unsigned pk_bf(float lo, float hi) { bf16x2 v = {(__bf16)lo, (__bf16)hi}; return __builtin_bit_cast(unsigned, v); }
__device__ __forceinline__ float rbf(float f) { return (float)(__bf16)f; }
__device__ __forceinline__ float rcp(float f) { return __builtin_amdgcn_rcpf(f); }
__device__ __forceinline__ float ex2(float f) { return __builtin_amdgcn_exp2f(f); }
__device__ __forceinline__ float lg2(float f) { return __builtin_amdgcn_logf(f); }
__device__ __forceinline__ f32x4 mfma16(v4u a, v4u b, f32x4 c) {
    return __builtin_amdgcn_mfma_f32_16x16x32_bf16(__builtin_bit_cast(bf16x8, a), __builtin_bit_cast(bf16x8, b), c, 0, 0, 0);
}
__device__ __forceinline__ v2u trread(unsigned p) { return __builtin_bit_cast(v2u, __builtin_amdgcn_ds_read_tr16_b64_v4i16((LAS v4i16*)(size_t)p)); }
__device__ __forceinline__ v4u trfrag(unsigned p0, unsigned p1) { v2u a = trread(p0), b = trread(p1); return (v4u){a.x, a.y, b.x, b.y}; }
template <int CTRL> __device__ __forceinline__ unsigned dppu(unsigned x) { return (unsigned)__builtin_amdgcn_mov_dpp((int)x, CTRL, 0xf, 0xf, true); }
template <int CTRL> __device__ __forceinline__ float dpp(float x) { return __uint_as_float(dppu<CTRL>(__float_as_uint(x))); }
template <int CTRL> __device__ __forceinline__ v4u dpp4(v4u x) { return (v4u){dppu<CTRL>(x.x), dppu<CTRL>(x.y), dppu<CTRL>(x.z), dppu<CTRL>(x.w)}; }
__device__ __forceinline__ float x16_sum(float x) { auto s = __builtin_amdgcn_permlane16_swap(__float_as_uint(x), __float_as_uint(x), false, false); return __uint_as_float(s[0]) + __uint_as_float(s[1]); }
__device__ __forceinline__ float x32_sum(float x) { auto s = __builtin_amdgcn_permlane32_swap(__float_as_uint(x), __float_as_uint(x), false, false); return __uint_as_float(s[0]) + __uint_as_float(s[1]); }
__device__ __forceinline__ float x16_max(float x) { auto s = __builtin_amdgcn_permlane16_swap(__float_as_uint(x), __float_as_uint(x), false, false); return fmaxf(__uint_as_float(s[0]), __uint_as_float(s[1])); }
__device__ __forceinline__ float x32_max(float x) { auto s = __builtin_amdgcn_permlane32_swap(__float_as_uint(x), __float_as_uint(x), false, false); return fmaxf(__uint_as_float(s[0]), __uint_as_float(s[1])); }
__device__ __forceinline__ float wave_sum(float x) {
    x += dpp<0xB1>(x); x += dpp<0x4E>(x); x += dpp<0x141>(x); x += dpp<0x140>(x);
    return x32_sum(x16_sum(x));
}
template <int K> __device__ __forceinline__ v4u ld_sc1(__amdgpu_buffer_rsrc_t rs, unsigned off, unsigned so) { return __builtin_amdgcn_raw_buffer_load_b128(rs, off, so, ((LNT >> K) & 1) ? 18 : 16); }
template <int K> __device__ __forceinline__ void st_sc1(__amdgpu_buffer_rsrc_t rs, unsigned off, unsigned so, v4u v) { __builtin_amdgcn_raw_buffer_store_b128(v, rs, off, so, ((SNT >> K) & 1) ? 18 : 16); }
__device__ __forceinline__ unsigned opq(unsigned v) { asm("" : "+v"(v)); return v; }
#define CFENCE() asm volatile("" ::: "memory")
#define LDS16(off) (*(LAS v4u*)(size_t)(off))
#define LDS8(off) (*(LAS v2u*)(size_t)(off))
#define LDS4(off) (*(LAS unsigned*)(size_t)(off))
#define VLDS4(off) (*(volatile LAS unsigned*)(size_t)(off))
#define STAMP(role, i) do { } while (0)
#define STAMP_END() do { } while (0)

template <int NT> __device__ __forceinline__ void glds4(const void* base, unsigned dst, unsigned v0, unsigned v1, unsigned v2, unsigned v3) {
    if (NT) asm volatile("s_mov_b32 m0, %1\n\ts_nop 0\n\tglobal_load_lds_dwordx4 %2, %0 nt\n\tglobal_load_lds_dwordx4 %3, %0 offset:1024 nt\n\t"
                         "global_load_lds_dwordx4 %4, %0 offset:2048 nt\n\tglobal_load_lds_dwordx4 %5, %0 offset:3072 nt"
                         : : "s"(base), "s"(dst), "v"(v0), "v"(v1), "v"(v2), "v"(v3) : "memory", "m0");
    else    asm volatile("s_mov_b32 m0, %1\n\ts_nop 0\n\tglobal_load_lds_dwordx4 %2, %0\n\tglobal_load_lds_dwordx4 %3, %0 offset:1024\n\t"
                         "global_load_lds_dwordx4 %4, %0 offset:2048\n\tglobal_load_lds_dwordx4 %5, %0 offset:3072"
                         : : "s"(base), "s"(dst), "v"(v0), "v"(v1), "v"(v2), "v"(v3) : "memory", "m0");
}
template <int SLEEP> __device__ __forceinline__ void lds_wait_ge(unsigned off, unsigned need) {
#pragma clang loop unroll(disable)
    for (unsigned spin = 0; spin < SPIN_CAP; ++spin) {
        if ((unsigned)__builtin_amdgcn_readfirstlane(VLDS4(off)) >= need) break;
        if (SLEEP) __builtin_amdgcn_s_sleep(SLEEP);
    }
    CFENCE();
}
template <unsigned OFF> __device__ __forceinline__ void lds_arrive() {
    asm volatile("s_mov_b64 exec, 1\n\tds_add_u32 %0, %1 offset:%2\n\ts_mov_b64 exec, -1" : : "v"(0u), "v"(1u), "n"(OFF) : "memory");
}
__device__ __forceinline__ void lds_arrive_at(unsigned off) {
    asm volatile("s_mov_b64 exec, 1\n\tds_add_u32 %0, %1\n\ts_mov_b64 exec, -1" : : "v"(off), "v"(1u) : "memory");
}
#define KEEP4(a, i) asm volatile("" : "+v"(a[i]), "+v"(a[i + 1]), "+v"(a[i + 2]), "+v"(a[i + 3]) : : "memory")
#define SB() __builtin_amdgcn_sched_barrier(0)
#define VMWAIT(n) asm volatile("s_waitcnt vmcnt(%0)" : : "n"(n) : "memory")
#define PROG() do { if (WE == WL || early) VMWAIT(4 * (WE - 1)); else VMWAIT(4 * (WL - 1)); ++prog; VLDS4(F_PROG) = prog; if (STAMPS && prog == 16) STAMP(0, 2); } while (0)
__device__ __forceinline__ v4u enc4(f32x4 v, unsigned tag) {
    v4u o;
#pragma unroll
    for (int k = 0; k < 4; ++k) o[k] = ((__float_as_uint(v[k]) + 0x80u) & 0xffffff00u) | ((tag >> (8 * k)) & 0xffu);
    return o;
}
__device__ __forceinline__ v4u encb(unsigned p01, unsigned p23, unsigned tag) {
    return (v4u){__builtin_amdgcn_perm(p01, tag, 0x05040c00u), __builtin_amdgcn_perm(p01, tag, 0x07060c01u),
                 __builtin_amdgcn_perm(p23, tag, 0x05040c02u), __builtin_amdgcn_perm(p23, tag, 0x07060c03u)};
}
__device__ __forceinline__ unsigned bad4(v4u w, unsigned tag) {
    return (__builtin_amdgcn_perm(w.y, w.x, 0x0c0c0400u) | __builtin_amdgcn_perm(w.w, w.z, 0x04000c0cu)) ^ tag;
}
__device__ __forceinline__ f32x4 dec4(v4u w) {
    return (f32x4){__uint_as_float(w.x & 0xffffff00u), __uint_as_float(w.y & 0xffffff00u), __uint_as_float(w.z & 0xffffff00u), __uint_as_float(w.w & 0xffffff00u)};
}
__device__ __forceinline__ bool any_lane(unsigned v) { return __builtin_amdgcn_ballot_w64(v != 0u) != 0ull; }
__device__ __forceinline__ v2u hi16(v4u w) { return (v2u){__builtin_amdgcn_perm(w.y, w.x, 0x07060302u), __builtin_amdgcn_perm(w.w, w.z, 0x07060302u)}; }
__device__ __forceinline__ float x32_pair(float a, float b) { auto s = __builtin_amdgcn_permlane32_swap(__float_as_uint(a), __float_as_uint(b), false, false); return __uint_as_float(s[0]) + __uint_as_float(s[1]); }

#define IPAD(k) do { SB(); if (IWARM) asm volatile("s_branch .Lik" #k "\n.Lip" #k ":\n\ts_setpc_b64 s[94:95]\n.Lik" #k ":"); SB(); } while (0)
#define IPADP(k) do { SB(); if (IWARM) asm volatile("s_branch .Lik" #k "\n\t.p2align 12\n.Lip" #k ":\n\ts_setpc_b64 s[94:95]\n.Lik" #k ":"); SB(); } while (0)
#define ITOUCH(k) do { if (IWARM) asm volatile("s_call_b64 s[94:95], .Lip" #k : : : "s94", "s95"); } while (0)
#define ITOUCH8(w, a, b, c, d, e, f, g, h) do { if (IWARM) asm volatile( \
    "s_getpc_b64 s[92:93]\n\ts_lshl_b32 s94, %0, 2\n\ts_add_u32 s94, s94, 24\n\ts_add_u32 s92, s92, s94\n\ts_addc_u32 s93, s93, 0\n\t" \
    "s_swappc_b64 s[94:95], s[92:93]\n\ts_branch .Lit" #a "\n\t" \
    "s_branch .Lip" #a "\n\ts_branch .Lip" #b "\n\ts_branch .Lip" #c "\n\ts_branch .Lip" #d "\n\t" \
    "s_branch .Lip" #e "\n\ts_branch .Lip" #f "\n\ts_branch .Lip" #g "\n\ts_branch .Lip" #h "\n.Lit" #a ":" \
    : : "s"(w) : "s92", "s93", "s94", "s95", "scc"); } while (0)

__global__ void __attribute__((aligned(4096))) __launch_bounds__(NTHR, 1) fused_decode(Args a) {
    extern __shared__ __attribute__((aligned(16))) unsigned char smem[];
    const unsigned tid = threadIdx.x, lane = tid & 63u, wave = __builtin_amdgcn_readfirstlane(tid >> 6);
    const unsigned j = blockIdx.x, xcd = j & 7u, r = j >> 3;
    const unsigned n16 = lane & 15u, g = lane >> 4, q4 = n16 >> 2, p4 = lane & 3u;
    constexpr float LOG2E = 1.4426950408889634f;
    if (wave == 0) STAMP(0, 0);
    const bool early = xcd < 4u, help = wave == 1u || wave == 2u;
    const unsigned kb = 2u * ((r >> 2) & 3u) + (((xcd >> 1) ^ (r >> 4)) & 1u);
    const bool up = !early && kb < 7u && r < 16u, down = !early && kb < 7u && r >= 16u, prod = early || up;
    const unsigned hb = up ? kb * 8u + 4u * (xcd & 1u) + (r & 3u) : 0u;
    const unsigned hq = r < 16u ? ((xcd + 1u) & 3u) : xcd + 4u;
    v4u xv; unsigned ep; v2u b1v;
    asm volatile("s_cmp_lt_u32 %3, 3\n\ts_cbranch_scc1 .Lfa%=\n\tglobal_load_dwordx4 %0, %4, %5 offset:-3072\n\ts_branch .Lfb%=\n.Lfa%=:\n\t"
                 "s_cmp_eq_u32 %3, 0\n\ts_cbranch_scc1 .Lfb%=\n\tglobal_load_dword %1, %6, %7 sc1\n\tglobal_load_dwordx2 %2, %8, %9 offset:-32\n.Lfb%=:"
                 : "=&v"(xv), "=&v"(ep), "=&v"(b1v)
                 : "s"(wave), "v"(tid * 16u), "s"(a.x), "v"(0u), "s"(a.ws + WS_EPOCH), "v"(hb * 64u + (tid >> 4) * 8u), "s"(a.b1) : "memory", "scc");
    if (wave == 0) STAMP(0, 1);
    if (wave != 0) {
        if (help && prod) {
            const unsigned pitch = early ? 8192u : 3584u, st = 16u * pitch - 1024u;
            const unsigned q0 = (lane >> 2) * pitch + (((lane & 3u) ^ ((g & 1u) << 1)) << 4);
            const unsigned char* qb = (early ? (const unsigned char*)a.wq + hq * 1024u + (r & 15u) * 64u : (const unsigned char*)a.w1 + hb * 64u) + (wave - 1u) * 256u * pitch;
#pragma clang loop unroll(disable)
            for (unsigned k = 0; k < 4; ++k) glds4<WNT>(qb + k * 64u * pitch, L_P - 16384u + wave * 16384u + k * 4096u, q0, q0 + st, q0 + 2u * st, q0 + 3u * st);
        }
        if (wave == 3 && lane < 16) LDS4(L_FLG + lane * 4u) = 0u;
        asm volatile("s_waitcnt lgkmcnt(0)\n\ts_barrier" ::: "memory");
    }
    if (wave == 0) {
        __builtin_amdgcn_s_setprio(3);
        const size_t kvoff = ((size_t)((r >> 2) * 8u + xcd) * 4096u + 3072u + 256u * (r & 3u)) * 256u;
        const unsigned rot = r & 15u;
        const unsigned char* kp = (const unsigned char*)a.kc + kvoff;
        unsigned o[4];
#pragma unroll
        for (int i = 0; i < 4; ++i) o[i] = g * 256u + ((n16 ^ g ^ (4u * i)) << 4);
        KEEP4(o, 0);
        unsigned prog = IBX;
        const unsigned ib = (early ? WE : WL) + IBX;
#pragma clang loop unroll(disable)
        for (unsigned c = 0; c < 32; ++c) {
            if (c == 2) asm volatile("s_barrier ; l" ::: "memory");
            if (c == 16) {
                o[0] = o[2] = g * 256u + ((n16 ^ (g << 1)) << 4);
                o[1] = o[3] = g * 256u + ((n16 ^ ((4u + g) << 1)) << 4);
                kp = (const unsigned char*)a.vc + kvoff;
            }
            if (c >= ib) PROG();
            if (c == 24) lds_wait_ge<1>(F_XF, prod ? 2u : 0u);
            glds4<1>(kp + ((c + rot) & 15u) * 4096u, L_K + c * 4096u, o[0], o[1], o[2], o[3]);
        }
#pragma unroll
        for (int k = (WE > WL ? WE : WL) - 1; k >= 0; --k) { VMWAIT(4 * k); if (WE == WL || 32u - k > prog) VLDS4(F_PROG) = 32 - k; }
        STAMP(0, 5);
    }
    const __amdgpu_buffer_rsrc_t rs = __builtin_amdgcn_make_buffer_rsrc(a.ws, 0, WS_END, 0x00020000);
    const bool isq = wave == 1;
    const unsigned row = 4u * g + q4;
    const unsigned w = wave - 3u;
    if (wave >= 3) {
        asm volatile("s_waitcnt vmcnt(0)" : "+v"(xv) : : "memory");
        if (prod) {
            float v[8];
#pragma unroll
            for (int k = 0; k < 4; ++k) { v[2 * k] = bf_lo(xv[k]); v[2 * k + 1] = bf_hi(xv[k]); }
            const float s = ((v[0] + v[1]) + (v[2] + v[3])) + ((v[4] + v[5]) + (v[6] + v[7]));
            const float mean = wave_sum(s) * (1.f / 512.f);
            float q = 0.f;
#pragma unroll
            for (int k = 0; k < 8; ++k) { const float d = v[k] - mean; q = fmaf(d, d, q); }
            const float var = rbf(wave_sum(q) * (1.f / 512.f));
            const float mb = rbf(mean);
            const float den = rbf(sqrtf(rbf(var + 1e-5f)));
            const float rc = rcp(den);
            float o[8];
#pragma unroll
            for (int k = 0; k < 8; ++k) { const float t = rbf(v[k] - mb); const float q0 = t * rc; const float e = fmaf(-q0, den, t); o[k] = fmaf(e, rc, q0); }
            const unsigned xo = L_XN + w * 1024u + (((lane & ~3u) + 2u * (lane & 1u)) ^ (4u * (w & 3u))) * 16u + (lane & 2u) * 4u;
            LDS8(xo) = (v2u){pk_bf(o[0], o[1]), pk_bf(o[2], o[3])};
            LDS8(xo + 16) = (v2u){pk_bf(o[4], o[5]), pk_bf(o[6], o[7])};
            lds_arrive<F_XN>();
            STAMP(3, 2);
        }
    }
    unsigned tag = 0;
    if (help) {
        __builtin_amdgcn_s_setprio(2);
        asm volatile("s_waitcnt vmcnt(16)\n\ts_cmp_lg_u32 %2, 0\n\ts_cbranch_scc1 .Lw%=\n\ts_waitcnt vmcnt(0)\n.Lw%=:" : "+v"(ep), "+v"(b1v) : "s"(__builtin_amdgcn_readfirstlane(prod ? 1 : 0)) : "memory", "scc");
        tag = ep + 0x9E3779B1u;
        if (isq) LDS4(F_TAG) = tag;
        if (prod) {
            const unsigned ia = opq(L_P + row * 64u + (((2u * wave - 2u + (p4 >> 1)) ^ ((g & 1u) << 1)) << 4) + 8u * (p4 & 1u));
            v4u af[16], xf[16];
            VMWAIT(0);
            lds_arrive<F_W>();
            lds_wait_ge<0>(F_W, 2u);
            STAMP(1, 14);
#pragma unroll
            for (int k = 0; k < 16; ++k) af[k] = trfrag(ia + 2048 * k, ia + 2048 * k + 1024);
            KEEP4(af, 0); KEEP4(af, 4); KEEP4(af, 8); KEEP4(af, 12);
            lds_arrive<F_XF>();
            lds_wait_ge<0>(F_XN, 8u);
#pragma unroll
            for (int k = 0; k < 16; ++k) xf[k] = LDS16(L_XN + (n16 & 7u) * 1024u + (k >> 2) * 256 + (((4u * (k & 3) + g) ^ (4u * (n16 & 3u))) << 4));
            SB();
            f32x4 d[4];
#pragma unroll
            for (int k = 0; k < 16; ++k) d[k & 3] = mfma16(af[k], xf[k], k < 4 ? (f32x4){0.f, 0.f, 0.f, 0.f} : d[k & 3]);
            f32x4 e = (d[0] + d[1]) + (d[2] + d[3]);
            if (up) {
#pragma unroll
                for (int k = 0; k < 4; ++k) {
                    const float t = rbf(rbf(e[k]) + ((k & 1) ? bf_hi(b1v[k >> 1]) : bf_lo(b1v[k >> 1])));
                    const float u = t * fmaf(t * t, -2.f * LOG2E * 0.7978845608028654f * 0.044715f, -2.f * LOG2E * 0.7978845608028654f);
                    e[k] = t * rcp(1.f + ex2(u));
                }
            }
            const unsigned so = up ? WS_H - 512u + hb * 1024u + wave * 512u : WS_Q - 64u + hq * 2048u + (r & 15u) * 128u + wave * 64u;
            if (n16 < 8u) st_sc1<0>(rs, n16 * (up ? 64u : 16384u) + g * 16u, so, encb(pk_bf(e[0], e[1]), pk_bf(e[2], e[3]), tag));
            CFENCE();
            STAMP(1, 4);
        }
    }
    const unsigned xp = (xcd & 4u) | ((xcd - 1u) & 3u);
    const unsigned hh = (r >> 2) != xcd ? xp : (xcd ^ 2u);
    const unsigned cc = (r >> 2) != xcd ? (r >> 2) : xp;
    const unsigned ho = hh * 4u + (r & 3u);
    const unsigned w0 = (lane >> 3) * 1024u + (((lane & 7u) ^ (g << 1)) << 4);
    if (wave >= 3) {
        const unsigned va = opq(L_V + row * 256u + (((2u * w + (p4 >> 1)) ^ ((row & 7u) << 1)) << 4) + 8u * (p4 & 1u));
        const unsigned wa = opq(L_WO + row * 128u + (((2u * (w & 3u) + (p4 >> 1)) ^ (((row >> 1) & 3u) << 1)) << 4) + 8u * (p4 & 1u));
        {
            v4u kf[8], qf[4];
            lds_wait_ge<2>(F_PROG, 2u * w + 2u);
#pragma unroll
            for (int t = 0; t < 4; ++t) {
                const unsigned ka = L_K + w * 8192u + n16 * 256u + (((4u * t + g) ^ n16) << 4);
                kf[t] = LDS16(ka); kf[4 + t] = LDS16(ka + 4096);
            }
            KEEP4(kf, 0); KEEP4(kf, 4);
            if (w < 4u) lds_arrive<F_KF>();
            STAMP(3, 3);
            lds_wait_ge<QSL>(F_Q, 1u);
            STAMP(3, 5);
#pragma unroll
            for (int t = 0; t < 4; ++t) qf[t] = LDS16(L_Q + (n16 & 3u) * 272u + 16u * g + 64 * t);
            SB();
            f32x4 s0 = {0.f, 0.f, 0.f, 0.f}, s1 = {0.f, 0.f, 0.f, 0.f};
#pragma unroll
            for (int t = 0; t < 4; ++t) { s0 = mfma16(kf[t], qf[t], s0); s1 = mfma16(kf[4 + t], qf[t], s1); }
            const f32x4 s2 = (q4 & 2u) ? s1 : s0;
            const unsigned sr = pk_bf((q4 & 1u) ? s2[2] : s2[0], (q4 & 1u) ? s2[3] : s2[1]);
            const unsigned sc = pk_bf(bf_lo(sr) * 0.08838834764831845f, bf_hi(sr) * 0.08838834764831845f);
            float m = fmaxf(bf_lo(sc), bf_hi(sc));
            m = fmaxf(m, dpp<0x124>(m)); m = fmaxf(m, dpp<0x128>(m));
            const float ml = x32_max(x16_max(m)) * LOG2E;
            const float p0 = ex2(fmaf(bf_lo(sc), LOG2E, -ml)), p1 = ex2(fmaf(bf_hi(sc), LOG2E, -ml));
            LDS4(L_SC + w * 256u + (n16 & 3u) * 64u + g * 16u + q4 * 4u) = pk_bf(p0, p1);
            float l = p0 + p1;
            l += dpp<0x124>(l); l += dpp<0x128>(l);
            l = x32_sum(x16_sum(l));
            if (lane < 4u) LDS8(L_ML + lane * 64u + w * 8u) = (v2u){__float_as_uint(ml), __float_as_uint(l)};
        }
        lds_arrive<F_SC>();
        tag = LDS4(F_TAG);
        lds_wait_ge<0>(F_SC, 8u);
        STAMP(3, 7);
        {
            v4u P[8];
            const v4u ML = LDS16(L_ML + (n16 & 3u) * 64u + q4 * 16u);
#pragma unroll
            for (int t = 0; t < 8; ++t) P[t] = LDS16(L_SC + t * 256 + (n16 & 3u) * 64u + g * 16u);
            float M = fmaxf(__uint_as_float(ML.x), __uint_as_float(ML.z));
            M = fmaxf(M, dpp<0x124>(M)); M = fmaxf(M, dpp<0x128>(M));
            const float f0 = ex2(__uint_as_float(ML.x) - M), f1 = ex2(__uint_as_float(ML.z) - M);
            float L = fmaf(f0, __uint_as_float(ML.y), f1 * __uint_as_float(ML.w));
            L += dpp<0x128>(L); L += dpp<0x124>(L);
            const float inv = rcp(L);
            float lse = M + lg2(L), c[8];
            c[0] = f0 * inv; c[1] = f1 * inv;
            c[2] = dpp<0x12C>(c[0]); c[3] = dpp<0x12C>(c[1]);
            c[4] = dpp<0x128>(c[0]); c[5] = dpp<0x128>(c[1]);
            c[6] = dpp<0x124>(c[0]); c[7] = dpp<0x124>(c[1]);
            KEEP4(c, 0); KEEP4(c, 4); KEEP4(P, 0); KEEP4(P, 4);
            asm volatile("" : "+v"(lse));
            STAMP(3, 8);
            const f32x4 z = {0.f, 0.f, 0.f, 0.f};
            f32x4 o, d[4];
            v4u vf[4];
            lds_wait_ge<2>(F_PROG, 24u);
#pragma unroll
            for (int t = 0; t < 4; ++t) vf[t] = trfrag(va + 8192 * t, va + 8192 * t + 4096);
#pragma unroll
            for (int t = 0; t < 4; ++t) d[t] = mfma16(vf[t], P[t], z);
            o = (d[0] * c[0] + d[1] * c[1]) + (d[2] * c[2] + d[3] * c[3]);
            asm volatile("" : "+v"(o));
            lds_wait_ge<1>(F_PROG, 30u);
#pragma unroll
            for (int t = 0; t < 3; ++t) vf[t] = trfrag(va + 8192 * (4 + t), va + 8192 * (4 + t) + 4096);
#pragma unroll
            for (int t = 0; t < 3; ++t) d[t] = mfma16(vf[t], P[4 + t], z);
            o = (o + d[0] * c[4]) + (d[1] * c[5] + d[2] * c[6]);
            asm volatile("" : "+v"(o));
            lds_wait_ge<0>(F_PROG, 32u);
            vf[3] = trfrag(va + 8192 * 7, va + 8192 * 7 + 4096);
            d[3] = mfma16(vf[3], P[7], z);
            o += d[3] * c[7];
            v4u ov = {pk_bf(o[0], o[1]), pk_bf(o[2], o[3]), __float_as_uint(lse), tag};
            if (n16 < 4u) st_sc1<1>(rs, n16 * 16384u + g * 16u, WS_O + (xcd * 128u + r) * 512u + w * 64u, ov);
        }
        CFENCE();
        STAMP(3, 9);
        {
            const unsigned po = WS_O + ho * 16384u + w * 2048u;
            v4u A0, A1, af[4];
            if (ODL) __builtin_amdgcn_s_sleep(ODL);
            CFENCE();
            A0 = ld_sc1<1>(rs, lane * 16u, po); A1 = ld_sc1<1>(rs, lane * 16u + 1024u, po);
            lds_wait_ge<0>(F_WO, 1u);
#pragma unroll
            for (int t = 0; t < 4; ++t) af[t] = trfrag(wa + 4096 * t, wa + 4096 * t + 2048);
            KEEP4(af, 0);
#pragma clang loop unroll(disable)
            for (unsigned spin = 0;; ++spin) {
                const unsigned bad = (A0.w ^ tag) | (A1.w ^ tag);
                if (!any_lane(bad) || spin > SPIN_CAP) break;
                if (PSO) __builtin_amdgcn_s_sleep(PSO);
                CFENCE();
                if (bad) { A0 = ld_sc1<1>(rs, lane * 16u, po); A1 = ld_sc1<1>(rs, lane * 16u + 1024u, po); }
            }
            STAMP(3, 12);
            const float l0 = __uint_as_float(A0.z), l1 = __uint_as_float(A1.z);
            const float M = x32_max(fmaxf(l0, l1));
            const float e0 = ex2(l0 - M), e1 = ex2(l1 - M);
            const float it = rcp(x32_sum(e0 + e1)), c0 = e0 * it, c1w = e1 * it;
            const float lo = x32_pair(fmaf(c0, bf_lo(A0.x), c1w * bf_lo(A1.x)), fmaf(c0, bf_lo(A0.y), c1w * bf_lo(A1.y)));
            const float hi = x32_pair(fmaf(c0, bf_hi(A0.x), c1w * bf_hi(A1.x)), fmaf(c0, bf_hi(A0.y), c1w * bf_hi(A1.y)));
            LDS4(L_O + w * 272u + ((lane >> 3) & 3u) * 64u + p4 * 16u + ((lane >> 2) & 1u) * 8u + (lane >> 5) * 4u) = pk_bf(lo, hi);
            lds_arrive<F_O>();
            lds_wait_ge<0>(F_O, 8u);
            STAMP(3, 13);
            v4u bf[4];
#pragma unroll
            for (int t = 0; t < 4; ++t) bf[t] = LDS16(L_O + (n16 & 7u) * 272u + 16u * g + 64 * t);
            SB();
            f32x4 d0 = mfma16(af[0], bf[0], (f32x4){0.f, 0.f, 0.f, 0.f}), d1 = mfma16(af[1], bf[1], (f32x4){0.f, 0.f, 0.f, 0.f});
            d0 = mfma16(af[2], bf[2], d0); d1 = mfma16(af[3], bf[3], d1);
            if (q4 == (w >> 2)) st_sc1<2>(rs, n16 * 65536u + g * 16u, WS_S + ((w & 3u) * 8u + cc) * 2048u + ho * 64u, enc4(d0 + d1, tag));
            CFENCE();
            lds_arrive<F_S>();
            STAMP(3, 14);
        }
    }
    if (isq) {
        lds_wait_ge<2>(F_PROG, early ? QGE : QGL);
        STAMP(1, 1);
        const unsigned so = WS_Q + ((r >> 2) * 8u + xcd) * 2048u;
        v4u G0, G1;
        asm volatile("" : "=v"(G0), "=v"(G1));
        unsigned bad = 1;
#pragma clang loop unroll(disable)
        for (unsigned spin = 0;; ++spin) {
            CFENCE();
            if (bad) { G0 = ld_sc1<0>(rs, lane * 16u, so); G1 = ld_sc1<0>(rs, lane * 16u + 1024u, so); bad = bad4(G0, tag) | bad4(G1, tag); }
            if (STAMPS && spin == 0) STAMP(1, 2);
            if (!any_lane(bad) || spin > SPIN_CAP) break;
            if (PSQ) __builtin_amdgcn_s_sleep(PSQ);
        }
        const unsigned qd = L_Q + (lane >> 5) * 272u + (lane & 31u) * 8u;
        LDS8(qd) = hi16(G0);
        LDS8(qd + 544) = hi16(G1);
        CFENCE();
        VLDS4(F_Q) = 1u;
        STAMP(1, 11);
        const unsigned char* wb = (const unsigned char*)a.wo + ho * 131072u + 128u * cc;
        lds_wait_ge<2>(F_PROG, WOG);
#pragma clang loop unroll(disable)
        for (unsigned c = 0; c < 4; ++c) glds4<WNT>(wb + c * 32768u, L_WO + c * 4096u, w0, w0 + 7168u, w0 + 2u * 7168u, w0 + 3u * 7168u);
        VMWAIT(0);
        VLDS4(F_WO) = 1u;
        STAMP(1, 6);
    }
    if (wave == 2) {
        const unsigned fo = (r >> 2) * 512u + (xcd * 4u + (r & 3u)) * 16u + 4u * p4;
        v2u xfin = *(const GAS v2u*)(a.x + fo), b2v = *(const GAS v2u*)(a.b2 + (fo & 511u));
        unsigned tw2 = TWP ? *(const GAS unsigned*)(a.out + fo) : 0u;
        if (down) {
            const unsigned cg = ((xcd | 1u) + 4u * (xcd & 1u) + (r & 3u)) & 7u;
            const unsigned char* tb = (const unsigned char*)a.w2 + kb * 262144u + cg * 128u;
            lds_wait_ge<2>(F_PROG, W2G);
            lds_wait_ge<1>(F_KF, 4u);
#pragma clang loop unroll(disable)
            for (unsigned c = 0; c < 8; ++c) glds4<WNT>(tb + c * 32768u, L_W2 + c * 4096u, w0, w0 + 7168u, w0 + 2u * 7168u, w0 + 3u * 7168u);
            lds_wait_ge<2>(F_PROG, HG);
            STAMP(1, 1);
            const unsigned so = WS_H + kb * 8192u, ga = n16 * 64u + g * 16u;
            v4u G[8];
#pragma unroll
            for (int k = 0; k < 8; ++k) asm volatile("" : "=v"(G[k]));
            unsigned bad = 1;
#pragma clang loop unroll(disable)
            for (unsigned spin = 0;; ++spin) {
                unsigned pa = ga, pb = ga + 4096u;
                asm volatile("" : "+v"(pa), "+v"(pb) : : "memory");
                if (bad) {
#pragma unroll
                    for (int k = 0; k < 4; ++k) { G[k] = ld_sc1<0>(rs, pa + 1024 * k, so); G[4 + k] = ld_sc1<0>(rs, pb + 1024 * k, so); }
                    bad = 0;
#pragma unroll
                    for (int k = 0; k < 8; ++k) bad |= bad4(G[k], tag);
                }
                if (!any_lane(bad) || spin > SPIN_CAP) break;
                if (PSH) __builtin_amdgcn_s_sleep(PSH);
            }
            VMWAIT(0);
            STAMP(1, 11);
            f32x4 d[4];
            unsigned wt[4];
#pragma unroll
            for (int t = 0; t < 4; ++t) wt[t] = opq(L_W2 + row * 128u + (((2u * t + (p4 >> 1)) ^ (((row >> 1) & 3u) << 1)) << 4) + 8u * (p4 & 1u));
#pragma unroll
            for (int k = 0; k < 8; ++k) {
                const v2u own = hi16(G[k]), oth = {dppu<0x128>(own.x), dppu<0x128>(own.y)};
                const v4u hf = n16 < 8 ? (v4u){own.x, own.y, oth.x, oth.y} : (v4u){oth.x, oth.y, own.x, own.y};
#pragma unroll
                for (int t = 0; t < 4; ++t) d[t] = mfma16(trfrag(wt[t] + 4096 * k, wt[t] + 4096 * k + 2048), hf, k ? d[t] : (f32x4){0.f, 0.f, 0.f, 0.f});
            }
            const unsigned yo = WS_Y + (n16 & 7u) * 16384u + (n16 >> 3) * 4096u + cg * 512u + kb * 64u + g * 16u;
            st_sc1<3>(rs, yo, 0, enc4(n16 < 8 ? d[0] : d[1], tag));
            st_sc1<3>(rs, yo, 8192, enc4(n16 < 8 ? d[2] : d[3], tag));
            STAMP(2, 7);
        }
        asm volatile("" : "+v"(tw2), "+v"(xfin), "+v"(b2v));
        float gt[4] = {0.f, 0.f, 0.f, 0.f};
        unsigned po = lane < 28u ? WS_Y + j * 512u + lane * 16u : OOR, pb = OOR, fin = 0;
#pragma clang loop unroll(disable)
        for (;;) {
            asm volatile("" : "+s"(fin));
            lds_wait_ge<1>(fin ? F_S : F_PROG, fin ? 8u : YG);
            if (fin) __builtin_amdgcn_s_sleep(SDL); else __builtin_amdgcn_s_sleep(YDL);
            v4u A0, A1;
            asm volatile("" : "=v"(A0), "=v"(A1), "+v"(po), "+v"(pb));
            unsigned bad = 1;
#pragma clang loop unroll(disable)
            for (unsigned spin = 0;; ++spin) {
                CFENCE();
                if (bad) { A0 = ld_sc1<2>(rs, po, 0); A1 = ld_sc1<2>(rs, pb, 0); bad = (po != OOR ? bad4(A0, tag) : 0u) | (pb != OOR ? bad4(A1, tag) : 0u); }
                if (!any_lane(bad) || spin > SPIN_CAP) break;
                if (PSS) __builtin_amdgcn_s_sleep(PSS);
            }
            STAMP(2, 12);
            const f32x4 y = dec4(A0) + dec4(A1);
            float v[4], o[4];
#pragma unroll
            for (int k = 0; k < 4; ++k) {
                float t = y[k];
                t += dpp<0x124>(t); t += dpp<0x128>(t);
                v[k] = rbf(x32_sum(x16_sum(t)));
                const float xx = (k & 1) ? bf_hi(xfin[k >> 1]) : bf_lo(xfin[k >> 1]);
                o[k] = xx + rbf(v[k] * gt[k]);
            }
            if (fin) {
                if (lane < 4) *(GAS v2u*)(a.out + fo) = (v2u){pk_bf(o[0], o[1]), pk_bf(o[2], o[3])};
                if (j == 0 && lane == 0) __hip_atomic_store((GAS unsigned*)(a.ws + WS_EPOCH), tag, __ATOMIC_RELAXED, __HIP_MEMORY_SCOPE_AGENT);
                break;
            }
#pragma unroll
            for (int k = 0; k < 4; ++k) {
                const float bb = (k & 1) ? bf_hi(b2v[k >> 1]) : bf_lo(b2v[k >> 1]);
                const float ya = rbf(v[k] + bb);
                gt[k] = rbf(ya * rcp(1.f + ex2(-LOG2E * ya)));
            }
            po = WS_S + j * 2048u + lane * 16u; pb = po + 1024u; fin = 1;
            STAMP(2, 8);
        }
        STAMP(2, 13);
    }
    STAMP_END();
    (void)smem;
}

extern "C" void kernel_launch(void* const* d_in, const int* in_sizes, int n_in,
                              void* d_out, int out_size, void* d_ws, size_t ws_size,
                              hipStream_t stream) {
    Args a;
    a.x = (const u16*)d_in[0]; a.kc = (const u16*)d_in[1]; a.vc = (const u16*)d_in[2]; a.wq = (const u16*)d_in[3]; a.wo = (const u16*)d_in[4];
    a.w1 = (const u16*)d_in[6]; a.b1 = (const u16*)d_in[7]; a.w2 = (const u16*)d_in[8]; a.b2 = (const u16*)d_in[9];
    a.out = (u16*)d_out; a.ws = (unsigned char*)d_ws;
    (void)hipFuncSetAttribute((const void*)fused_decode, hipFuncAttributeMaxDynamicSharedMemorySize, L_BYTES);
    hipLaunchKernelGGL(fused_decode, dim3(NWG), dim3(NTHR), L_BYTES, stream, a);
}
```

```cpp
#include <hip/hip_runtime.h>
#include <hip/hip_bf16.h>
#include <stdint.h>

typedef unsigned short u16;
typedef unsigned v4u __attribute__((ext_vector_type(4)));
typedef unsigned v2u __attribute__((ext_vector_type(2)));
typedef float f32x4 __attribute__((ext_vector_type(4)));
typedef float f32x2 __attribute__((ext_vector_type(2)));
typedef __bf16 bf16x8 __attribute__((ext_vector_type(8)));
typedef __bf16 bf16x2 __attribute__((ext_vector_type(2)));
typedef unsigned v16u __attribute__((ext_vector_type(16)));
typedef short v4i16 __attribute__((ext_vector_type(4)));
#define GAS __attribute__((address_space(1)))
#define LAS __attribute__((address_space(3)))

#define STAMPS 0
#define W0 8u
#define WS 5u
#define WS2 2u
#define WS3 WS2
#define WH 14u
#define PA 125u
#define PT 128u
#define PTA 112u
#define PTB 144u
#define WSB 4u
#define KVP 0x100u, 0x200u, 0x8000u, 0x10000u, 0x400u, 0x20000u
#define TO 770u
#define TS 890u
#define TQ 0u
#define TH 0u
#define REF 1
#define ROTF (r + 2u * xcd)
#define PHF 0u
#define CS 8u
#define RW 0u
#define RMAX 300u
#define PD 0u
#define PD2 0u
#define LPR 3
#define PT2 112u
#define WTP 0
#define TPM 15u
#define WTA 16
#define WOFF 0u
#define PWQ 0
#define PWO 0
#define PWY 0
#define ZV ((v4u){0u, 0u, 0u, 0u})
#define OTA 0
#define WHK 8u
#define WHA WH
#define TVP 1
#define HG 14
#define QGL 9
#define QGE 10
#define IWARM 1
#define YG 1u
#define PSQ 4
#define PSH 4
#define ODL 0
#define PSO 0
#define YDL 0
#define SDL 4
#define PSS 0
#define WNT 1
#define SNT 0
#define LNT 0
#define VPF 1
#define QSL 0

constexpr int NWG = 256, NWAVE = 11, NTHR = NWAVE * 64;
constexpr unsigned WS_EPOCH = 0;
constexpr unsigned WS_REF = 2048;
constexpr unsigned QP = 5632;
constexpr unsigned WS_Q = 4096;
constexpr unsigned WS_H = WS_Q + 64 * QP;
constexpr unsigned WS_O = WS_H + 28 * 8192;
constexpr unsigned WS_S = WS_O + 32 * 16384;
constexpr unsigned WS_Y = WS_S + 256 * 4096;
constexpr unsigned WS_END = WS_Y + 256 * 2048;
constexpr unsigned WS_STAMP = 8u << 20;
constexpr unsigned L_FLG = 64;
constexpr unsigned F_PROG = L_FLG, F_XN = L_FLG + 4, F_Q = L_FLG + 8, F_SC = L_FLG + 12;
constexpr unsigned F_TAG = L_FLG + 16, F_G = L_FLG + 20, F_S = L_FLG + 24, F_O = L_FLG + 32;
constexpr unsigned F_XF = L_FLG + 36, F_REF = L_FLG + 40, F_T0 = L_FLG + 44;
constexpr unsigned PG0 = 8;
constexpr unsigned L_G = 0;
constexpr unsigned L_Q = 128;
constexpr unsigned L_O = L_Q + 4 * 272;
constexpr unsigned L_SC = L_O + 8 * 272;
constexpr unsigned L_ML = L_SC + 2048;
constexpr unsigned L_K = 6144;
constexpr unsigned L_V = L_K + 65536;
constexpr unsigned L_WO = L_V + 65536;
constexpr unsigned L_XN = L_WO + 16384;
constexpr unsigned L_P = L_V + 8 * 4096;
constexpr unsigned L_W2 = L_XN;
constexpr unsigned L_BYTES = L_XN + 8192 + 256;
static_assert(L_SC % 16 == 0 && L_ML + 256 <= L_K && L_BYTES <= 163840, "LDS");
static_assert(W0 <= 15 && W0 >= 8 && WH <= 15, "loader");
constexpr unsigned KVB[6] = {KVP};
constexpr unsigned KVM = 0x3ff00u & ~(KVB[0] | KVB[1] | KVB[2] | KVB[3] | KVB[4] | KVB[5]), KVL = KVM & (0u - KVM), KVS = KVL | (0x3ff00u & ~KVM & ~(KVL - 1u));
constexpr unsigned kvbit(int k) { unsigned m = KVM; for (int i = 0; i < k; ++i) m &= m - 1u; return m & (0u - m); }
constexpr unsigned kvd(unsigned i) { return (i & 1u) * KVB[2] + (i >> 1) * KVB[3] + 3072u - 1024u * i; }
static_assert(kvbit(3) != 0u && kvbit(4) == 0u, "K/V address bits");
constexpr unsigned OOR = 0x40000000u;
constexpr unsigned SPIN_CAP = 1u << 18;

struct Args {
    const u16* x; const u16* kc; const u16* vc; const u16* wq; const u16* w1; unsigned char* ws; const u16* wo; const u16* w2;
    const u16* b1; const u16* b2; u16* out;
    unsigned role[256];
};

__device__ __forceinline__ float bf_lo(unsigned u) { return __uint_as_float(u << 16); }
__device__ __forceinline__ float bf_hi(unsigned u) { return __uint_as_float(u & 0xffff0000u); }
__device__ __forceinline__ unsigned pk_bf(float lo, float hi) { bf16x2 v = {(__bf16)lo, (__bf16)hi}; return __builtin_bit_cast(unsigned, v); }
__device__ __forceinline__ float rbf(float f) { return (float)(__bf16)f; }
__device__ __forceinline__ float rcp(float f) { return __builtin_amdgcn_rcpf(f); }
__device__ __forceinline__ float ex2(float f) { return __builtin_amdgcn_exp2f(f); }
__device__ __forceinline__ float lg2(float f) { return __builtin_amdgcn_logf(f); }
__device__ __forceinline__ f32x4 mfma16(v4u a, v4u b, f32x4 c) {
    return __builtin_amdgcn_mfma_f32_16x16x32_bf16(__builtin_bit_cast(bf16x8, a), __builtin_bit_cast(bf16x8, b), c, 0, 0, 0);
}
__device__ __forceinline__ v2u trread(unsigned p) { return __builtin_bit_cast(v2u, __builtin_amdgcn_ds_read_tr16_b64_v4i16((LAS v4i16*)(size_t)p)); }
__device__ __forceinline__ v4u trfrag(unsigned p0, unsigned p1) { v2u a = trread(p0), b = trread(p1); return (v4u){a.x, a.y, b.x, b.y}; }
template <int CTRL> __device__ __forceinline__ unsigned dppu(unsigned x) { return (unsigned)__builtin_amdgcn_mov_dpp((int)x, CTRL, 0xf, 0xf, true); }
template <int CTRL> __device__ __forceinline__ float dpp(float x) { return __uint_as_float(dppu<CTRL>(__float_as_uint(x))); }
template <int CTRL> __device__ __forceinline__ v4u dpp4(v4u x) { return (v4u){dppu<CTRL>(x.x), dppu<CTRL>(x.y), dppu<CTRL>(x.z), dppu<CTRL>(x.w)}; }
__device__ __forceinline__ float x16_sum(float x) { auto s = __builtin_amdgcn_permlane16_swap(__float_as_uint(x), __float_as_uint(x), false, false); return __uint_as_float(s[0]) + __uint_as_float(s[1]); }
__device__ __forceinline__ float x32_sum(float x) { auto s = __builtin_amdgcn_permlane32_swap(__float_as_uint(x), __float_as_uint(x), false, false); return __uint_as_float(s[0]) + __uint_as_float(s[1]); }
__device__ __forceinline__ float vmax(unsigned a, unsigned b) { float r; asm("v_max_f32 %0, %1, %2" : "=v"(r) : "v"(a), "v"(b)); return r; }
__device__ __forceinline__ float x16_max(float x) { auto s = __builtin_amdgcn_permlane16_swap(__float_as_uint(x), __float_as_uint(x), false, false); return vmax(s[0], s[1]); }
__device__ __forceinline__ float x32_max(float x) { auto s = __builtin_amdgcn_permlane32_swap(__float_as_uint(x), __float_as_uint(x), false, false); return vmax(s[0], s[1]); }
__device__ __forceinline__ float q4_max(float x) {
    float r;
    asm("s_nop 1\n\tv_max_f32_dpp %0, %1, %1 row_ror:4 row_mask:0xf bank_mask:0xf\n\ts_nop 1\n\tv_max_f32_dpp %0, %0, %0 row_ror:8 row_mask:0xf bank_mask:0xf" : "=v"(r) : "v"(x));
    return r;
}
template <int K> __device__ __forceinline__ v4u ld_sc1(__amdgpu_buffer_rsrc_t rs, unsigned off, unsigned so) { return __builtin_amdgcn_raw_buffer_load_b128(rs, off, so, ((LNT >> K) & 1) ? 18 : 16); }
template <int K> __device__ __forceinline__ void st_sc1(__amdgpu_buffer_rsrc_t rs, unsigned off, unsigned so, v4u v) { __builtin_amdgcn_raw_buffer_store_b128(v, rs, off, so, ((SNT >> K) & 1) ? 18 : 16); }
__device__ __forceinline__ unsigned opq(unsigned v) { asm("" : "+v"(v)); return v; }
#define CFENCE() asm volatile("" ::: "memory")
#define LDS16(off) (*(LAS v4u*)(size_t)(off))
#define LDS8(off) (*(LAS v2u*)(size_t)(off))
#define LDS4(off) (*(LAS unsigned*)(size_t)(off))
#define LDS2(off) (*(LAS u16*)(size_t)(off))
#define VLDS4(off) (*(volatile LAS unsigned*)(size_t)(off))
#define STAMP(role, i) do { } while (0)
#define STAMP_END() do { } while (0)

#define WQC " nt"
#define WQ 5u
#define BLDS4(KVC) "buffer_load_dwordx4 %[v0], %[rs], %[ao] offen" KVC " lds\n\tbuffer_load_dwordx4 %[v1], %[rs], %[ao] offen offset:1024" KVC " lds\n\t" \
              "buffer_load_dwordx4 %[v2], %[rs], %[ao] offen offset:2048" KVC " lds\n\tbuffer_load_dwordx4 %[v3], %[rs], %[ao] offen offset:3072" KVC " lds\n\t"
#define PCA ".Lp%=:\n\ts_memrealtime s[92:93]\n\t"
#define PCB(c) "s_getreg_b32 %[n], hwreg(HW_REG_IB_STS)\n\ts_bfe_u32 %[h], %[n], 0x20016\n\ts_and_b32 %[n], %[n], 15\n\t" \
            "s_lshl4_add_u32 %[n], %[h], %[n]\n\tv_subrev_u32 %[p], %[n], %[pb]\n\ts_cmp_lt_u32 %[n], %[so]\n\ts_waitcnt lgkmcnt(0)\n\tds_write_b32 %[z], %[p] offset:%[fp]\n\t" \
            "s_cbranch_scc1 .Lg%=\n\ts_cmp_lt_u32 s92, %[nt]\n\ts_cbranch_scc1 .Lp%=\n\t" c "s_waitcnt vmcnt(%[wh])\n.Lg%=:\n\t" BLDS4(" nt")
#define PCD "s_sub_u32 %[h], %[dl], %[ap]\n\ts_min_u32 %[h], %[h], %[cs]\n\ts_add_u32 %[ap], %[ap], %[h]\n\ts_sub_u32 %[nt], %[nt], %[h]\n\t"
#define PCC "s_add_u32 %[nf], %[nf], %[tf]\n\ts_addc_u32 %[nt], %[nt], %[ti]\n\ts_add_u32 %[ao], %[ao], %[st]\n\ts_and_b32 %[ao], %[ao], %[mk]\n\tv_add_u32 %[pb], 4, %[pb]\n\t" \
            "s_add_u32 m0, m0, 0x1000\n\ts_cmp_lg_u32 m0, %[me]\n\ts_cbranch_scc1 .Lp%="
#define PCO : [n] "=&s"(n), [h] "=&s"(h), [p] "=&v"(p), [d] "=&v"(d), [ao] "+s"(ao), [nt] "+s"(nt), [nf] "+s"(nf), [pb] "+v"(pb), [dl] "+s"(dl), [ap] "+s"(ap) \
            : [rs] "s"(rs), [v0] "v"(v0), [v1] "v"(v1), [v2] "v"(v2), [v3] "v"(v3), [z] "v"(lz), [so] "n"(4u * SOFT - 3u), [fp] "n"(F_PROG), [fr] "n"(F_REF), [wh] "n"(4u * (HARD - 1u)), \
              [tf] "n"((T & 7u) << 29), [ti] "n"(T >> 3), [st] "n"(STEP), [mk] "n"(MASK), [me] "n"(MEND), [cs] "n"(CS), [fq] "n"(F_Q), [qc] "n"(4u * WQ - 3u) \
            : "memory", "scc", "m0", "s92", "s93"
template <unsigned SOFT, unsigned T, unsigned STEP, unsigned MASK, unsigned MEND, int RF = 0, unsigned HARD = WH>
__device__ __forceinline__ void paced(__amdgpu_buffer_rsrc_t rs, unsigned& ao, unsigned v0, unsigned v1, unsigned v2, unsigned v3, unsigned& nt, unsigned& nf, unsigned& pb, unsigned lz, unsigned& dl, unsigned& ap) {
    static_assert(SOFT > 0, "progress");
    unsigned n, h, p, d;
    if (RF == 1)           asm volatile(PCA "ds_read_b32 %[d], %[z] offset:%[fr]\n\t" PCB("") "v_readfirstlane_b32 %[dl], %[d]\n\t" PCD PCC PCO);
    else if (RF == 2 && WQ) asm volatile(PCA "ds_read_b32 %[d], %[z] offset:%[fq]\n\t" PCB("v_readfirstlane_b32 %[h], %[d]\n\ts_add_u32 %[h], %[h], %[qc]\n\ts_cmp_ge_u32 %[n], %[h]\n\ts_cbranch_scc1 .Lp%=\n\t") PCD PCC PCO);
    else if (RF == 2)      asm volatile(PCA PCB("") PCD PCC PCO);
    else                   asm volatile(PCA PCB("") PCC PCO);
}
#define LWH "s_mov_b32 %[c], 0x40000\n.Lw%=:\n\tds_read_b32 %[t], %[a] offset:%[o]\n\t"
#define LWM "s_sub_u32 %[c], %[c], 1\n\ts_cbranch_scc1 .Lx%=\n\ts_waitcnt lgkmcnt(0)\n\t"
#define LWT "v_cmp_gt_u32 vcc, %[n], %[t]\n\ts_cbranch_vccnz .Lw%=\n.Lx%=:\n\ts_waitcnt lgkmcnt(0)"
template <int SLEEP, unsigned OFF> __device__ __forceinline__ void lds_wait_at(unsigned a, unsigned need) {
    unsigned t, c;
    if (SLEEP) asm volatile(LWH LWM "v_cmp_gt_u32 vcc, %[n], %[t]\n\ts_cbranch_vccz .Lx%=\n\ts_sleep %[s]\n\ts_branch .Lw%=\n.Lx%=:\n\ts_waitcnt lgkmcnt(0)"
                            : [t] "=&v"(t), [c] "=&s"(c) : [a] "v"(a), [n] "s"(need), [o] "n"(OFF), [s] "n"(SLEEP) : "vcc", "scc", "memory");
    else       asm volatile(LWH LWM LWT : [t] "=&v"(t), [c] "=&s"(c) : [a] "v"(a), [n] "s"(need), [o] "n"(OFF) : "vcc", "scc", "memory");
}
template <int SLEEP, unsigned OFF> __device__ __forceinline__ void lds_wait(unsigned need) { lds_wait_at<SLEEP, OFF>(0u, OFF == F_PROG ? 4u * need : need); }
__device__ __forceinline__ bool lds_wait_qv(unsigned need) {
    unsigned t, u, c; unsigned long long r;
    asm volatile(LWH "ds_read_b32 %[u], %[a] offset:%[q]\n\t" LWM "v_cmp_ne_u32 vcc, 0, %[u]\n\ts_cbranch_vccnz .Lx%=\n\t" LWT "\n\ts_mov_b64 %[r], vcc"
                 : [t] "=&v"(t), [u] "=&v"(u), [c] "=&s"(c), [r] "=s"(r) : [a] "v"(0u), [n] "s"(4u * need), [o] "n"(F_PROG), [q] "n"(F_Q) : "vcc", "scc", "memory");
    return r != 0ull;
}
__device__ __forceinline__ void until(unsigned t) {
    asm volatile(".Lu%=:\n\ts_memrealtime s[92:93]\n\ts_waitcnt lgkmcnt(0)\n\ts_sub_u32 s92, s92, %0\n\ts_cmp_lt_i32 s92, 0\n\ts_cbranch_scc1 .Lu%=" : : "s"(t) : "memory", "scc", "s92", "s93");
}
template <unsigned T> __device__ __forceinline__ void until_ref(unsigned own) {
    unsigned v;
    asm volatile(".Lu%=:\n\tds_read_b32 %0, %2 offset:%3\n\ts_memrealtime s[92:93]\n\ts_waitcnt lgkmcnt(0)\n\tv_readfirstlane_b32 s93, %0\n\ts_cmp_eq_u32 s93, 0\n\t"
                 "s_cselect_b32 s93, %1, s93\n\ts_sub_u32 s92, s92, s93\n\ts_cmp_lt_i32 s92, %4\n\ts_cbranch_scc1 .Lu%="
                 : "=&v"(v) : "s"(own), "v"(0u), "n"(F_T0), "n"(T) : "memory", "scc", "s92", "s93");
}
template <unsigned OFF> __device__ __forceinline__ void lds_arrive() {
    asm volatile("s_mov_b64 exec, 1\n\tds_add_u32 %0, %1 offset:%2\n\ts_mov_b64 exec, -1" : : "v"(0u), "v"(1u), "n"(OFF) : "memory");
}
#define KEEP4(a, i) asm volatile("" : "+v"(a[i]), "+v"(a[i + 1]), "+v"(a[i + 2]), "+v"(a[i + 3]) : : "memory")
#define SB() __builtin_amdgcn_sched_barrier(0)
#define VMWAIT(n) asm volatile("s_waitcnt vmcnt(%0)" : : "n"(n) : "memory")
#define PTR(v, i) ((unsigned char*)(((unsigned long long)v[(i) + 1] << 32) | v[i]))
__device__ __forceinline__ v4u enc4(f32x4 v, unsigned tag) {
    v4u o;
#pragma unroll
    for (int k = 0; k < 4; ++k) o[k] = ((__float_as_uint(v[k]) + 0x80u) & 0xffffff00u) | ((tag >> (8 * k)) & 0xffu);
    return o;
}
__device__ __forceinline__ v4u tagv(unsigned tag) { return (v4u){tag & 0xffu, (tag >> 8) & 0xffu, (tag >> 16) & 0xffu, tag >> 24}; }
__device__ __forceinline__ unsigned or4(v4u z) { return (z.x | z.y) | (z.z | z.w); }
__device__ __forceinline__ f32x4 f4(v4u w) { return __builtin_bit_cast(f32x4, w); }
__device__ __forceinline__ bool any_lane(unsigned v) { return __builtin_amdgcn_ballot_w64(v != 0u) != 0ull; }
__device__ __forceinline__ float x16_pair(float a, float b) { auto s = __builtin_amdgcn_permlane16_swap(__float_as_uint(a), __float_as_uint(b), false, false); return __uint_as_float(s[0]) + __uint_as_float(s[1]); }
__device__ __forceinline__ float x32_pair(float a, float b) { auto s = __builtin_amdgcn_permlane32_swap(__float_as_uint(a), __float_as_uint(b), false, false); return __uint_as_float(s[0]) + __uint_as_float(s[1]); }

#define IPAD(k) do { SB(); if (IWARM) asm volatile(".long 0xbedd00ff\n.Lip" #k ":\n\ts_setpc_b64 s[94:95]" : : : "s93"); SB(); } while (0)
#define IPADP(k) do { SB(); if (IWARM) asm volatile("s_branch .Lik" #k "\n\t.p2align 12\n.Lip" #k ":\n\ts_setpc_b64 s[94:95]\n.Lik" #k ":"); SB(); } while (0)
#define ITOUCH(k) do { if (IWARM) asm volatile("s_call_b64 s[94:95], .Lip" #k : : : "s94", "s95"); } while (0)
#define IB(k) "s_branch .Lip" #k "\n\t"
#define ITOUCHW(w, LIST) "s_getpc_b64 s[92:93]\n\ts_lshl2_add_u32 s92, " w ", s92\n\ts_add_u32 s92, s92, 12\n\ts_swappc_b64 s[94:95], s[92:93]\n\ts_branch .Lit\n\t" LIST ".Lit:\n\t"

__global__ void __attribute__((aligned(4096))) __launch_bounds__(NTHR, 1) fused_decode(Args) {
    extern __shared__ __attribute__((aligned(16))) unsigned char smem[];
    v16u ka; v4u kb4; v2u kc2, ts; unsigned ro, rt;
    asm volatile("s_load_dwordx16 %0, %6, 0\n\ts_load_dwordx4 %1, %6, 0x40\n\ts_load_dwordx2 %2, %6, 0x50\n\t"
                 "s_lshl_b32 %4, %7, 7\n\ts_lshr_b32 %3, %7, 1\n\ts_or_b32 %4, %4, %3\n\ts_and_b32 %4, %4, 0x3fc\n\ts_load_dword %3, %6, %4 offset:0x58\n\ts_memrealtime %5"
                 : "=&{s[8:23]}"(ka), "=&s"(kb4), "=&s"(kc2), "=&s"(ro), "=&s"(rt), "=&{s[24:25]}"(ts) : "s"(__builtin_amdgcn_kernarg_segment_ptr()), "s"(blockIdx.x) : "scc");
    SB();
    const unsigned tid = threadIdx.x, lane = tid & 63u, wave = __builtin_amdgcn_readfirstlane(tid >> 6);
    const unsigned j = blockIdx.x, xcd = j & 7u, r = j >> 3;
    const unsigned n16 = lane & 15u, g = lane >> 4, q4 = n16 >> 2, p4 = lane & 3u;
    constexpr float LOG2E = 1.4426950408889634f;
    STAMP(0, 0);
    const unsigned w = wave - 3u;
    const bool early = xcd < 4u, help = wave == 1u || wave == 2u, comp = w < 8u;
    unsigned sw = ((lane & 7u) ^ (g << 1)) << 4, xo_ = tid * 16u, l8 = lane >> 3, o[4];
    unsigned kvoff = (((r >> 2) * 8u + xcd) * 4096u + 3072u) * 256u - 3072u + (r & 1u) * KVB[4] + ((r >> 1) & 1u) * KVB[5];
    const unsigned kg = (g & 1u) * KVB[0] + (g >> 1) * KVB[1];
#pragma unroll
    for (int i = 0; i < 4; ++i) o[i] = kg + ((n16 ^ g ^ (4u * i)) << 4) + kvd(i);
    v4u xv; unsigned ep, t0, t1, t2, t3;
    asm volatile("s_cmp_eq_u32 %[wv], 0\n\ts_cbranch_scc1 .Lld\n\ts_cmp_eq_u32 %[wv], 2\n\ts_cbranch_scc1 .Ltc\n\t"
                 "s_cmp_eq_u32 %[wv], 1\n\ts_waitcnt lgkmcnt(0)\n\ts_cbranch_scc0 .Lxl\n\tglobal_load_dword %[ep], %[z], s[18:19] sc1\n\t"
                 "s_cmp_gt_u32 %[j], 7\n\ts_cbranch_scc1 .Ltc\n\tv_mov_b32 %[t0], s24\n\tglobal_store_dword %[ra], %[t0], s[18:19] offset:%[wr] sc1\n\ts_branch .Ltc\n"
                 ".Lxl:\n\tglobal_load_dwordx4 %[xv], %[xo], s[8:9] offset:-3072\n.Ltc:\n\t"
                 ITOUCHW("%[wv]", IB(12) IB(11) IB(4) IB(5) IB(1) IB(6) IB(7) IB(2) IB(3) IB(8))
                 "s_waitcnt lgkmcnt(0)\n\ts_branch .Lpe\n"
                 ".Lld:\n\ts_setprio 3\n\ts_movk_i32 s94, 0x2000\n\ts_mov_b32 s95, 0xfc00\n\ts_mov_b32 m0, %[lp]\n\ts_waitcnt lgkmcnt(0)\n\t"
                 "s_bitcmp1_b32 %[ro], 0\n\ts_cselect_b64 s[92:93], s[16:17], s[14:15]\n\ts_cselect_b32 s94, 0xe00, s94\n\ts_cselect_b32 s95, 0x6c00, s95\n\t"
                 "s_and_b32 vcc_lo, %[ro], 0x3fff80\n\tv_mad_u32_u24 %[t0], %[l8], s94, %[sw]\n\ts_add_u32 s92, s92, vcc_lo\n\tv_add_u32 %[t1], s95, %[t0]\n\t"
                 "s_addc_u32 s93, s93, 0\n\tv_add_u32 %[t2], s95, %[t1]\n\ts_and_b32 vcc_hi, %[ro], 12\n\tv_add_u32 %[t3], s95, %[t2]\n\ts_cbranch_scc0 .Lpe\n\ts_lshl_b32 s94, s94, 5\n"
                 ".Lwl:\n\tglobal_load_lds_dwordx4 %[t0], s[92:93]" WQC "\n\tglobal_load_lds_dwordx4 %[t1], s[92:93] offset:1024" WQC "\n\t"
                 "global_load_lds_dwordx4 %[t2], s[92:93] offset:2048" WQC "\n\tglobal_load_lds_dwordx4 %[t3], s[92:93] offset:3072" WQC "\n\t"
                 "s_add_u32 s92, s92, s94\n\ts_addc_u32 s93, s93, 0\n\ts_add_u32 m0, m0, 0x1000\n\ts_sub_u32 vcc_hi, vcc_hi, 1\n\ts_cmp_lg_u32 vcc_hi, 0\n\ts_cbranch_scc1 .Lwl\n.Lpe:"
                 : [xv] "=&v"(xv), [ep] "=&v"(ep), [t0] "=&v"(t0), [t1] "=&v"(t1), [t2] "=&v"(t2), [t3] "=&v"(t3), "+{s[8:23]}"(ka), "+s"(kb4), "+s"(kc2), [ro] "+s"(ro),
                   [l8] "+v"(l8), [sw] "+v"(sw), "+v"(o[0]), "+v"(o[1]), "+v"(o[2]), "+v"(o[3]), "+s"(kvoff), "+{s[24:25]}"(ts)
                 : [wv] "s"(wave), [xo] "v"(xo_), [z] "v"(0u), [lp] "n"(L_P), [j] "s"(j), [ra] "v"((lane & 31u) * 64u + xcd * 4u), [wr] "n"(WS_REF), "s"(__builtin_amdgcn_kernarg_segment_ptr())
                 : "memory", "scc", "vcc", "m0", "s92", "s93", "s94", "s95");
    SB();
    static_assert(WS_EPOCH == 0 && WNT, "prologue");
    const bool up = ro & 1u;
    const unsigned nw = ro & 12u, rq = (ro >> 4) & 3u, rb = (ro >> 22) & 31u, cg = ro >> 27;
    unsigned char* const ws = PTR(ka, 10);
    STAMP(3, 10);
    const bool sx = (r >> 3) == (xcd >> 1);
    const unsigned hh = (xcd & 4u) | ((xcd + (sx ? 1u : 3u)) & 3u);
    const unsigned cc = (r >> 3) ^ (sx ? 1u : 0u);
    const unsigned ho = hh * 8u + (r & 7u);
    if (wave == 0) {
        const unsigned cw = W0 - nw;
        const unsigned rot = ROTF;
        unsigned ao = (rot & 1u) * kvbit(0) + ((rot >> 1) & 1u) * kvbit(1) + ((rot >> 2) & 1u) * kvbit(2) + ((rot >> 3) & 1u) * kvbit(3), nt, nf = 0, pb = 4u * (PG0 + cw), lz = 0, dl = 0, ap = 0;
        __amdgpu_buffer_rsrc_t rk = __builtin_amdgcn_make_buffer_rsrc(PTR(ka, 2) + kvoff, 0, 262144 + 3072, 0x00020000);
        asm volatile("s_mov_b32 m0, %[lk]\n\ts_cmp_eq_u32 %[n], 0\n\ts_cbranch_scc1 .Lbe\n.Lbl:\n\t" BLDS4(" nt")
                     "s_add_u32 %[ao], %[ao], %[st]\n\ts_and_b32 %[ao], %[ao], %[mk]\n\ts_add_u32 m0, m0, 0x1000\n\ts_cmp_lg_u32 m0, %[n]\n\ts_cbranch_scc1 .Lbl\n.Lbe:"
                     : [ao] "+s"(ao) : [rs] "s"(rk), [v0] "v"(o[0]), [v1] "v"(o[1]), [v2] "v"(o[2]), [v3] "v"(o[3]), [n] "s"(cw ? L_K + cw * 4096u : 0u), [lk] "n"(L_K), [st] "n"(KVS), [mk] "n"(KVM) : "memory", "scc", "m0");
        nt = ts.x + PA + ((cw * PT) >> 3) + (PHF);
        __builtin_amdgcn_s_setprio(LPR);
        STAMP(0, 11); asm volatile("s_barrier ; l" ::: "memory"); STAMP(0, 7);
        asm volatile("" : "+v"(pb), "+v"(lz));
        paced<WS, PT, KVS, KVM, L_V, REF, WHK>(rk, ao, o[0], o[1], o[2], o[3], nt, nf, pb, lz, dl, ap);
        o[0] = kg + ((n16 ^ (g << 1)) << 4);
        o[1] = kg + ((n16 ^ ((4u + g) << 1)) << 4);
        o[2] = o[0] + kvd(2); o[3] = o[1] + kvd(3); o[1] += kvd(1); o[0] += kvd(0);
        rk = __builtin_amdgcn_make_buffer_rsrc(PTR(ka, 4) + kvoff, 0, 262144 + 3072, 0x00020000);
        STAMP(0, 2);
        paced<WS, PTA, KVS, KVM, L_P, 2 * REF, WHA>(rk, ao, o[0], o[1], o[2], o[3], nt, nf, pb, lz, dl, ap);
        lds_wait<1, F_XF>(nw ? 2u : 0u);
        IPAD(11);
        paced<WSB, PTB, KVS, KVM, L_WO, 2 * REF>(rk, ao, o[0], o[1], o[2], o[3], nt, nf, pb, lz, dl, ap);
        STAMP(0, 4);
        const unsigned w0 = l8 * 1024u + sw;
        rk = __builtin_amdgcn_make_buffer_rsrc(PTR(ka, 14) + (rb == 31u ? 0u : rb * 65536u) + 128u * cg, 0, 65536, 0x00020000);
        ao = 0; nt += PD;
        asm volatile("s_mov_b32 m0, %0" : : "n"(L_W2) : "m0");
        paced<WS2, PT2, 0x8000u, 0xffffu, L_W2 + 8192u>(rk, ao, w0, w0 + 7168u, w0 + 2u * 7168u, w0 + 3u * 7168u, nt, nf, pb, lz, dl, ap);
        rk = __builtin_amdgcn_make_buffer_rsrc(PTR(ka, 12) + ho * 65536u + 256u * cc, 0, 65536, 0x00020000);
        nt += PD2;
        asm volatile("s_mov_b32 m0, %0" : : "n"(L_WO) : "m0");
        paced<WS3, PT2, 0x8000u, 0xffffu, L_WO + 8192u>(rk, ao, w0, w0 + 7168u, w0 + 2u * 7168u, w0 + 3u * 7168u, nt, nf, pb, lz, dl, ap);
        ao = 128u;
        paced<WS3, PT2, 0x8000u, 0xffffu, L_WO + 16384u>(rk, ao, w0, w0 + 7168u, w0 + 2u * 7168u, w0 + 3u * 7168u, nt, nf, pb, lz, dl, ap);
        STAMP(0, 1);
        if (WH > 14) { VMWAIT(56); VLDS4(F_PROG) = 4u * (PG0 + 24u); }
        if (WH > 8) { VMWAIT(32); VLDS4(F_PROG) = 4u * (PG0 + 30u); }
        VMWAIT(24); VLDS4(F_PROG) = 4u * (PG0 + 32u); STAMP(0, 5);
        VMWAIT(16); VLDS4(F_PROG) = 4u * (PG0 + 34u);
        VMWAIT(0); VLDS4(F_PROG) = 4u * (PG0 + 38u);
        STAMP(0, 3);
    } else {
        if (wave == 2 && lane < 16) LDS4(L_FLG + lane * 4u) = 0u;
        asm volatile("s_waitcnt lgkmcnt(0)\n\ts_barrier" ::: "memory");
        STAMP(3, 15);
    }
    const __amdgpu_buffer_rsrc_t rs = __builtin_amdgcn_make_buffer_rsrc(ws, 0, WS_END, 0x00020000);
    const bool isq = wave == 1;
    const unsigned row = 4u * g + q4;
    unsigned tag = 0;
    const unsigned hw = wave - 1u;
    const unsigned ta = row * 128u + 8u * (p4 & 1u), tx = ((row >> 1) & 3u) << 1;
    if (help) {
        IPADP(4);
        const unsigned fo = (r >> 2) * 512u + (xcd * 4u + (r & 3u)) * 16u + 4u * p4 + g;
        v2u b1v[2];
        unsigned fv;
        unsigned tw2 = 0, tw3 = 0, tw4 = 0, tw5 = 0;
        if (wave == 2) {
#pragma unroll
            for (int k = 0; k < 2; ++k) b1v[k] = *(const GAS v2u*)(PTR(kb4, 0) + (rb == 31u ? 0u : rb * 128u) + (n16 >> 3) * 32u + g * 8u + 64 * k);
            if (TVP == 2) {
                const unsigned char* tp = g < 2u ? PTR(ka, 4) + kvoff + 3072u + g * 65536u : g == 2u ? PTR(ka, 12) + ho * 65536u + 256u * cc : PTR(ka, 14) + (rb == 31u ? 0u : rb * 65536u) + 128u * cg;
                tw2 = __builtin_nontemporal_load((const GAS unsigned*)(tp + (n16 & TPM) * 4096u));
                tw3 = __builtin_nontemporal_load((const GAS unsigned*)(PTR(kc2, 0) + (fo & ~1u) * 2u));
            } else if (TVP) {
                const unsigned char* tp = g == 0u ? PTR(ka, 4) + kvoff + 3072u : g == 1u ? PTR(ka, 12) + ho * 65536u + 256u * cc
                                        : g == 2u || OTA ? PTR(ka, 14) + (rb == 31u ? 0u : rb * 65536u) + 128u * cg : PTR(kc2, 0) + (fo & ~1u) * 2u;
                tw2 = __builtin_nontemporal_load((const GAS unsigned*)tp);
            }
            if (PWY == 1) { const unsigned yo = rb != 31u ? WS_Y + (n16 & 7u) * 65536u + (n16 >> 3) * 16384u + cg * 2048u + rb * 64u + g * 16u : OOR; st_sc1<3>(rs, yo, 0, ZV); st_sc1<3>(rs, yo, 32768, ZV); CFENCE(); }
            if (OTA == 1) tw4 = *(const GAS unsigned*)(PTR(kc2, 0) + (fo & ~1u) * 2u);
            if (OTA >= 2) tw4 = __hip_atomic_load((const GAS unsigned*)(PTR(kc2, 0) + (fo & ~1u) * 2u), __ATOMIC_RELAXED, __HIP_MEMORY_SCOPE_AGENT);
            if (WTP == 3) tw3 = __builtin_amdgcn_raw_buffer_load_b32(rs, lane < 36u ? lane * 64u : OOR, WS_Q + j * 2304u, WTA);
            else if (WTP == 4) { tw3 = __builtin_amdgcn_raw_buffer_load_b32(rs, lane * 64u, WS_Q + j * 10496u, WTA); tw4 = __builtin_amdgcn_raw_buffer_load_b32(rs, lane * 64u + 4096u, WS_Q + j * 10496u, WTA);
                                 tw5 = __builtin_amdgcn_raw_buffer_load_b32(rs, lane < 36u ? lane * 64u + 8192u : OOR, WS_Q + j * 10496u, WTA); }
            else if (WTP) tw3 = __builtin_amdgcn_raw_buffer_load_b32(rs, WTP == 1 ? (lane < 21u ? lane * 4096u : OOR) : lane * 65536u, WTP == 1 ? r * 86016u + 64u : 64u, 16);
            CFENCE();
        }
        __builtin_amdgcn_s_setprio(2);
        if (isq) {
            asm volatile("s_waitcnt vmcnt(0)" : "+v"(ep) : : "memory");
            LDS4(F_TAG) = (ep + 0x9E3779B1u) | 0x01010101u;
            lds_arrive<F_XN>();
        }
        fv = *(const GAS u16*)(isq ? PTR(kb4, 2) + (fo & 511u) * 2u : PTR(ka, 0) + fo * 2u);
        CFENCE();
        if (nw) {
            unsigned i0 = L_P + ta + (((4u * hw + (p4 >> 1)) ^ tx) << 4), i1 = L_P + ta + (((4u * hw + 2u + (p4 >> 1)) ^ tx) << 4), xa[4];
#pragma unroll
            for (int k = 0; k < 4; ++k) xa[k] = L_XN + rq * 256u + (n16 & 7u) * 1024u + (((4u * k + g) ^ (4u * (n16 & 3u))) << 4);
            const unsigned col = up ? ((ro >> 7) & 0x7fffu) - rq * 3584u : (ro >> 7) & 63u, pl = nw == 4u ? rq : rq >> 1;
            unsigned so = up ? WS_H + col * 8192u + hw * 4096u + pl * 1024u
                             : WS_Q + (col >> 3) * QP + hw * 128u + (col & 7u) * 256u + (pl >= 2u ? 2304u + pl * 768u : pl * 2048u);
            unsigned sv = (n16 & 7u) * (up ? 64u : 8u * QP) + (n16 >> 3) * (up ? 512u : 64u) + g * 16u;
            asm volatile("" : "+v"(i0), "+v"(i1), "+v"(sv), "+s"(so));
            if (PWQ) { st_sc1<0>(rs, sv, so, ZV); CFENCE(); }
            KEEP4(xa, 0);
            v4u af[8], xf[8];
            lds_wait<0, F_XN>(9u);
            STAMP(1, 5);
            tag = LDS4(F_TAG);
#pragma unroll
            for (int k = 0; k < 8; ++k) xf[k] = LDS16(xa[k & 3] + (k >> 2) * 256);
            v4u tv = tagv(tag);
            KEEP4(xf, 0); KEEP4(xf, 4);
            asm volatile("" : "+v"(tv));
            IPAD(5);
            f32x4 d0 = {0.f, 0.f, 0.f, 0.f}, d1 = {0.f, 0.f, 0.f, 0.f};
            lds_wait<0, F_PROG>(PG0 + 4u - nw);
#pragma unroll
            for (int k = 0; k < 4; ++k) { af[k] = trfrag(i0 + 4096 * k, i0 + 4096 * k + 2048); af[4 + k] = trfrag(i1 + 4096 * k, i1 + 4096 * k + 2048); }
            SB();
#pragma unroll
            for (int k = 0; k < 4; ++k) { d0 = mfma16(af[k], xf[k], d0); d1 = mfma16(af[4 + k], xf[k], d1); }
            if (nw == 8u) {
                lds_wait<0, F_PROG>(PG0);
                STAMP(1, 14);
#pragma unroll
                for (int k = 0; k < 4; ++k) { af[k] = trfrag(i0 + 4096 * k + 16384, i0 + 4096 * k + 18432); af[4 + k] = trfrag(i1 + 4096 * k + 16384, i1 + 4096 * k + 18432); }
                SB();
#pragma unroll
                for (int k = 0; k < 4; ++k) { d0 = mfma16(af[k], xf[4 + k], d0); d1 = mfma16(af[4 + k], xf[4 + k], d1); }
            }
            st_sc1<0>(rs, sv, so, ((__builtin_bit_cast(v4u, n16 < 8u ? d0 : d1) + 0x80u) & 0xffffff00u) | tv);
            CFENCE();
            lds_arrive<F_XF>();
            STAMP(1, 4);
        }
        if (isq) {
            tag = LDS4(F_TAG);
            if (TQ) until_ref<TQ>(ts.x); else lds_wait<2, F_PROG>(PG0 + (early ? QGE : QGL));
            STAMP(1, 1);
            IPAD(6);
            const unsigned so = WS_Q + ((r >> 2) * 8u + xcd) * QP, xo = lane >= 16u ? lane * 16u + 3840u : OOR;
            const v4u ta_ = tagv(tag);
            v4u G[6], tb_ = tagv(lane >= 16u ? tag : 0u);
            asm volatile("" : "+v"(tb_));
#pragma unroll
            for (int k = 0; k < 6; ++k) asm volatile("" : "=v"(G[k]));
            unsigned bad = 1;
#pragma clang loop unroll(disable)
            for (unsigned spin = 0;; ++spin) {
                CFENCE();
                if (bad) {
#pragma unroll
                    for (int k = 0; k < 4; ++k) G[k] = ld_sc1<0>(rs, lane * 16u + 1024 * k, so);
                    G[4] = ld_sc1<0>(rs, xo, so); G[5] = ld_sc1<0>(rs, xo + 768u, so);
                    bad = 0;
#pragma unroll
                    for (int k = 0; k < 6; ++k) { G[k] ^= k < 4 ? ta_ : tb_; bad |= or4(G[k]); }
                    bad &= 0xffu;
                }
                if (STAMPS && spin == 0) STAMP(1, 2);
                if (!any_lane(bad) || spin > SPIN_CAP) break;
                if (PSQ) __builtin_amdgcn_s_sleep(PSQ);
            }
            const f32x4 q0 = (f4(G[0]) + f4(G[2])) + (f4(G[4]) + f4(G[5])), q1 = f4(G[1]) + f4(G[3]);
            const unsigned qd = L_Q + (lane >> 5) * 272u + (lane & 31u) * 8u;
            LDS8(qd) = (v2u){pk_bf(q0[0], q0[1]), pk_bf(q0[2], q0[3])};
            LDS8(qd + 544) = (v2u){pk_bf(q1[0], q1[1]), pk_bf(q1[2], q1[3])};
            CFENCE();
            VLDS4(F_Q) = 64u;
            STAMP(1, 11);
        }
        IPADP(7);
        if (wave == 2) {
            if (PWY == 2) { const unsigned yo = rb != 31u ? WS_Y + (n16 & 7u) * 65536u + (n16 >> 3) * 16384u + cg * 2048u + rb * 64u + g * 16u : OOR; st_sc1<3>(rs, yo, 0, ZV); st_sc1<3>(rs, yo, 32768, ZV); CFENCE(); }
            if (TH) until_ref<TH>(ts.x); else lds_wait<2, F_PROG>(PG0 + HG);
            lds_wait<1, F_XN>(1u);
            tag = LDS4(F_TAG);
            if (rb != 31u) {
                STAMP(1, 1);
                v4u hf[2];
                const unsigned so = WS_H + rb * 8192u, ga = n16 * 64u + g * 16u, gb = (rb & 3u) == 3u ? ga + 2048u : OOR;
                const v4u ta_ = tagv(tag), tb_ = tagv((rb & 3u) == 3u ? tag : 0u);
                v4u G[8];
#pragma unroll
                for (int k = 0; k < 8; ++k) asm volatile("" : "=v"(G[k]));
                unsigned bad = 1;
#pragma clang loop unroll(disable)
                for (unsigned spin = 0;; ++spin) {
                    CFENCE();
                    if (bad) {
#pragma unroll
                        for (int k = 0; k < 4; ++k) { G[k] = ld_sc1<0>(rs, ga + 1024 * (k & 1), so + 4096 * (k >> 1)); G[4 + k] = ld_sc1<0>(rs, gb + 1024 * (k & 1), so + 4096 * (k >> 1)); }
                        bad = 0;
#pragma unroll
                        for (int k = 0; k < 8; ++k) { G[k] ^= k < 4 ? ta_ : tb_; bad |= or4(G[k]); }
                        bad &= 0xffu;
                    }
                    if (!any_lane(bad) || spin > SPIN_CAP) break;
                    if (PSH) __builtin_amdgcn_s_sleep(PSH);
                }
                STAMP(1, 11);
                IPAD(8);
#pragma unroll
                for (int k = 0; k < 2; ++k) {
                    f32x4 e = (f4(G[2 * k]) + f4(G[2 * k + 1])) + (f4(G[4 + 2 * k]) + f4(G[5 + 2 * k]));
#pragma unroll
                    for (int i = 0; i < 4; ++i) {
                        const float t = rbf(rbf(e[i]) + ((i & 1) ? bf_hi(b1v[k][i >> 1]) : bf_lo(b1v[k][i >> 1])));
                        const float u = t * fmaf(t * t, -2.f * LOG2E * 0.7978845608028654f * 0.044715f, -2.f * LOG2E * 0.7978845608028654f);
                        e[i] = t * rcp(1.f + ex2(u));
                    }
                    const v2u own = {pk_bf(e[0], e[1]), pk_bf(e[2], e[3])}, oth = {dppu<0x128>(own.x), dppu<0x128>(own.y)};
                    hf[k] = n16 < 8 ? (v4u){own.x, own.y, oth.x, oth.y} : (v4u){oth.x, oth.y, own.x, own.y};
                }
                IPAD(9);
                unsigned wt[4], yo = WS_Y + (n16 & 7u) * 65536u + (n16 >> 3) * 16384u + cg * 2048u + rb * 64u + g * 16u;
#pragma unroll
                for (int t = 0; t < 4; ++t) wt[t] = L_W2 + ta + (((2u * t + (p4 >> 1)) ^ tx) << 4);
                KEEP4(wt, 0);
                asm volatile("" : "+v"(hf[0]), "+v"(hf[1]), "+v"(yo));
                lds_wait<1, F_PROG>(PG0 + 34u);
                f32x4 d[4];
#pragma unroll
                for (int k = 0; k < 2; ++k)
#pragma unroll
                    for (int t = 0; t < 4; ++t) d[t] = mfma16(trfrag(wt[t] + 4096 * k, wt[t] + 4096 * k + 2048), hf[k], k ? d[t] : (f32x4){0.f, 0.f, 0.f, 0.f});
                st_sc1<3>(rs, yo, 0, enc4(n16 < 8 ? d[0] : d[1], tag));
                st_sc1<3>(rs, yo, 32768, enc4(n16 < 8 ? d[2] : d[3], tag));
                STAMP(2, 7);
            }
            asm volatile("" : "+v"(tw2), "+v"(tw3), "+v"(tw4), "+v"(tw5));
        }
        unsigned po = (isq ? WS_Y + j * 2048u : WS_S + j * 4096u) + lane * 16u, pb = isq && lane >= 48u ? OOR : po + 1024u, pc = isq ? OOR : po + 2048u;
        unsigned ga = L_G + p4 * 16u + g * 4u, oa = fo * 2u;
        v4u ta_ = tagv(tag), tb_ = tagv(pb != OOR ? tag : 0u), tc_ = tagv(isq ? 0u : tag);
        fv <<= 16;
        asm volatile("" : "+v"(fv), "+v"(po), "+v"(pb), "+v"(pc), "+v"(ga), "+v"(oa), "+v"(ta_), "+v"(tb_), "+v"(tc_));
        lds_wait_at<1, 0>(isq ? F_O : F_S, isq ? YG : 8u);
        if (isq) __builtin_amdgcn_s_sleep(YDL); else { __builtin_amdgcn_s_sleep(SDL); if (TS) until(__builtin_amdgcn_readfirstlane(VLDS4(F_T0)) + TS); }
        IPAD(10);
        unsigned gf, gt;
        f32x4 y;
        v4u A0, A1, A2, A3;
        asm volatile("" : "=v"(A0), "=v"(A1), "=v"(A2), "=v"(A3));
        unsigned bad = 1;
#pragma clang loop unroll(disable)
        for (unsigned spin = 0;; ++spin) {
            CFENCE();
            gf = VLDS4(F_G); gt = VLDS4(ga);
            if (bad) {
                A0 = ld_sc1<2>(rs, po, 0) ^ ta_; A1 = ld_sc1<2>(rs, pb, 0) ^ tb_; A2 = ld_sc1<2>(rs, pc, 0) ^ tc_; A3 = ld_sc1<2>(rs, pc + 1024u, 0) ^ tc_;
                bad = ((or4(A0) | or4(A1)) | (or4(A2) | or4(A3))) & 0xffu;
            }
            if (!any_lane(bad) || spin > SPIN_CAP) break;
            if (PSS) __builtin_amdgcn_s_sleep(PSS);
        }
        STAMP(2, 12);
        y = (f4(A0) + f4(A1)) + (f4(A2) + f4(A3));
        float v = x32_pair(x16_pair(y[0], y[1]), x16_pair(y[2], y[3]));
        v += dpp<0x124>(v); v += dpp<0x128>(v);
        v = rbf(v);
        if (isq) {
            const float ya = rbf(v + __uint_as_float(fv));
            LDS4(ga) = __float_as_uint(rbf(ya * rcp(1.f + ex2(-LOG2E * ya))));
            CFENCE();
            VLDS4(F_G) = 1u;
            STAMP(1, 8);
        } else {
            if (!__builtin_amdgcn_readfirstlane(gf)) { lds_wait<0, F_G>(1u); gt = VLDS4(ga); }
            const u16 res = (u16)pk_bf(__uint_as_float(fv) + rbf(v * __uint_as_float(gt)), 0.f);
            if (q4 == 0u) { if (OTA == 3) __hip_atomic_store((GAS u16*)(PTR(kc2, 0) + oa), res, __ATOMIC_RELAXED, __HIP_MEMORY_SCOPE_AGENT); else *(GAS u16*)(PTR(kc2, 0) + oa) = res; }
            if (j == 0 && lane == 0) __hip_atomic_store((GAS unsigned*)(ws + WS_EPOCH), tag, __ATOMIC_RELAXED, __HIP_MEMORY_SCOPE_AGENT);
            STAMP(2, 13);
        }
    } else if (comp) {
        unsigned xo = L_XN + w * 1024u + (((lane & ~3u) + 2u * (lane & 1u)) ^ (4u * (w & 3u))) * 16u + (lane & 2u) * 4u;
        unsigned rf = 0;
        if (REF) rf = __builtin_amdgcn_raw_buffer_load_b32(rs, w == RW && lane < 8u ? WS_REF + r * 64u + lane * 4u : OOR, 0, 16);
        CFENCE();
        if (PWO == 1) {
            st_sc1<1>(rs, n16 < 4u ? n16 * 16384u + g * 16u : OOR, WS_O + (xcd * 128u + r) * 512u + w * 64u, ZV);
            st_sc1<2>(rs, n16 < 8u ? n16 * 131072u + g * 16u : OOR, WS_S + ((w & 3u) * 8u + 2u * cc + (w >> 2)) * 4096u + (ho & 3u) * 1024u + (ho >> 2) * 64u, ZV);
            CFENCE();
        }
        STAMP(3, 11);
        asm volatile("s_waitcnt vmcnt(%2)" : "+v"(xv), "+v"(xo) : "n"(REF + (PWO == 1 ? 2 : 0)) : "memory");
        STAMP(3, 1);
        IPAD(12);
        if (nw) {
            f32x2 v[4], d[4];
#pragma unroll
            for (int k = 0; k < 4; ++k) v[k] = (f32x2){bf_lo(xv[k]), bf_hi(xv[k])};
            const float x0 = __uint_as_float(__builtin_amdgcn_readfirstlane(xv[0]) << 16);
#pragma unroll
            for (int k = 0; k < 4; ++k) d[k] = v[k] - x0;
            const f32x2 s2 = (v[0] + v[1]) + (v[2] + v[3]), q2 = (d[0] * d[0] + d[1] * d[1]) + (d[2] * d[2] + d[3] * d[3]);
            float t = x16_sum(x32_pair(s2.x + s2.y, q2.x + q2.y));
            t += dpp<0xB1>(t); t += dpp<0x4E>(t); t += dpp<0x141>(t); t += dpp<0x140>(t);
            const float mean = __uint_as_float(__builtin_amdgcn_readlane(__float_as_uint(t), 0)) * (1.f / 512.f), md = mean - x0;
            const float var = rbf(vmax(__float_as_uint(fmaf(-md, md, __uint_as_float(__builtin_amdgcn_readlane(__float_as_uint(t), 32)) * (1.f / 512.f))), 0u));
            const float mb = rbf(mean);
            const float rc = rcp(rbf(__builtin_amdgcn_sqrtf(rbf(var + 1e-5f))));
            unsigned on[4];
#pragma unroll
            for (int k = 0; k < 4; ++k) { const f32x2 e = v[k] - mb; const unsigned u = opq(pk_bf(e.x, e.y)); const f32x2 z = (f32x2){bf_lo(u), bf_hi(u)} * rc; on[k] = pk_bf(z.x, z.y); }
            LDS8(xo) = (v2u){on[0], on[1]};
            LDS8(xo + 16) = (v2u){on[2], on[3]};
            lds_arrive<F_XN>();
            STAMP(3, 2);
        }
        if (REF && w == RW) {
            unsigned a = ts.x - rf;
            a = a < RMAX ? a : 0u;
            a = max(a, dppu<0xB1>(a)); a = max(a, dppu<0x4E>(a)); a = max(a, dppu<0x141>(a));
            if (lane == 0u) { VLDS4(F_REF) = a; VLDS4(F_T0) = ts.x - a; }
            STAMP(3, 4);
        }
        if (PWO == 2) {
            st_sc1<1>(rs, n16 < 4u ? n16 * 16384u + g * 16u : OOR, WS_O + (xcd * 128u + r) * 512u + w * 64u, ZV);
            st_sc1<2>(rs, n16 < 8u ? n16 * 131072u + g * 16u : OOR, WS_S + ((w & 3u) * 8u + 2u * cc + (w >> 2)) * 4096u + (ho & 3u) * 1024u + (ho >> 2) * 64u, ZV);
            CFENCE();
        }
        if (w == 6u) ITOUCH(9);
        if (w == 7u) ITOUCH(10);
        const unsigned va = opq(L_V + row * 256u + (((2u * w + (p4 >> 1)) ^ ((row & 7u) << 1)) << 4) + 8u * (p4 & 1u));
        const unsigned wa = opq(L_WO + (w >> 2) * 8192u + ta + (((2u * (w & 3u) + (p4 >> 1)) ^ tx) << 4));
        v4u vf[8];
        unsigned vs = 0;
        unsigned pa = L_SC + (n16 & 3u) * 64u + g * 16u, ov_ = n16 < 4u ? n16 * 16384u + g * 16u : OOR, os = WS_O + (xcd * 128u + r) * 512u + w * 64u;
#define VRD(a, b) _Pragma("unroll") for (int t = a; t < b; ++t) vf[t] = trfrag(va + 8192 * t, va + 8192 * t + 4096)
        {
            v4u kf[8], qf[4];
            unsigned qa = L_Q + (n16 & 3u) * 272u + 16u * g, pw = pa + w * 256u + q4 * 4u, mw = L_ML + lane * 64u + w * 8u;
            lds_wait<2, F_PROG>(PG0 + 2u * w + 2u);
#pragma unroll
            for (int t = 0; t < 4; ++t) {
                const unsigned ka = L_K + w * 8192u + n16 * 256u + (((4u * t + g) ^ n16) << 4);
                kf[t] = LDS16(ka); kf[4 + t] = LDS16(ka + 4096);
            }
            KEEP4(kf, 0); KEEP4(kf, 4);
            asm volatile("" : "+v"(qa), "+v"(pw), "+v"(mw), "+v"(pa), "+v"(ov_), "+s"(os));
            if (PWO == 3) { st_sc1<1>(rs, ov_, os, ZV); st_sc1<2>(rs, n16 < 8u ? n16 * 131072u + g * 16u : OOR, WS_S + ((w & 3u) * 8u + 2u * cc + (w >> 2)) * 4096u + (ho & 3u) * 1024u + (ho >> 2) * 64u, ZV); CFENCE(); }
            STAMP(3, 3);
            IPADP(1);
#pragma unroll
            for (int t = 0; t < 8; ++t) asm volatile("" : "=v"(vf[t]));
            if (!VPF) lds_wait<QSL, F_Q>(1u);
            else if (!lds_wait_qv(PG0 + 24u)) {
                VRD(0, 4); vs = 1;
                if (!lds_wait_qv(PG0 + 30u)) {
                    VRD(4, 7); vs = 2;
                    if (!lds_wait_qv(PG0 + 32u)) { VRD(7, 8); vs = 3; lds_wait<0, F_Q>(1u); }
                }
            }
            STAMP(3, 5);
#pragma unroll
            for (int t = 0; t < 4; ++t) qf[t] = LDS16(qa + 64 * t);
            tag = LDS4(F_TAG);
            SB();
            f32x4 s0 = {0.f, 0.f, 0.f, 0.f}, s1 = {0.f, 0.f, 0.f, 0.f};
#pragma unroll
            for (int t = 0; t < 4; ++t) { s0 = mfma16(kf[t], qf[t], s0); s1 = mfma16(kf[4 + t], qf[t], s1); }
            const f32x4 s2 = (q4 & 2u) ? s1 : s0;
            const unsigned sr = opq(pk_bf((q4 & 1u) ? s2[2] : s2[0], (q4 & 1u) ? s2[3] : s2[1]));
            const unsigned sc = pk_bf(bf_lo(sr) * 0.08838834764831845f, bf_hi(sr) * 0.08838834764831845f);
            const float ml = x32_max(x16_max(q4_max(vmax(sc << 16, sc & 0xffff0000u)))) * LOG2E;
            const float p0 = ex2(fmaf(bf_lo(sc), LOG2E, -ml)), p1 = ex2(fmaf(bf_hi(sc), LOG2E, -ml));
            LDS4(pw) = pk_bf(p0, p1);
            float l = p0 + p1;
            l += dpp<0x124>(l); l += dpp<0x128>(l);
            l = x32_sum(x16_sum(l));
            if (lane < 4u) LDS8(mw) = (v2u){__float_as_uint(ml), __float_as_uint(l)};
        }
        lds_arrive<F_SC>();
        unsigned ma = L_ML + (n16 & 3u) * 64u + q4 * 16u;
        asm volatile("" : "+v"(ma));
        lds_wait<0, F_SC>(8u);
        STAMP(3, 7);
        IPAD(2);
        {
            v4u P[8];
            const v4u ML = LDS16(ma);
            if (TO) t0 = LDS4(F_T0);
#pragma unroll
            for (int t = 0; t < 8; ++t) P[t] = LDS16(pa + t * 256);
            SB();
            if (vs < 1u) { lds_wait<2, F_PROG>(PG0 + 24u); VRD(0, 4); SB(); }
            const f32x4 z = {0.f, 0.f, 0.f, 0.f};
            f32x4 o, d[4];
            const float M = q4_max(vmax(ML.x, ML.z));
            const float f0 = ex2(__uint_as_float(ML.x) - M), f1 = ex2(__uint_as_float(ML.z) - M);
            float L = fmaf(f0, __uint_as_float(ML.y), f1 * __uint_as_float(ML.w));
            L += dpp<0x128>(L); L += dpp<0x124>(L);
            const float inv = rcp(L);
            float lse = M + lg2(L), c[8];
            c[0] = f0 * inv; c[1] = f1 * inv;
            c[2] = dpp<0x12C>(c[0]); c[3] = dpp<0x12C>(c[1]);
            c[4] = dpp<0x128>(c[0]); c[5] = dpp<0x128>(c[1]);
            c[6] = dpp<0x124>(c[0]); c[7] = dpp<0x124>(c[1]);
#pragma unroll
            for (int t = 0; t < 4; ++t) d[t] = mfma16(vf[t], P[t], z);
            o = (d[0] * c[0] + d[1] * c[1]) + (d[2] * c[2] + d[3] * c[3]);
            KEEP4(c, 4); KEEP4(P, 4);
            asm volatile("" : "+v"(o), "+v"(lse));
            STAMP(3, 8);
            if (vs < 2u) { lds_wait<1, F_PROG>(PG0 + 30u); VRD(4, 7); SB(); }
#pragma unroll
            for (int t = 0; t < 3; ++t) d[t] = mfma16(vf[4 + t], P[4 + t], z);
            o = (o + d[0] * c[4]) + (d[1] * c[5] + d[2] * c[6]);
            asm volatile("" : "+v"(o));
            if (vs < 3u) {
                unsigned t_, c_; v2u a_, b_;
                asm volatile(LWH "ds_read_b64_tr_b16 %[x], %[v] offset:57344\n\tds_read_b64_tr_b16 %[y], %[v] offset:61440\n\t" LWM LWT
                             : [t] "=&v"(t_), [c] "=&s"(c_), [x] "=&v"(a_), [y] "=&v"(b_) : [a] "v"(0u), [n] "s"(4u * (PG0 + 32u)), [o] "n"(F_PROG), [v] "v"(va) : "vcc", "scc", "memory");
                vf[7] = (v4u){a_.x, a_.y, b_.x, b_.y};
            }
            d[3] = mfma16(vf[7], P[7], z);
            o += d[3] * c[7];
            v4u ov = {pk_bf(o[0], o[1]), pk_bf(o[2], o[3]), __float_as_uint(lse), tag};
            st_sc1<1>(rs, ov_, os, ov);
        }
        CFENCE();
        STAMP(3, 9);
        IPAD(3);
        {
            const unsigned po = WS_O + (ho >> 1) * 16384u + w * 2048u + (ho & 1u) * 256u, lo = g * 512u + n16 * 16u;
            v4u A0, af[2];
            if (ODL) __builtin_amdgcn_s_sleep(ODL);
            if (TO) until(__builtin_amdgcn_readfirstlane(t0) + TO);
            CFENCE();
            A0 = ld_sc1<1>(rs, lo, po);
            unsigned ow = L_O + w * 272u + (n16 >> 3) * 64u + p4 * 16u + (q4 & 1u) * 8u + g * 2u, orr = L_O + (n16 & 7u) * 272u + 16u * g;
            unsigned sv = n16 < 8u ? n16 * 131072u + g * 16u : OOR, ss = WS_S + ((w & 3u) * 8u + 2u * cc + (w >> 2)) * 4096u + (ho & 3u) * 1024u + (ho >> 2) * 64u;
            v4u tv = tagv(tag);
            unsigned wp;
            asm volatile("" : "+v"(ow), "+v"(orr), "+v"(sv), "+s"(ss), "+v"(tv) : : "memory");
#pragma clang loop unroll(disable)
            for (unsigned spin = 0;; ++spin) {
                const unsigned bad = A0.w ^ tag;
                if (!any_lane(bad) || spin > SPIN_CAP) break;
                if (PSO) __builtin_amdgcn_s_sleep(PSO);
                CFENCE();
                if (bad) A0 = ld_sc1<1>(rs, lo, po);
            }
            wp = VLDS4(F_PROG);
            CFENCE();
#pragma unroll
            for (int t = 0; t < 2; ++t) af[t] = trfrag(wa + 4096 * t, wa + 4096 * t + 2048);
            SB();
            STAMP(3, 12);
            const float l0 = __uint_as_float(A0.z);
            const float e0 = ex2(l0 - x32_max(x16_max(l0)));
            const float it = rcp(x32_sum(x16_sum(e0)));
            const float o = x32_pair(x16_pair(e0 * bf_lo(A0.x), e0 * bf_hi(A0.x)), x16_pair(e0 * bf_lo(A0.y), e0 * bf_hi(A0.y)));
            LDS2(ow) = (u16)pk_bf(o * it, 0.f);
            lds_arrive<F_O>();
            if (__builtin_amdgcn_readfirstlane(wp) < 4u * (PG0 + 38u)) {
                lds_wait<0, F_PROG>(PG0 + 38u);
#pragma unroll
                for (int t = 0; t < 2; ++t) af[t] = trfrag(wa + 4096 * t, wa + 4096 * t + 2048);
                asm volatile("" : "+v"(af[0]), "+v"(af[1]) : : "memory");
            }
            v4u bf[2];
            {
                unsigned t_, c_;
                asm volatile(LWH "ds_read_b128 %[x], %[v]\n\tds_read_b128 %[y], %[v] offset:64\n\t" LWM LWT
                             : [t] "=&v"(t_), [c] "=&s"(c_), [x] "=&v"(bf[0]), [y] "=&v"(bf[1]) : [a] "v"(0u), [n] "s"(8u), [o] "n"(F_O), [v] "v"(orr) : "vcc", "scc", "memory");
            }
            STAMP(3, 13);
            f32x4 d0 = mfma16(af[0], bf[0], (f32x4){0.f, 0.f, 0.f, 0.f});
            d0 = mfma16(af[1], bf[1], d0);
            st_sc1<2>(rs, sv, ss, ((__builtin_bit_cast(v4u, d0) + 0x80u) & 0xffffff00u) | tv);
            CFENCE();
            lds_arrive<F_S>();
            STAMP(3, 14);
        }
    }
    STAMP_END();
    (void)smem;
}

static const unsigned ROLE_TAB[256] = {
    0xefd6009u, 0x9b1000au, 0x279400au, 0xf51000bu, 0x6f52012u, 0xe394012u, 0x9352013u, 0xb452019u,
    0x2b5201au, 0xcb1001bu, 0x335201bu, 0x8752021u, 0x97d6021u, 0xaf94022u, 0xabd6022u, 0x5710023u,
    0x3552029u, 0xf4d6029u, 0x631002au, 0x239402au, 0xf69402bu, 0x75d602bu, 0x8f52032u, 0x2f10033u,
    0xc752033u, 0x95d6033u, 0xf310039u, 0x9652039u, 0x835203au, 0x779403au, 0x341003bu, 0xd7d603bu,
    0x6e20000u, 0x6520004u, 0xe020006u, 0x48a4006u, 0x0420007u, 0x11a4007u, 0xce20010u, 0x00a4014u,
    0x09a4016u, 0xa4a4018u, 0x08a401cu, 0x9120025u, 0x4e20027u, 0xc0a4028u, 0xc4a402eu, 0xf120034u,
    0xaa20038u, 0xada403du, 0xc22003fu, 0x68a403fu, 0x0228006u, 0x4da9c06u, 0xee2800cu, 0xcda9c0cu,
    0x6a28010u, 0x84a9c10u, 0x7128012u, 0xe1a9c12u, 0x0d28016u, 0xa1a9c16u, 0x852801au, 0xb1a9c1au,
    0x01a4000u, 0x602000du, 0xe5a400fu, 0x8920018u, 0x0c2001du, 0xc82001eu, 0xe2a401eu, 0xc9a401fu,
    0x2920020u, 0x6620024u, 0x82a4024u, 0x64a4026u, 0xbaa4027u, 0x3020028u, 0xed2002cu, 0x88a402cu,
    0x812002du, 0x2220030u, 0x8ca4034u, 0x8d20035u, 0x21a4036u, 0x2da4038u, 0xa92003du, 0xc12003eu,
    0xb02800au, 0x62a9c0au, 0x6c2800eu, 0xa2a9c0eu, 0xac28011u, 0x61a9c11u, 0x2028014u, 0xe9a9c14u,
    0x1f20005u, 0x1f20008u, 0x1fa4008u, 0x1f2000cu, 0x1fa400du, 0x122000fu, 0xcfd6012u, 0xb210013u,
    0xa394013u, 0x4020015u, 0xd1a4015u, 0x2c20016u, 0x8ea4017u, 0x51a4020u, 0x46a4025u, 0xa020026u,
    0xa6a402du, 0x102002eu, 0x4120036u, 0x50a4037u, 0x802003cu, 0x86a403cu, 0xcc28002u, 0x26a9c02u,
    0xa828004u, 0x8aa9c04u, 0xea28005u, 0x4aa9c05u, 0x0628009u, 0x90a9c09u, 0x0a2800du, 0xe8a9c0du,
    0x1fa4004u, 0x1fa4005u, 0x1f9400bu, 0x1fa400cu, 0x1f2000eu, 0x4ca400eu, 0x24a4010u, 0x3120014u,
    0xec20017u, 0xa52001cu, 0x45a401du, 0x052001fu, 0xc35202bu, 0xca2002fu, 0x2aa402fu, 0xe6a4030u,
    0xd694031u, 0x25a4035u, 0x4920037u, 0x44a403eu, 0xae28000u, 0xe4a9c00u, 0xc628001u, 0x0ea9c01u,
    0x6d28008u, 0xd0a9c08u, 0xf028015u, 0x70a9c15u, 0x2e28018u, 0x69a9c18u, 0xc528019u, 0x42a9c19u,
    0x1f18003u, 0x1f99c03u, 0x1fdaa03u, 0x1f18007u, 0x1fdaa07u, 0x1f58e0bu, 0x1f99c0bu, 0x1fdaa0bu,
    0x1f1800fu, 0x1f58e0fu, 0x1f99c0fu, 0x1f18013u, 0x1f58e13u, 0x1f99c13u, 0x1fdaa13u, 0x1f18017u,
    0x1f58e17u, 0x1fdaa17u, 0x1f1801bu, 0x1f58e1bu, 0x1f99c1bu, 0x1fdaa1bu, 0x1a00000u, 0x9a00000u,
    0x2800000u, 0xf900000u, 0xda00000u, 0x7a00000u, 0x3a00000u, 0x5a00000u, 0xd900000u, 0xfa00000u,
    0x9952001u, 0xa794001u, 0xb610002u, 0x6794002u, 0x76d6002u, 0x0710003u, 0x1794003u, 0x9252009u,
    0x5594009u, 0x37d600au, 0x035200bu, 0x0b94011u, 0xf8d6013u, 0xe7d6019u, 0xf21001au, 0xeb9401au,
    0x13d601au, 0x789401bu, 0xb7d601bu, 0x1594021u, 0x4b94023u, 0x6bd6023u, 0x1894029u, 0x0f5202au,
    0x4fd602au, 0x431002bu, 0xf794039u, 0xfbd6039u, 0x741003au, 0x54d603au, 0x475203bu, 0x8b9403bu,
    0xd510001u, 0xd4d6001u, 0xbb52002u, 0x7b52003u, 0x98d6003u, 0x5810009u, 0x395200au, 0x5bd600bu,
    0x5210011u, 0x3852011u, 0x79d6011u, 0x9410012u, 0x1910019u, 0x3b94019u, 0xd310021u, 0xb910022u,
    0xb552022u, 0x5952023u, 0x3210029u, 0x1410031u, 0xb352031u, 0xd2d6031u, 0x1b10032u, 0x5694032u,
    0x73d6032u, 0x3694033u, 0x5358e03u, 0xdb58e07u, 0xb899c07u, 0xd81800bu, 0x72daa0fu, 0x1699c17u,
};

extern "C" void kernel_launch(void* const* d_in, const int* in_sizes, int n_in,
                              void* d_out, int out_size, void* d_ws, size_t ws_size,
                              hipStream_t stream) {
    Args a;
    a.x = (const u16*)d_in[0]; a.kc = (const u16*)d_in[1]; a.vc = (const u16*)d_in[2]; a.wq = (const u16*)d_in[3]; a.wo = (const u16*)d_in[4];
    a.w1 = (const u16*)d_in[6]; a.b1 = (const u16*)d_in[7]; a.w2 = (const u16*)d_in[8]; a.b2 = (const u16*)d_in[9];
    a.out = (u16*)d_out; a.ws = (unsigned char*)d_ws + (WOFF && ws_size >= (size_t)WOFF + (4u << 20) ? (size_t)WOFF : 0);
    for (int i = 0; i < 256; ++i) { const unsigned e = ROLE_TAB[i]; a.role[i] = (e & 0x7fffu) << 7 | ((e >> 15) & 1u) | ((e >> 16) & 15u) << 2 | (e >> 20) << 22; }
    (void)hipFuncSetAttribute((const void*)fused_decode, hipFuncAttributeMaxDynamicSharedMemorySize, L_BYTES);
    hipLaunchKernelGGL(fused_decode, dim3(NWG), dim3(NTHR), L_BYTES, stream, a);
}
```
